```python
import torch

def _softmax(x, axis=-1):
    return torch.softmax(x, dim=axis)

def _gelu(x):
    return torch.nn.functional.gelu(x, approximate='tanh')

def _silu(x):
    return torch.nn.functional.silu(x)

def _sigmoid(x):
    return torch.sigmoid(x)

def _layernorm(x, axis=-1, eps=1e-05):
    m = x.mean(dim=axis, keepdim=True)
    v = x.var(dim=axis, keepdim=True, unbiased=False)
    return (x - m) / torch.sqrt(v + eps)

def _scatter_add(tgt, idx, src, axis=0):
    return tgt.clone().index_add_(axis, idx, src)

def _segment_sum(src, seg, n):
    out = torch.zeros((n,) + src.shape[1:], dtype=src.dtype)
    return out.index_add_(0, seg, src)

def _pad(x, pw):
    flat = tuple((v for pair in reversed(pw) for v in pair))
    return torch.nn.functional.pad(x, flat)
kernel_name = 'adaLN_cond__ssm_conv_s4__mixer_gmlp__prompt'

def setup_inputs():

    _w = lambda a: (lambda t: t.to(torch.bfloat16) if t.is_floating_point() else t)(torch.as_tensor(a))

    def _A_setup():
        B, L, D, N = (1, 2048, 512, 128)
        A_re = -(lambda _x: torch.exp(torch.as_tensor(_x)))((lambda *s: torch.randn(*s, dtype=torch.bfloat16) * 3.2)(D, N))
        A_im = torch.pi * (lambda *s: torch.randn(*s, dtype=torch.bfloat16) * 0.2)(D, N)
        B_re = (lambda *s: torch.randn(*s, dtype=torch.bfloat16) * 0.2)(D, N)
        B_im = (lambda *s: torch.randn(*s, dtype=torch.bfloat16) * 0.2)(D, N)
        C_re = (lambda *s: torch.randn(*s, dtype=torch.bfloat16) * 0.2)(D, N)
        C_im = (lambda *s: torch.randn(*s, dtype=torch.bfloat16) * 0.2)(D, N)
        log_dt = (lambda *s: torch.randn(*s, dtype=torch.bfloat16) * 0.2)(D) * 0.5 - 4.0
        Dskip = (lambda *s: torch.randn(*s, dtype=torch.bfloat16) * 0.2)(D)
        return {'u': _w((lambda *s: torch.randn(*s, dtype=torch.bfloat16) * 0.05)(B, L, D)), 'A_re': _w(A_re), 'A_im': _w(A_im), 'B_re': _w(B_re), 'B_im': _w(B_im), 'C_re': _w(C_re), 'C_im': _w(C_im), 'log_dt': _w(log_dt), 'Dskip': _w(Dskip)}
    _A_in = _A_setup()

    def _B_setup():
        B, S, D, F = (1, 2048, 512, 1792)
        Fh = F // 2
        return {'x': _w((lambda *s: torch.randn(*s, dtype=torch.bfloat16) * 0.05)(B, S, D)), 'w_in': _w((lambda *s: torch.randn(*s, dtype=torch.bfloat16) * 0.2)(D, 2 * Fh)), 'w_sp': _w(0.01 * (lambda *s: torch.randn(*s, dtype=torch.bfloat16) * 0.2)(S, S)), 'b_sp': _w(1.0 + 0.01 * (lambda *s: torch.randn(*s, dtype=torch.bfloat16) * 0.05)(S)), 'w_out': _w((lambda *s: torch.randn(*s, dtype=torch.bfloat16) * 0.05)(Fh, D))}
    _B_in = _B_setup()
    return {'A_u': _A_in['u'], 'A_A_re': _A_in['A_re'], 'A_A_im': _A_in['A_im'], 'A_B_re': _A_in['B_re'], 'A_B_im': _A_in['B_im'], 'A_C_re': _A_in['C_re'], 'A_C_im': _A_in['C_im'], 'A_log_dt': _A_in['log_dt'], 'A_Dskip': _A_in['Dskip'], 'B_x': _B_in['x'], 'B_w_in': _B_in['w_in'], 'B_w_sp': _B_in['w_sp'], 'B_b_sp': _B_in['b_sp'], 'B_w_out': _B_in['w_out'], 'c': _w((lambda *s: torch.randn(*s, dtype=torch.bfloat16) * 0.1)(1, 512) * 8.0), 'w_mod': _w((lambda *s: torch.randn(*s, dtype=torch.bfloat16) * 0.1)(512, 6 * 512) * 512 ** (-0.5) * 8.0)}

def reference(A_u, A_A_re, A_A_im, A_B_re, A_B_im, A_C_re, A_C_im, A_log_dt, A_Dskip, B_x, B_w_in, B_w_sp, B_b_sp, B_w_out, c, w_mod):

    def _A_ref(u, A_re, A_im, B_re, B_im, C_re, C_im, log_dt, Dskip):
        Bsz, L, D = u.shape
        dt = torch.exp(log_dt)[:, None]
        A = A_re + 1j * A_im
        Bc = B_re + 1j * B_im
        Cc = C_re + 1j * C_im
        logdA = dt * A
        dB = dt * Bc
        l = (lambda x, dt: x.to(dt))(torch.arange(L), A_re.dtype)
        P = torch.exp(logdA[:, :, None] * l[None, None, :])
        K = torch.einsum('dn,dnl,dn->dl', Cc, P, dB).real
        L2 = 2 * L
        Kp = _pad(K, ((0, 0), (0, L)))
        up = (lambda x, *d: x.permute(*d))(u, 0, 2, 1)
        up = _pad(up, ((0, 0), (0, 0), (0, L)))
        Kf = (lambda x, axes=(-2, -1): torch.fft.rfft2(x.float(), dim=axes))(Kp[:, None, :], axes=(-2, -1))
        Uf = (lambda x, axes=(-2, -1): torch.fft.rfft2(x.float(), dim=axes))(up[:, :, None, :], axes=(-2, -1))
        Yf = Uf * Kf[None, :, :, :]
        y = (lambda x, s, axes=(-2, -1): torch.fft.irfft2(x, s=s, dim=axes).to(torch.bfloat16))(Yf, (1, L2), axes=(-2, -1))[:, :, 0, :L]
        y = (lambda x, *d: x.permute(*d))(y, 0, 2, 1) + u * Dskip[None, None, :]
        return y

    def _B_ref(x, w_in, w_sp, b_sp, w_out):
        B, S, D = x.shape
        F = w_in.shape[-1]
        Fh = F // 2
        z = _gelu(torch.einsum('bsd,df->bsf', x, w_in))
        z1 = z[..., :Fh].clone()
        z2 = _layernorm(z[..., Fh:], axis=-1)
        g = torch.einsum('ts,bsf->btf', w_sp, z2) + b_sp[None, :, None]
        return torch.einsum('bsf,fd->bsd', z1 * g, w_out)
    x = A_u
    mod = (_silu(c) @ w_mod).reshape(c.shape[0], 6, 1, -1)
    sa, ca, ga, sb, cb, gb = (mod[:, 0].clone(), mod[:, 1].clone(), mod[:, 2].clone(), mod[:, 3].clone(), mod[:, 4].clone(), mod[:, 5].clone())
    h = _layernorm(x, axis=-1) * (1 + ca) + sa
    x = x + ga * _A_ref(h, A_A_re, A_A_im, A_B_re, A_B_im, A_C_re, A_C_im, A_log_dt, A_Dskip)
    h = _layernorm(x, axis=-1) * (1 + cb) + sb
    x = x + gb * _B_ref(h, B_w_in, B_w_sp, B_b_sp, B_w_out)
    return x
```

```cpp
#include <hip/hip_runtime.h>
#include <stdint.h>
#include <string.h>

#define GAS __attribute__((address_space(1)))
#define LAS __attribute__((address_space(3)))
#define DEVI __device__ __forceinline__
typedef unsigned short u16;
typedef unsigned long long u64;
typedef short bf16x8 __attribute__((ext_vector_type(8)));
typedef short s16x4 __attribute__((ext_vector_type(4)));
typedef float f32x2 __attribute__((ext_vector_type(2)));
typedef float f32x4 __attribute__((ext_vector_type(4)));
typedef float f32x16 __attribute__((ext_vector_type(16)));
typedef unsigned u32x2 __attribute__((ext_vector_type(2)));
typedef unsigned u32x4 __attribute__((ext_vector_type(4)));
typedef __bf16 bf16x2_t __attribute__((ext_vector_type(2)));
typedef GAS unsigned gu32;
typedef GAS u64 gu64;
#define RLX_AGENT __ATOMIC_RELAXED, __HIP_MEMORY_SCOPE_AGENT

constexpr int SEQ = 2048, DM = 512, NS = 128, FF = 1792, FH = 896;

constexpr size_t O_CTRL = 0;
constexpr size_t O_GRAN = 65536;
constexpr size_t GRAN_BYTES = 32 * 8 * 64 * 2 * 8;
constexpr size_t O_E12 = O_GRAN + GRAN_BYTES;
constexpr size_t E12_BYTES = 2 * FF * 8;
constexpr size_t ZERO_BYTES = O_E12 + E12_BYTES;
constexpr size_t O_MODP = 1 << 20;
constexpr size_t O_XT = 2 << 20;
constexpr size_t O_XNT = 4 << 20;
constexpr size_t O_KREV = 6 << 20;
constexpr int KREV_ROW = 4160;
constexpr size_t O_X1T = 9 << 20;
constexpr size_t O_WP = 11 << 20;
constexpr size_t O_WOF = 13 << 20;
constexpr size_t O_Z2F = 14 << 20;
constexpr size_t O_Z1F = 18 << 20;
constexpr size_t O_X1F = 22 << 20;
constexpr size_t O_VF = 24 << 20;

struct Params {
  const u16 *x, *a_re, *a_im, *b_re, *b_im, *c_re, *c_im, *log_dt, *dskip;
  const u16 *w_in, *w_sp, *b_sp, *w_out, *cvec, *w_mod;
  u16 *out;
  unsigned char *ws;
};

DEVI float bf_lo(unsigned u) { return __builtin_bit_cast(float, u << 16); }
DEVI float bf_hi(unsigned u) { return __builtin_bit_cast(float, u & 0xffff0000u); }
DEVI float bf2f(u16 h) { return __builtin_bit_cast(float, (unsigned)h << 16); }
DEVI unsigned pack_bf(float lo, float hi) {
  f32x2 v = {lo, hi};
  return __builtin_bit_cast(unsigned, __builtin_convertvector(v, bf16x2_t));
}
DEVI float rbf(float f) { return bf_lo(pack_bf(f, f)); }
template <int CTRL> DEVI float dpp_f(float x) {
  return __builtin_bit_cast(float, __builtin_amdgcn_update_dpp(0, __builtin_bit_cast(int, x), CTRL, 0xf, 0xf, true));
}
DEVI float row16_sum(float x) {
  x += dpp_f<0xB1>(x);
  x += dpp_f<0x4E>(x);
  x += dpp_f<0x141>(x);
  x += dpp_f<0x140>(x);
  return x;
}
DEVI float u2f(unsigned u) { return __builtin_bit_cast(float, u); }
DEVI unsigned f2u(float f) { return __builtin_bit_cast(unsigned, f); }
DEVI float xrow_sum(float x) {
  auto s = __builtin_amdgcn_permlane16_swap(f2u(x), f2u(x), false, false);
  x = u2f(s[0]) + u2f(s[1]);
  auto t = __builtin_amdgcn_permlane32_swap(f2u(x), f2u(x), false, false);
  return u2f(t[0]) + u2f(t[1]);
}
DEVI float wave_sum(float x) { return xrow_sum(row16_sum(x)); }
DEVI f32x4 mfma16(u32x4 a, u32x4 b, f32x4 c) {
  return __builtin_amdgcn_mfma_f32_16x16x32_bf16(__builtin_bit_cast(bf16x8, a), __builtin_bit_cast(bf16x8, b), c, 0, 0, 0);
}
DEVI f32x16 mfma32(u32x4 a, u32x4 b, f32x16 c) {
  return __builtin_amdgcn_mfma_f32_32x32x16_bf16(__builtin_bit_cast(bf16x8, a), __builtin_bit_cast(bf16x8, b), c, 0, 0, 0);
}
DEVI u32x2 tr_read(LAS unsigned char *p) {
  typedef short v4i16_t __attribute__((ext_vector_type(4)));
  return __builtin_bit_cast(u32x2, __builtin_amdgcn_ds_read_tr16_b64_v4i16((LAS v4i16_t *)p));
}
DEVI u32x4 cat2(u32x2 a, u32x2 b) { return u32x4{a.x, a.y, b.x, b.y}; }
DEVI float ln_rstd(float var) {
  float s = rbf(__builtin_sqrtf(rbf(rbf(var) + 1e-5f)));
  return 1.0f / s;
}
DEVI float gelu_tanh(float x) {
  float u = x * (0.7978845608028654f * 2.0f * 1.4426950408889634f) * __builtin_fmaf(x * x, 0.044715f, 1.0f);
  float e = __builtin_amdgcn_exp2f(-u);
  return x * __builtin_amdgcn_rcpf(1.0f + e);
}
DEVI float mod_val(const unsigned char *ws, int idx) {
  const GAS float *mp = (const GAS float *)(ws + O_MODP);
  float s = 0.f;
#pragma unroll
  for (int q = 0; q < 8; ++q) s += mp[q * 3072 + idx];
  return rbf(s);
}
DEVI void block_rt_j(int b, int &rt, int &j) {
  const int x = b & 7, q = b >> 3;
  rt = 8 * (x >> 1) + (q & 7);
  j = 4 * (x & 1) + (q >> 3);
}
DEVI int tile_row(int tau, int i) { return 32 * (tau >> 1) + 8 * (i >> 2) + 4 * (tau & 1) + (i & 3); }

constexpr int K1_ROWB = 528;
constexpr int K1_CH = 96 * K1_ROWB;
constexpr int K1_KD = 2 * K1_CH;
constexpr int K1_LN = K1_KD + 2 * 2048;
constexpr int K1_MOD = K1_LN + 16384;
constexpr int K1_WO = K1_MOD + 512;
constexpr int K1_LDS = K1_WO + 8192;

struct cf { float re, im; };
DEVI cf cmul(cf a, cf b) { return cf{a.re * b.re - a.im * b.im, a.re * b.im + a.im * b.re}; }
DEVI cf cexp_m(float lr2, float rev, float m) {
  const float er = __builtin_amdgcn_exp2f(lr2 * m);
  const float fr = __builtin_amdgcn_fractf(rev * m);
  return cf{er * __builtin_amdgcn_cosf(fr), er * __builtin_amdgcn_sinf(fr)};
}

__global__ __launch_bounds__(1024) void k1_prep(Params p) {
  extern __shared__ __attribute__((aligned(16))) unsigned char smem_raw[];
  LAS unsigned char *L = (LAS unsigned char *)smem_raw;
  const int tid = threadIdx.x, lane = tid & 63, b = blockIdx.x;
  const int w = __builtin_amdgcn_readfirstlane(tid >> 6);
  const int i16 = lane & 15, g = lane >> 4;

  {
    GAS u32x4 *z = (GAS u32x4 *)p.ws;
    for (int idx = b * 1024 + tid; idx < (int)(ZERO_BYTES / 16); idx += 256 * 1024) z[idx] = u32x4{0, 0, 0, 0};
  }

  float modacc[8];
  if (w < 8) {
    const int ch = w >> 2, d = 2 * b + ch, part = (w >> 1) & 1, n = (w & 1) * 64 + lane;
    const int pi = d * NS + n;
    const float are = bf2f(p.a_re[pi]), aim = bf2f(p.a_im[pi]), bre = bf2f(p.b_re[pi]), bim = bf2f(p.b_im[pi]);
    const float cre = bf2f(p.c_re[pi]), cim = bf2f(p.c_im[pi]);
    const float dt = rbf(expf(bf2f(p.log_dt[d])));
    const float lr2 = dt * are * 1.4426950408889634f, rev = dt * aim * 0.15915494309189535f;
    const cf beta = cmul(cf{cre, cim}, cf{dt * bre, dt * bim});
    LAS unsigned *T = (LAS unsigned *)(L + ch * K1_CH) + n;
    constexpr int RP = K1_ROWB / 4;
    if (part == 0) {
      const cf w1 = cexp_m(lr2, rev, 1.f), v = cexp_m(lr2, rev, 16.f);
      cf y = cf{1.f, 0.f};
#pragma unroll
      for (int q = 0; q < 16; ++q) { T[(64 + q) * RP] = pack_bf(y.re, -y.im); y = cmul(y, w1); }
      cf z = beta;
#pragma unroll
      for (int a = 0; a < 32; ++a) { T[a * RP] = pack_bf(z.re, z.im); z = cmul(z, v); }
    } else {
      const cf w4 = cexp_m(lr2, rev, 4.f), v4 = cexp_m(lr2, rev, 64.f);
      cf y = cf{1.f, 0.f};
#pragma unroll
      for (int q = 0; q < 16; ++q) { T[(80 + q) * RP] = pack_bf(y.re, -y.im); y = cmul(y, w4); }
      cf z = cmul(beta, cexp_m(lr2, rev, 512.f));
#pragma unroll
      for (int a = 0; a < 25; ++a) { T[(32 + a) * RP] = pack_bf(z.re, z.im); z = cmul(z, v4); }
    }
  } else if (w < 12) {
    const int wl = w - 8, t0 = 256 * (b & 7) + 8 * (b >> 3);
    u32x4 xr[2];
#pragma unroll
    for (int r = 0; r < 2; ++r) xr[r] = *(const GAS u32x4 *)(p.x + (size_t)(t0 + 2 * wl + r) * DM + 8 * lane);
    float xn[2][8], xv[2][8];
#pragma unroll
    for (int r = 0; r < 2; ++r) {
      float s = 0.f;
#pragma unroll
      for (int e = 0; e < 4; ++e) { xv[r][2 * e] = bf_lo(xr[r][e]); xv[r][2 * e + 1] = bf_hi(xr[r][e]); s += xv[r][2 * e] + xv[r][2 * e + 1]; }
      const float mean = wave_sum(s) * (1.f / DM);
      float q = 0.f;
#pragma unroll
      for (int e = 0; e < 8; ++e) { const float c = xv[r][e] - mean; q += c * c; }
      const float rs = ln_rstd(wave_sum(q) * (1.f / DM)), mb = rbf(mean);
#pragma unroll
      for (int e = 0; e < 8; ++e) xn[r][e] = rbf(xv[r][e] - mb) * rs;
    }
    u32x4 a0, a1, n0, n1;
#pragma unroll
    for (int e = 0; e < 4; ++e) {
      a0[e] = pack_bf(xv[0][e], xv[1][e]); a1[e] = pack_bf(xv[0][4 + e], xv[1][4 + e]);
      n0[e] = pack_bf(xn[0][e], xn[1][e]); n1[e] = pack_bf(xn[0][4 + e], xn[1][4 + e]);
    }
    LAS u32x4 *B0 = (LAS u32x4 *)(L + K1_LN + wl * 2048) + 2 * lane;
    B0[0] = a0; B0[1] = a1;
    LAS u32x4 *B1 = (LAS u32x4 *)(L + K1_LN + 8192 + wl * 2048) + 2 * lane;
    B1[0] = n0; B1[1] = n1;
  } else if (w < 14) {
    if (b < 192) {
      const int cg = b % 24, ks = b / 24, wv = w - 12;
      const int r0 = 64 * ks + 32 * wv + g;
      u32x4 wv8[8];
      float sc[8];
#pragma unroll
      for (int q = 0; q < 8; ++q) {
        wv8[q] = *(const GAS u32x4 *)(p.w_mod + (size_t)(r0 + 4 * q) * 3072 + 128 * cg + 8 * i16);
        sc[q] = bf2f(p.cvec[r0 + 4 * q]);
      }
#pragma unroll
      for (int e = 0; e < 8; ++e) modacc[e] = 0.f;
#pragma unroll
      for (int q = 0; q < 8; ++q) {
        const float s = rbf(sc[q] / (1.f + __expf(-sc[q])));
#pragma unroll
        for (int e = 0; e < 4; ++e) { modacc[2 * e] += s * bf_lo(wv8[q][e]); modacc[2 * e + 1] += s * bf_hi(wv8[q][e]); }
      }
#pragma unroll
      for (int e = 0; e < 8; ++e) modacc[e] = xrow_sum(modacc[e]);
      if (wv == 1 && g == 0) {
        LAS f32x4 *M = (LAS f32x4 *)(L + K1_MOD) + 2 * i16;
        M[0] = f32x4{modacc[0], modacc[1], modacc[2], modacc[3]};
        M[1] = f32x4{modacc[4], modacc[5], modacc[6], modacc[7]};
      }
    }
  } else {
    if (b < 112) {
      const int pi_ = b >> 2, cq = b & 3, t2 = tid - 14 * 64;
#pragma unroll
      for (int q = 0; q < 4; ++q) {
        const int idx = t2 + 128 * q, r = idx >> 4, c = idx & 15;
        *(LAS u32x4 *)(L + K1_WO + r * 256 + c * 16) = *(const GAS u32x4 *)(p.w_out + (size_t)(32 * pi_ + r) * DM + 128 * cq + 8 * c);
      }
    }
  }
  __syncthreads();

  if (w < 8) {
    const int ch = w >> 2, tile = w & 3;
    LAS unsigned char *T = L + ch * K1_CH;
    const int yrow = tile < 2 ? 64 : 80;
    f32x4 acc = {0.f, 0.f, 0.f, 0.f};
#pragma unroll
    for (int ks = 0; ks < 8; ++ks) {
      const u32x4 a = *(LAS u32x4 *)(T + (16 * tile + i16) * K1_ROWB + (16 * ks + 4 * g) * 4);
      const u32x4 bb = *(LAS u32x4 *)(T + (yrow + i16) * K1_ROWB + (16 * ks + 4 * g) * 4);
      acc = mfma16(a, bb, acc);
    }
    LAS u16 *KD = (LAS u16 *)(L + K1_KD + ch * 2048);
#pragma unroll
    for (int r = 0; r < 4; ++r) KD[(16 * tile + 4 * g + r) * 16 + i16] = (u16)(pack_bf(acc[r], acc[r]) & 0xffffu);
  } else if (w < 12) {
    const int tl = tid - 512, t0 = 256 * (b & 7) + 8 * (b >> 3);
#pragma unroll
    for (int arr = 0; arr < 2; ++arr)
#pragma unroll
      for (int h = 0; h < 2; ++h) {
        const int d = tl + 256 * h;
        LAS unsigned *S = (LAS unsigned *)(L + K1_LN + arr * 8192) + d;
        const u32x4 v = {S[0], S[512], S[1024], S[1536]};
        *(GAS u32x4 *)(p.ws + (arr ? O_XNT : O_XT) + ((size_t)d * SEQ + t0) * 2) = v;
      }
  } else if (w < 14) {
    if (b < 192 && w == 12 && g == 0) {
      const int cg = b % 24, ks = b / 24;
      LAS f32x4 *M = (LAS f32x4 *)(L + K1_MOD) + 2 * i16;
      const f32x4 m0 = M[0], m1 = M[1];
      GAS f32x4 *O = (GAS f32x4 *)(p.ws + O_MODP + ((size_t)ks * 3072 + 128 * cg + 8 * i16) * 4);
      O[0] = f32x4{modacc[0], modacc[1], modacc[2], modacc[3]} + m0;
      O[1] = f32x4{modacc[4], modacc[5], modacc[6], modacc[7]} + m1;
    }
  } else {
    if (b < 112) {
      const int pi_ = b >> 2, cq = b & 3, q = i16 >> 2, pp = lane & 3;
#pragma unroll
      for (int k = 0; k < 4; ++k) {
        const int dl = 4 * (w - 14) + k;
        LAS unsigned char *A = L + K1_WO + (4 * g + q) * 256 + (32 * (dl >> 1) + 8 * pp + 4 * (dl & 1)) * 2;
        const u32x4 f = cat2(tr_read(A), tr_read(A + 16 * 256));
        *(GAS u32x4 *)(p.ws + O_WOF + ((size_t)((8 * cq + dl) * 28 + pi_) * 64 + lane) * 16) = f;
      }
    }
  }
  __syncthreads();

  if (w < 8) {
    const int ch = w >> 2, d = 2 * b + ch, c = tid & 255;
    LAS u16 *KD = (LAS u16 *)(L + K1_KD + ch * 2048);
    unsigned v[8];
#pragma unroll
    for (int e = 0; e < 8; ++e) {
      const int l = 2047 - (8 * c + e);
      const int lr = (int)rbf((float)l);
      v[e] = KD[lr < 512 ? lr : 512 + ((lr - 512) >> 2)];
    }
    GAS u32x4 *O = (GAS u32x4 *)(p.ws + O_KREV + (size_t)d * KREV_ROW);
    O[c] = u32x4{v[0] | (v[1] << 16), v[2] | (v[3] << 16), v[4] | (v[5] << 16), v[6] | (v[7] << 16)};
    if (c < 4) O[256 + c] = u32x4{0, 0, 0, 0};
  }
}

constexpr int K2_RC = 4160;
constexpr int K2_U = 4 * K2_RC;
constexpr int K2_CH = K2_U + 95 * 80 + 16;
constexpr int K2_RED = 2 * K2_CH;
constexpr int K2_WT = K2_RED + 16384;
constexpr int K2_CS = K2_WT + 2 * 32 * 240;
constexpr int K2_LDS = K2_CS + 256;

__global__ __launch_bounds__(256) void k2_conv(Params p) {
  extern __shared__ __attribute__((aligned(16))) unsigned char smem_raw[];
  LAS unsigned char *L = (LAS unsigned char *)smem_raw;
  const int tid = threadIdx.x, lane = tid & 63, b = blockIdx.x;
  const int w = __builtin_amdgcn_readfirstlane(tid >> 6);
  const int ch = w >> 1, pp = w & 1, d0 = 2 * b;
  const int i32 = lane & 31, h = lane >> 5;

  u32x4 xn[2], ka[2]; u32x2 kb[2];
#pragma unroll
  for (int c = 0; c < 2; ++c) {
    xn[c] = *(const GAS u32x4 *)(p.ws + O_XNT + ((size_t)(d0 + c) * SEQ + 8 * tid) * 2);
    const GAS unsigned char *kp = (const GAS unsigned char *)(p.ws + O_KREV + (size_t)(d0 + c) * KREV_ROW + 16 * tid);
    ka[c] = *(const GAS u32x4 *)kp;
    kb[c] = *(const GAS u32x2 *)(kp + 16);
  }
  u32x2 xe[4];
#pragma unroll
  for (int rg = 0; rg < 4; ++rg)
    xe[rg] = *(const GAS u32x2 *)(p.ws + O_XT + ((size_t)(d0 + ch) * SEQ + 1024 * pp + 32 * i32 + 8 * rg + 4 * h) * 2);
  const int wks = b & 15, wfg = b >> 4;
  u32x4 wr[2];
#pragma unroll
  for (int q = 0; q < 2; ++q) {
    const int idx = tid + 256 * q, r = idx / 14, cc = idx % 14;
    wr[q] = idx < 448 ? *(const GAS u32x4 *)(p.w_in + (size_t)(32 * wks + r) * FF + 112 * wfg + 8 * cc) : u32x4{0, 0, 0, 0};
  }
  float sa[2], c1[2], ga[2];
  {
    const int which = lane >> 3, part = lane & 7;
    float v = 0.f;
    if (lane < 48) v = ((const GAS float *)(p.ws + O_MODP))[part * 3072 + (which >> 1) * 512 + d0 + (which & 1)];
    v += dpp_f<0xB1>(v); v += dpp_f<0x4E>(v); v += dpp_f<0x141>(v);
    v = rbf(v);
#pragma unroll
    for (int c = 0; c < 2; ++c) {
      sa[c] = __builtin_bit_cast(float, __builtin_amdgcn_readlane(__builtin_bit_cast(int, v), 8 * c));
      c1[c] = rbf(1.f + __builtin_bit_cast(float, __builtin_amdgcn_readlane(__builtin_bit_cast(int, v), 16 + 8 * c)));
      ga[c] = __builtin_bit_cast(float, __builtin_amdgcn_readlane(__builtin_bit_cast(int, v), 32 + 8 * c));
    }
  }
  if (tid < 64) {
    const int which = tid >> 5, r = tid & 31;
    const float v = mod_val(p.ws, (which ? 3 : 4) * 512 + 32 * wks + r);
    ((LAS float *)(L + K2_CS))[tid] = which ? v : rbf(1.f + v);
  }
#pragma unroll
  for (int c = 0; c < 2; ++c) {
    LAS unsigned char *C = L + c * K2_CH;
    u32x4 hv;
#pragma unroll
    for (int e = 0; e < 4; ++e) {
      const unsigned m = pack_bf(bf_lo(xn[c][e]) * c1[c], bf_hi(xn[c][e]) * c1[c]);
      hv[e] = pack_bf(bf_lo(m) + sa[c], bf_hi(m) + sa[c]);
    }
    *(LAS u32x4 *)(C + K2_U + (31 + (tid >> 2)) * 80 + 16 * (tid & 3)) = hv;
    if (tid < 155) *(LAS u32x4 *)(C + K2_U + 16 * tid) = u32x4{0, 0, 0, 0};
    const unsigned D0 = ka[c][0], D1 = ka[c][1], D2 = ka[c][2], D3 = ka[c][3], D4 = kb[c][0], D5 = kb[c][1];
    const unsigned A0 = __builtin_amdgcn_alignbit(D1, D0, 16), A1 = __builtin_amdgcn_alignbit(D2, D1, 16),
                   A2 = __builtin_amdgcn_alignbit(D3, D2, 16), A3 = __builtin_amdgcn_alignbit(D4, D3, 16),
                   A4 = __builtin_amdgcn_alignbit(D5, D4, 16);
    *(LAS u32x4 *)(C + 0 * K2_RC + 16 * tid) = u32x4{D0, D1, D2, D3};
    *(LAS u32x4 *)(C + 1 * K2_RC + 16 * tid) = u32x4{A0, A1, A2, A3};
    *(LAS u32x4 *)(C + 2 * K2_RC + 16 * tid) = u32x4{D1, D2, D3, D4};
    *(LAS u32x4 *)(C + 3 * K2_RC + 16 * tid) = u32x4{A1, A2, A3, A4};
    if (tid < 16) *(LAS u32x4 *)(C + (tid >> 2) * K2_RC + 4096 + 16 * (tid & 3)) = u32x4{0, 0, 0, 0};
  }
  __syncthreads();

  {
    LAS float *CS = (LAS float *)(L + K2_CS);
#pragma unroll
    for (int q = 0; q < 2; ++q) {
      const int idx = tid + 256 * q, r = idx / 14, cc = idx % 14;
      if (idx < 448) {
        const float s = CS[r];
        u32x4 o;
#pragma unroll
        for (int e = 0; e < 4; ++e) o[e] = pack_bf(bf_lo(wr[q][e]) * s, bf_hi(wr[q][e]) * s);
        *(LAS u32x4 *)(L + K2_WT + r * 240 + cc * 16) = wr[q];
        *(LAS u32x4 *)(L + K2_WT + 32 * 240 + r * 240 + cc * 16) = o;
      }
    }
  }

  f32x16 acc0, acc1;
#pragma unroll
  for (int e = 0; e < 16; ++e) { acc0[e] = 0.f; acc1[e] = 0.f; }
  {
    LAS unsigned char *C = L + ch * K2_CH;
    LAS unsigned char *Ab = C + (3 - (i32 & 3)) * K2_RC + (2044 - 4 * (i32 >> 2) + 8 * h) * 2;
    LAS unsigned char *Bb = C + K2_U + 80 * i32 + 16 * h;
#pragma unroll 4
    for (int kk = 0; kk < 16; ++kk) {
      const int k = 2 * kk + pp;
#pragma unroll
      for (int s = 0; s < 2; ++s) {
        const int q = 2 * k - s;
        LAS unsigned char *a0p = Ab - 32 * q;
        const u32x4 A0 = cat2(*(LAS u32x2 *)a0p, *(LAS u32x2 *)(a0p + 8));
        const u32x4 A1 = cat2(*(LAS u32x2 *)(a0p - 2048), *(LAS u32x2 *)(a0p - 2048 + 8));
        const u32x4 B0 = *(LAS u32x4 *)(Bb + (31 - k) * 80 + 32 * s);
        const u32x4 B1 = *(LAS u32x4 *)(Bb + (63 - k) * 80 + 32 * s);
        acc0 = mfma32(A0, B0, acc0);
        acc1 = mfma32(A0, B1, acc1);
        acc1 = mfma32(A1, B0, acc1);
      }
    }
  }
  {
    LAS f32x4 *R = (LAS f32x4 *)(L + K2_RED + (ch * 2 + (1 - pp)) * 4096) + lane;
#pragma unroll
    for (int rg = 0; rg < 4; ++rg) {
      const f32x16 &o = pp ? acc0 : acc1;
      R[rg * 64] = f32x4{o[4 * rg], o[4 * rg + 1], o[4 * rg + 2], o[4 * rg + 3]};
    }
  }
  __syncthreads();
  {
    const int d = d0 + ch;
    const float dsk = bf2f(p.dskip[d]);
    LAS f32x4 *R = (LAS f32x4 *)(L + K2_RED + (ch * 2 + pp) * 4096) + lane;
    LAS unsigned char *U = L + ch * K2_CH + K2_U + (31 + 32 * pp + i32) * 80 + 8 * h;
#pragma unroll
    for (int rg = 0; rg < 4; ++rg) {
      const f32x4 o = R[rg * 64];
      const u32x2 hv = *(LAS u32x2 *)(U + 16 * rg);
      float hh[4] = {bf_lo(hv.x), bf_hi(hv.x), bf_lo(hv.y), bf_hi(hv.y)};
      float xx[4] = {bf_lo(xe[rg].x), bf_hi(xe[rg].x), bf_lo(xe[rg].y), bf_hi(xe[rg].y)};
      float r[4];
#pragma unroll
      for (int e = 0; e < 4; ++e) {
        const float y = rbf((pp ? acc1[4 * rg + e] : acc0[4 * rg + e]) + o[e]);
        const float s = rbf(y + rbf(hh[e] * dsk));
        r[e] = xx[e] + rbf(ga[ch] * s);
      }
      const int t = (1024 * pp + 32 * i32 + 8 * rg + 4 * h) ^ (4 * ((d >> 3) & 1));
      *(GAS u32x2 *)(p.ws + O_X1T + ((size_t)d * SEQ + t) * 2) = u32x2{pack_bf(r[0], r[1]), pack_bf(r[2], r[3])};
    }
  }
  {
    const int i16 = lane & 15, g = lane >> 4, q = i16 >> 2, pq = lane & 3;
    u32x4 ones = u32x4{0, 0, 0, 0}, sbv = u32x4{0, 0, 0, 0};
    if (i16 == 0) {
      LAS float *CS = (LAS float *)(L + K2_CS) + 32 + 8 * g;
      ones = u32x4{0x3f803f80u, 0x3f803f80u, 0x3f803f80u, 0x3f803f80u};
      sbv = u32x4{pack_bf(CS[0], CS[1]), pack_bf(CS[2], CS[3]), pack_bf(CS[4], CS[5]), pack_bf(CS[6], CS[7])};
    }
#pragma unroll
    for (int k = 0; k < 2; ++k) {
      const int ph = w + 4 * k;
      if (ph < 7) {
        LAS unsigned char *A = L + K2_WT + (8 * g + q) * 240 + (16 * ph + 4 * pq) * 2;
        const u32x4 raw = cat2(tr_read(A), tr_read(A + 4 * 240));
        const u32x4 scl = cat2(tr_read(A + 32 * 240), tr_read(A + 36 * 240));
        const int F = 7 * wfg + ph;
        *(GAS u32x4 *)(p.ws + O_WP + ((size_t)(F * 16 + wks) * 64 + lane) * 16) = scl;
        const f32x4 z4 = {0.f, 0.f, 0.f, 0.f};
        const f32x4 e1 = mfma16(ones, scl, z4), e2 = mfma16(sbv, raw, z4);
        if (g == 0) {
          GAS u64 *E = (GAS u64 *)(p.ws + O_E12) + 16 * F + i16;
          __hip_atomic_fetch_add(E, (u64)(long long)__builtin_rint((double)e1[0] * 4294967296.0), RLX_AGENT);
          __hip_atomic_fetch_add(E + FF, (u64)(long long)__builtin_rint((double)e2[0] * 4294967296.0), RLX_AGENT);
        }
      }
    }
  }
}

constexpr int K3_X = 0;
constexpr int K3_ZT = 65536;
constexpr int K3_ST = K3_ZT + 16384;
constexpr int K3_E = K3_ST + 512;
constexpr int K3_RED = K3_E + 2 * 224 * 4;
constexpr int K3_ZS = K3_RED + 2048;
constexpr int K3_LDS = K3_ZS + 512;
constexpr int G1_WIN = 5;

template <int VAR>
DEVI void gemm1_loop(const unsigned char *ws, LAS unsigned char *L, int lane, const int (&Fs)[4], f32x4 (&acc)[4][4],
                     f32x4 (&ssum)[2], f32x4 (&ssq)[2]) {
  const int g = lane >> 4, q = (lane & 15) >> 2, pq = lane & 3;
  LAS unsigned char *xb[4];
#pragma unroll
  for (int tau = 0; tau < 4; ++tau)
    xb[tau] = L + K3_X + (8 * g + q) * 128 + (((4 * (tau >> 1) + pq) ^ (4 * (q >> 1))) * 16) + (((tau & 1) ^ (g & 1)) * 8);
  const GAS u32x4 *wp[4];
#pragma unroll
  for (int s = 0; s < 4; ++s) wp[s] = (const GAS u32x4 *)(ws + O_WP) + (size_t)Fs[s] * 16 * 64 + lane;
  constexpr bool act[4] = {true, VAR != 3, true, VAR != 2};
  u32x4 wf[G1_WIN][4];
#pragma unroll
  for (int ks = 0; ks < G1_WIN; ++ks)
#pragma unroll
    for (int s = 0; s < 4; ++s)
      if (act[s]) wf[ks][s] = wp[s][ks * 64];
  const u32x4 ones = u32x4{0x3f803f80u, 0x3f803f80u, 0x3f803f80u, 0x3f803f80u};
  __syncthreads();
#pragma unroll
  for (int ks = 0; ks < 16; ++ks) {
    u32x4 xf[4];
#pragma unroll
    for (int tau = 0; tau < 4; ++tau) xf[tau] = cat2(tr_read(xb[tau] + ks * 4096), tr_read(xb[tau] + ks * 4096 + 512));
    u32x4 cur[4];
#pragma unroll
    for (int s = 0; s < 4; ++s)
      if (act[s]) {
        cur[s] = wf[ks % G1_WIN][s];
        if (ks + G1_WIN < 16) wf[ks % G1_WIN][s] = wp[s][(ks + G1_WIN) * 64];
      }
#pragma unroll
    for (int tau = 0; tau < 4; ++tau) {
      acc[0][tau] = mfma16(xf[tau], cur[0], acc[0][tau]);
      if (act[1]) acc[1][tau] = mfma16(xf[tau], cur[1], acc[1][tau]);
      acc[2][tau] = mfma16(cur[2], xf[tau], acc[2][tau]);
      if (act[3]) acc[3][tau] = mfma16(cur[3], xf[tau], acc[3][tau]);
    }
    if (VAR != 0) {
#pragma unroll
      for (int k = 0; k < 2; ++k) {
        const int tau = (VAR == 2 ? 0 : 2) + k;
        ssum[k] = mfma16(xf[tau], ones, ssum[k]);
        ssq[k] = mfma16(xf[tau], xf[tau], ssq[k]);
      }
    }
  }
}

DEVI float pick4(f32x4 v, int r) { return r == 0 ? v[0] : r == 1 ? v[1] : r == 2 ? v[2] : v[3]; }

__global__ __launch_bounds__(256) void k3_gemm1(Params p) {
  extern __shared__ __attribute__((aligned(16))) unsigned char smem_raw[];
  LAS unsigned char *L = (LAS unsigned char *)smem_raw;
  const int tid = threadIdx.x, lane = tid & 63, b = blockIdx.x;
  const int w = __builtin_amdgcn_readfirstlane(tid >> 6);
  const int i16 = lane & 15, g = lane >> 4;
  int rt, j;
  block_rt_j(b, rt, j);

  {
    u32x4 xs[16];
    const int r0 = tid >> 3, cc = tid & 7;
#pragma unroll
    for (int i = 0; i < 16; ++i)
      xs[i] = *(const GAS u32x4 *)(p.ws + O_X1T + ((size_t)(r0 + 32 * i) * SEQ + 64 * rt + 8 * cc) * 2);
    if (tid < 224) {
      const int f = tid < 112 ? 112 * j + tid : FH + 112 * j + (tid - 112);
      const GAS long long *E = (const GAS long long *)(p.ws + O_E12);
      ((LAS float *)(L + K3_E))[tid] = (float)((double)E[f] * (1.0 / 4294967296.0));
      ((LAS float *)(L + K3_E))[224 + tid] = (float)((double)E[FF + f] * (1.0 / 4294967296.0));
    }
    if (tid < 128) *(LAS u32x4 *)(L + K3_ZT + 112 * 128 + 16 * tid) = u32x4{0, 0, 0, 0};
#pragma unroll
    for (int i = 0; i < 16; ++i)
      *(LAS u32x4 *)(L + K3_X + (r0 + 32 * i) * 128 + ((cc ^ (4 * ((r0 >> 1) & 1))) * 16)) = xs[i];
  }
  int ph2[2] = {2 * w, 2 * w + 1};
  int ph1[2] = {w < 3 ? 2 * w : 5, w < 3 ? 2 * w + 1 : 6};
  const int Fs[4] = {56 + 7 * j + ph2[0], 56 + 7 * j + (ph2[1] < 7 ? ph2[1] : 0), 7 * j + ph1[0], 7 * j + (w == 2 ? 0 : ph1[1])};
  f32x4 acc[4][4], ssum[2], ssq[2];
#pragma unroll
  for (int s = 0; s < 4; ++s)
#pragma unroll
    for (int t = 0; t < 4; ++t) acc[s][t] = f32x4{0.f, 0.f, 0.f, 0.f};
  ssum[0] = ssum[1] = ssq[0] = ssq[1] = f32x4{0.f, 0.f, 0.f, 0.f};
  if (w < 2) gemm1_loop<0>(p.ws, L, lane, Fs, acc, ssum, ssq);
  else if (w == 2) gemm1_loop<2>(p.ws, L, lane, Fs, acc, ssum, ssq);
  else gemm1_loop<3>(p.ws, L, lane, Fs, acc, ssum, ssq);

  LAS float *SM = (LAS float *)(L + K3_ST), *SR = SM + 64;
  if (w >= 2 && g == (i16 >> 2)) {
#pragma unroll
    for (int k = 0; k < 2; ++k) {
      const int tau = (w == 2 ? 0 : 2) + k;
      const float mean = pick4(ssum[k], i16 & 3) * (1.f / DM);
      const float var = pick4(ssq[k], i16 & 3) * (1.f / DM) - mean * mean;
      const int s = tile_row(tau, i16);
      SM[s] = rbf(mean);
      SR[s] = ln_rstd(fmaxf(var, 0.f));
    }
  }
  {
    const int q = i16 >> 2, pq = lane & 3;
    LAS unsigned char *xb = L + K3_X + (8 * g + q) * 128 + (((4 * (w >> 1) + pq) ^ (4 * (q >> 1))) * 16) + (((w & 1) ^ (g & 1)) * 8);
#pragma unroll
    for (int dl = 0; dl < 2; ++dl) {
      const int ks = 2 * j + dl;
      const u32x4 f = cat2(tr_read(xb + ks * 4096), tr_read(xb + ks * 4096 + 512));
      *(GAS u32x4 *)(p.ws + O_X1F + ((size_t)((b * 4 + w) * 2 + dl) * 64 + lane) * 16) = f;
    }
  }
  __syncthreads();

  LAS float *E1 = (LAS float *)(L + K3_E), *E2 = E1 + 224;
  u32x4 zf[2][2];
  const int nz2 = w == 3 ? 1 : 2;
#pragma unroll
  for (int s = 0; s < 2; ++s) {
    if (s < nz2) {
      const float e1 = E1[112 + 16 * ph2[s] + i16], e2 = E2[112 + 16 * ph2[s] + i16];
#pragma unroll
      for (int kp = 0; kp < 2; ++kp) {
#pragma unroll
        for (int hf = 0; hf < 2; ++hf) {
          const f32x4 m4 = *(LAS f32x4 *)(SM + 32 * kp + 8 * g + 4 * hf), r4 = *(LAS f32x4 *)(SR + 32 * kp + 8 * g + 4 * hf);
          float z[4];
#pragma unroll
          for (int r = 0; r < 4; ++r) z[r] = gelu_tanh(rbf((acc[s][2 * kp + hf][r] - m4[r] * e1) * r4[r] + e2));
          zf[s][kp][2 * hf] = pack_bf(z[0], z[1]);
          zf[s][kp][2 * hf + 1] = pack_bf(z[2], z[3]);
        }
        *(LAS u32x4 *)(L + K3_ZT + (16 * ph2[s] + i16) * 128 + (32 * kp + 8 * g) * 2) = zf[s][kp];
      }
    }
  }
  __syncthreads();
  gu64 *GR = (gu64 *)(p.ws + O_GRAN) + (size_t)rt * 8 * 128;
  {
    const int q = i16 >> 2, pq = lane & 3;
    const u32x4 ones = u32x4{0x3f803f80u, 0x3f803f80u, 0x3f803f80u, 0x3f803f80u};
    f32x4 su = {0.f, 0.f, 0.f, 0.f}, sq = {0.f, 0.f, 0.f, 0.f};
#pragma unroll
    for (int st = 0; st < 4; ++st) {
      LAS unsigned char *A = L + K3_ZT + (32 * st + 8 * g + q) * 128 + (16 * w + 4 * pq) * 2;
      const u32x4 a = cat2(tr_read(A), tr_read(A + 512));
      su = mfma16(a, ones, su);
      sq = mfma16(a, a, sq);
    }
    if (g == q) {
      gu64 *G = GR + (j * 64 + 16 * w + i16) * 2;
      __hip_atomic_store(G, (1ull << 32) | __builtin_bit_cast(unsigned, pick4(su, i16 & 3)), RLX_AGENT);
      __hip_atomic_store(G + 1, (1ull << 32) | __builtin_bit_cast(unsigned, pick4(sq, i16 & 3)), RLX_AGENT);
    }
  }
  {
    const int nz1 = w == 2 ? 1 : 2;
#pragma unroll
    for (int s = 0; s < 2; ++s) {
      if (s < nz1) {
        const f32x4 e1 = *(LAS f32x4 *)(E1 + 16 * ph1[s] + 4 * g), e2 = *(LAS f32x4 *)(E2 + 16 * ph1[s] + 4 * g);
#pragma unroll
        for (int tau = 0; tau < 4; ++tau) {
          const int sr = tile_row(tau, i16);
          const float m = SM[sr], r = SR[sr];
          float z[4];
#pragma unroll
          for (int e = 0; e < 4; ++e) z[e] = gelu_tanh(rbf((acc[2 + s][tau][e] - m * e1[e]) * r + e2[e]));
          *(GAS u32x2 *)(p.ws + O_Z1F + ((size_t)((b * 7 + ph1[s]) * 4 + tau) * 64 + lane) * 8) =
              u32x2{pack_bf(z[0], z[1]), pack_bf(z[2], z[3])};
        }
      }
    }
  }
  {
    const int row = tid & 63, jq = tid >> 6;
    float su = 0.f, sq = 0.f;
    for (unsigned spins = 0;; ++spins) {
      const u64 a0 = __hip_atomic_load(GR + (jq * 64 + row) * 2, RLX_AGENT), a1 = __hip_atomic_load(GR + (jq * 64 + row) * 2 + 1, RLX_AGENT);
      const u64 b0 = __hip_atomic_load(GR + ((jq + 4) * 64 + row) * 2, RLX_AGENT), b1 = __hip_atomic_load(GR + ((jq + 4) * 64 + row) * 2 + 1, RLX_AGENT);
      const bool ok = (a0 >> 32) == 1 && (a1 >> 32) == 1 && (b0 >> 32) == 1 && (b1 >> 32) == 1;
      su = __builtin_bit_cast(float, (unsigned)a0) + __builtin_bit_cast(float, (unsigned)b0);
      sq = __builtin_bit_cast(float, (unsigned)a1) + __builtin_bit_cast(float, (unsigned)b1);
      if (__all(ok) || spins > (1u << 16)) break;
      __builtin_amdgcn_s_sleep(1);
    }
    LAS float *RD = (LAS float *)(L + K3_RED);
    RD[(jq * 64 + row) * 2] = su;
    RD[(jq * 64 + row) * 2 + 1] = sq;
  }
  __syncthreads();
  LAS float *ZM = (LAS float *)(L + K3_ZS), *ZR = ZM + 64;
  if (tid < 64) {
    LAS float *RD = (LAS float *)(L + K3_RED);
    const float su = (RD[tid * 2] + RD[(64 + tid) * 2]) + (RD[(128 + tid) * 2] + RD[(192 + tid) * 2]);
    const float sq = (RD[tid * 2 + 1] + RD[(64 + tid) * 2 + 1]) + (RD[(128 + tid) * 2 + 1] + RD[(192 + tid) * 2 + 1]);
    const float mean = su * (1.f / FH), var = sq * (1.f / FH) - mean * mean;
    ZM[tid] = rbf(mean);
    ZR[tid] = ln_rstd(fmaxf(var, 0.f));
  }
  __syncthreads();
#pragma unroll
  for (int s = 0; s < 2; ++s) {
    if (s < nz2) {
#pragma unroll
      for (int kp = 0; kp < 2; ++kp) {
        u32x4 o;
#pragma unroll
        for (int hf = 0; hf < 2; ++hf) {
          const f32x4 m4 = *(LAS f32x4 *)(ZM + 32 * kp + 8 * g + 4 * hf), r4 = *(LAS f32x4 *)(ZR + 32 * kp + 8 * g + 4 * hf);
          const unsigned u0 = zf[s][kp][2 * hf], u1 = zf[s][kp][2 * hf + 1];
          o[2 * hf] = pack_bf(rbf(bf_lo(u0) - m4[0]) * r4[0], rbf(bf_hi(u0) - m4[1]) * r4[1]);
          o[2 * hf + 1] = pack_bf(rbf(bf_lo(u1) - m4[2]) * r4[2], rbf(bf_hi(u1) - m4[3]) * r4[3]);
        }
        *(GAS u32x4 *)(p.ws + O_Z2F + ((size_t)((7 * j + ph2[s]) * 64 + 2 * rt + kp) * 64 + lane) * 16) = o;
      }
    }
  }
}

constexpr int K4_LDS = 4 * 3 * 7 * 1024;
constexpr int G2_WIN = 3;

__global__ __launch_bounds__(256) void k4_gemm2(Params p) {
  extern __shared__ __attribute__((aligned(16))) unsigned char smem_raw[];
  LAS unsigned char *L = (LAS unsigned char *)smem_raw;
  const int tid = threadIdx.x, lane = tid & 63, b = blockIdx.x;
  const int w = __builtin_amdgcn_readfirstlane(tid >> 6);
  const int i16 = lane & 15, g = lane >> 4;
  int rt, j;
  block_rt_j(b, rt, j);
  const GAS u32x4 *bp[4];
#pragma unroll
  for (int tau = 0; tau < 4; ++tau)
    bp[tau] = (const GAS u32x4 *)(p.w_sp + (size_t)(64 * rt + tile_row(tau, i16)) * SEQ + 512 * w + 8 * g);
  const GAS u32x4 *ap = (const GAS u32x4 *)(p.ws + O_Z2F) + ((size_t)(7 * j) * 64 + 16 * w) * 64 + lane;
  u32x2 z1[7];
#pragma unroll
  for (int ph = 0; ph < 7; ++ph) z1[ph] = *(const GAS u32x2 *)(p.ws + O_Z1F + ((size_t)((b * 7 + ph) * 4 + w) * 64 + lane) * 8);
  const float bsp = bf2f(p.b_sp[64 * rt + tile_row(w, i16)]);
  u32x4 af[G2_WIN][7], bf[G2_WIN][4];
#pragma unroll
  for (int k = 0; k < G2_WIN; ++k) {
#pragma unroll
    for (int tau = 0; tau < 4; ++tau) bf[k][tau] = bp[tau][4 * k];
#pragma unroll
    for (int ph = 0; ph < 7; ++ph) af[k][ph] = ap[(ph * 64 + k) * 64];
  }
  f32x4 acc[7][4];
#pragma unroll
  for (int ph = 0; ph < 7; ++ph)
#pragma unroll
    for (int t = 0; t < 4; ++t) acc[ph][t] = f32x4{0.f, 0.f, 0.f, 0.f};
#pragma unroll
  for (int k = 0; k < 16; ++k) {
    u32x4 ca[7], cb[4];
#pragma unroll
    for (int tau = 0; tau < 4; ++tau) { cb[tau] = bf[k % G2_WIN][tau]; if (k + G2_WIN < 16) bf[k % G2_WIN][tau] = bp[tau][4 * (k + G2_WIN)]; }
#pragma unroll
    for (int ph = 0; ph < 7; ++ph) { ca[ph] = af[k % G2_WIN][ph]; if (k + G2_WIN < 16) af[k % G2_WIN][ph] = ap[(ph * 64 + k + G2_WIN) * 64]; }
#pragma unroll
    for (int ph = 0; ph < 7; ++ph)
#pragma unroll
      for (int tau = 0; tau < 4; ++tau) acc[ph][tau] = mfma16(ca[ph], cb[tau], acc[ph][tau]);
    __builtin_amdgcn_sched_barrier(0);
  }
  LAS f32x4 *R = (LAS f32x4 *)L;
#pragma unroll
  for (int tau = 0; tau < 4; ++tau)
    if (tau != w) {
      const int slot = (w - tau - 1) & 3;
#pragma unroll
      for (int ph = 0; ph < 7; ++ph) R[((tau * 3 + slot) * 7 + ph) * 64 + lane] = acc[ph][tau];
    }
  __syncthreads();
  unsigned vv[7][2];
#pragma unroll
  for (int ph = 0; ph < 7; ++ph) {
    f32x4 s = w == 0 ? acc[ph][0] : w == 1 ? acc[ph][1] : w == 2 ? acc[ph][2] : acc[ph][3];
#pragma unroll
    for (int slot = 0; slot < 3; ++slot) s += R[((w * 3 + slot) * 7 + ph) * 64 + lane];
    const float zz[4] = {bf_lo(z1[ph].x), bf_hi(z1[ph].x), bf_lo(z1[ph].y), bf_hi(z1[ph].y)};
    float v[4];
#pragma unroll
    for (int e = 0; e < 4; ++e) v[e] = zz[e] * rbf(rbf(s[e]) + bsp);
    vv[ph][0] = pack_bf(v[0], v[1]);
    vv[ph][1] = pack_bf(v[2], v[3]);
  }
  unsigned char *vb = p.ws + O_VF + ((size_t)(4 * rt + w) * 28 * 64 + lane) * 16;
  if ((j & 1) == 0) {
    const int p0 = 7 * j / 2;
#pragma unroll
    for (int k = 0; k < 3; ++k) *(GAS u32x4 *)(vb + (size_t)(p0 + k) * 1024) = u32x4{vv[2 * k][0], vv[2 * k][1], vv[2 * k + 1][0], vv[2 * k + 1][1]};
    *(GAS u32x2 *)(vb + (size_t)(p0 + 3) * 1024) = u32x2{vv[6][0], vv[6][1]};
  } else {
    const int p0 = (7 * j - 1) / 2;
    *(GAS u32x2 *)(vb + (size_t)p0 * 1024 + 8) = u32x2{vv[0][0], vv[0][1]};
#pragma unroll
    for (int k = 0; k < 3; ++k)
      *(GAS u32x4 *)(vb + (size_t)(p0 + 1 + k) * 1024) = u32x4{vv[2 * k + 1][0], vv[2 * k + 1][1], vv[2 * k + 2][0], vv[2 * k + 2][1]};
  }
}

constexpr int K5_W = 0;
constexpr int K5_GB = 4 * 28 * 1024;
constexpr int K5_LDS = K5_GB + 256;

__global__ __launch_bounds__(256) void k5_gemm3(Params p) {
  extern __shared__ __attribute__((aligned(16))) unsigned char smem_raw[];
  LAS unsigned char *L = (LAS unsigned char *)smem_raw;
  const int tid = threadIdx.x, lane = tid & 63, b = blockIdx.x;
  const int w = __builtin_amdgcn_readfirstlane(tid >> 6);
  const int i16 = lane & 15, g = lane >> 4;
  int rt, j;
  block_rt_j(b, rt, j);
  {
    const unsigned char *src = p.ws + O_WOF + (size_t)(4 * j) * 28 * 1024 + (size_t)w * 28 * 1024 + lane * 16;
#pragma unroll
    for (int k = 0; k < 28; ++k)
      __builtin_amdgcn_global_load_lds((const GAS unsigned *)(src + k * 1024), (LAS unsigned *)(L + K5_W + (w * 28 + k) * 1024), 16, 0, 0);
  }
  u32x4 vf[28];
  const GAS u32x4 *vp = (const GAS u32x4 *)(p.ws + O_VF) + (size_t)(4 * rt + w) * 28 * 64 + lane;
#pragma unroll
  for (int k = 0; k < 28; ++k) vf[k] = vp[k * 64];
  u32x4 xr[2];
#pragma unroll
  for (int dl = 0; dl < 2; ++dl) xr[dl] = *(const GAS u32x4 *)(p.ws + O_X1F + ((size_t)((b * 4 + w) * 2 + dl) * 64 + lane) * 16);
  if (tid < 64) ((LAS float *)(L + K5_GB))[tid] = mod_val(p.ws, 5 * 512 + 64 * j + tid);
  asm volatile("s_waitcnt vmcnt(0)" ::: "memory");
  __syncthreads();
  f32x4 acc[4];
#pragma unroll
  for (int dd = 0; dd < 4; ++dd) acc[dd] = f32x4{0.f, 0.f, 0.f, 0.f};
#pragma unroll
  for (int k = 0; k < 28; ++k)
#pragma unroll
    for (int dd = 0; dd < 4; ++dd) acc[dd] = mfma16(*(LAS u32x4 *)(L + K5_W + (dd * 28 + k) * 1024 + lane * 16), vf[k], acc[dd]);
  const int t = 64 * rt + tile_row(w, i16);
#pragma unroll
  for (int dl = 0; dl < 2; ++dl) {
    LAS float *GB = (LAS float *)(L + K5_GB) + 32 * dl + 8 * g;
    float o[8];
#pragma unroll
    for (int e = 0; e < 8; ++e) {
      const float a = rbf(acc[2 * dl + (e >> 2)][e & 3]);
      const unsigned xu = xr[dl][e >> 1];
      o[e] = ((e & 1) ? bf_hi(xu) : bf_lo(xu)) + rbf(GB[e] * a);
    }
    *(GAS u32x4 *)(p.out + (size_t)t * DM + 64 * j + 32 * dl + 8 * g) =
        u32x4{pack_bf(o[0], o[1]), pack_bf(o[2], o[3]), pack_bf(o[4], o[5]), pack_bf(o[6], o[7])};
  }
}

extern "C" void kernel_launch(void *const *d_in, const int *in_sizes, int n_in, void *d_out, int out_size, void *d_ws,
                              size_t ws_size, hipStream_t stream) {
  Params p;
  p.x = (const u16 *)d_in[0];
  p.a_re = (const u16 *)d_in[1]; p.a_im = (const u16 *)d_in[2];
  p.b_re = (const u16 *)d_in[3]; p.b_im = (const u16 *)d_in[4];
  p.c_re = (const u16 *)d_in[5]; p.c_im = (const u16 *)d_in[6];
  p.log_dt = (const u16 *)d_in[7]; p.dskip = (const u16 *)d_in[8];
  p.w_in = (const u16 *)d_in[10]; p.w_sp = (const u16 *)d_in[11]; p.b_sp = (const u16 *)d_in[12];
  p.w_out = (const u16 *)d_in[13]; p.cvec = (const u16 *)d_in[14]; p.w_mod = (const u16 *)d_in[15];
  p.out = (u16 *)d_out;
  p.ws = (unsigned char *)d_ws;
  (void)hipFuncSetAttribute((const void *)k1_prep, hipFuncAttributeMaxDynamicSharedMemorySize, K1_LDS);
  (void)hipFuncSetAttribute((const void *)k2_conv, hipFuncAttributeMaxDynamicSharedMemorySize, K2_LDS);
  (void)hipFuncSetAttribute((const void *)k3_gemm1, hipFuncAttributeMaxDynamicSharedMemorySize, K3_LDS);
  (void)hipFuncSetAttribute((const void *)k4_gemm2, hipFuncAttributeMaxDynamicSharedMemorySize, K4_LDS);
  (void)hipFuncSetAttribute((const void *)k5_gemm3, hipFuncAttributeMaxDynamicSharedMemorySize, K5_LDS);
  k1_prep<<<256, 1024, K1_LDS, stream>>>(p);
  k2_conv<<<256, 256, K2_LDS, stream>>>(p);
  k3_gemm1<<<256, 256, K3_LDS, stream>>>(p);
  k4_gemm2<<<256, 256, K4_LDS, stream>>>(p);
  k5_gemm3<<<256, 256, K5_LDS, stream>>>(p);
}
```

```cpp
#include <hip/hip_runtime.h>
#include <stdint.h>
#include <string.h>

#define GAS __attribute__((address_space(1)))
#define LAS __attribute__((address_space(3)))
#define CAS __attribute__((address_space(4)))
#define DEVI __device__ __forceinline__
typedef unsigned short u16;
typedef unsigned long long u64;
typedef float f32x2 __attribute__((ext_vector_type(2)));
typedef float f32x4 __attribute__((ext_vector_type(4)));
typedef float f32x16 __attribute__((ext_vector_type(16)));
typedef unsigned u32x2 __attribute__((ext_vector_type(2)));
typedef unsigned u32x4 __attribute__((ext_vector_type(4)));
typedef __bf16 bf16x2_t __attribute__((ext_vector_type(2)));
typedef GAS unsigned gu32;
typedef GAS u64 gu64;
typedef __amdgpu_buffer_rsrc_t rsrc_t;
#define RLX_AGENT __ATOMIC_RELAXED, __HIP_MEMORY_SCOPE_AGENT

constexpr int SEQ = 2048, DM = 512, NS = 128, FF = 1792, FH = 896;

constexpr size_t O_GRAN = 131072;
constexpr size_t O_E12 = O_GRAN + 32 * 8 * 64 * 16;
constexpr size_t O_MOD = 1 << 20;
constexpr size_t O_XT = 2 << 20;
constexpr size_t O_XNT = 4 << 20;
constexpr size_t O_X1T = 9 << 20;
constexpr size_t O_WP = 11 << 20;
constexpr size_t O_WOF = 13 << 20;
constexpr size_t O_Z2F = 14 << 20;
constexpr size_t O_VF = 18 << 20;
constexpr int WS_USED = 22 << 20;
constexpr int C_EPOCH = 0;
constexpr int C_E1 = 1024;
constexpr int C_E2 = 4096;
constexpr int C_F3 = C_E2 + 64 * 32;
constexpr int C_F4 = C_F3 + 256 * 32;
static_assert((C_F4 + 256 * 32) * 4 <= (int)O_GRAN, "control block");

struct Params {
  const u16 *x, *a_re, *a_im, *b_re, *b_im, *c_re, *c_im, *log_dt, *dskip;
  const u16 *w_in, *w_sp, *b_sp, *w_out, *cvec, *w_mod;
  u16 *out;
  unsigned char *ws;
};

#define STAMP_DECL
#define STAMP(kid, slot) do {} while (0)
#define STAMP_FLUSH(kid, n) do {} while (0)
DEVI float u2f(unsigned u) { return __builtin_bit_cast(float, u); }
DEVI unsigned f2u(float f) { return __builtin_bit_cast(unsigned, f); }
DEVI float bf_lo(unsigned u) { return u2f(u << 16); }
DEVI float bf_hi(unsigned u) { return u2f(u & 0xffff0000u); }
DEVI f32x2 bf_2(unsigned u) { return f32x2{bf_lo(u), bf_hi(u)}; }
DEVI float bf2f(u16 h) { return u2f((unsigned)h << 16); }
DEVI unsigned pack2(f32x2 v) { return __builtin_bit_cast(unsigned, __builtin_convertvector(v, bf16x2_t)); }
DEVI unsigned pack_bf(float lo, float hi) { return pack2(f32x2{lo, hi}); }
DEVI float rbf(float f) { return bf_lo(pack_bf(f, f)); }
DEVI f32x2 pk_fma(f32x2 a, f32x2 b, f32x2 c) { return __builtin_elementwise_fma(a, b, c); }
DEVI f32x2 lo2(f32x4 v) { return f32x2{v[0], v[1]}; }
DEVI f32x2 hi2(f32x4 v) { return f32x2{v[2], v[3]}; }
template <int CTRL> DEVI float dpp_f(float x) {
  return __builtin_bit_cast(float, __builtin_amdgcn_update_dpp(0, __builtin_bit_cast(int, x), CTRL, 0xf, 0xf, true));
}
DEVI float wave_sum(float x) {
  x += dpp_f<0xB1>(x);
  x += dpp_f<0x4E>(x);
  x += dpp_f<0x141>(x);
  x += dpp_f<0x140>(x);
  auto s = __builtin_amdgcn_permlane16_swap(f2u(x), f2u(x), false, false);
  x = u2f(s[0]) + u2f(s[1]);
  auto t = __builtin_amdgcn_permlane32_swap(f2u(x), f2u(x), false, false);
  return u2f(t[0]) + u2f(t[1]);
}
DEVI u32x2 tr_read(LAS unsigned char *p) {
  typedef short v4i16_t __attribute__((ext_vector_type(4)));
  return __builtin_bit_cast(u32x2, __builtin_amdgcn_ds_read_tr16_b64_v4i16((LAS v4i16_t *)p));
}
DEVI u32x4 cat2(u32x2 a, u32x2 b) { return u32x4{a.x, a.y, b.x, b.y}; }
DEVI float ln_rstd(float var) {
  return __builtin_amdgcn_rcpf(rbf(__builtin_amdgcn_sqrtf(rbf(rbf(var) + 1e-5f))));
}
DEVI f32x2 gelu2(f32x2 x) {
  constexpr float K0 = -0.7978845608028654f * 2.0f * 1.4426950408889634f, K1 = K0 * 0.044715f;
  const f32x2 u = x * pk_fma(x * x, f32x2{K1, K1}, f32x2{K0, K0});
  const f32x2 d = f32x2{__builtin_amdgcn_exp2f(u[0]), __builtin_amdgcn_exp2f(u[1])} + f32x2{1.f, 1.f};
  return x * f32x2{__builtin_amdgcn_rcpf(d[0]), __builtin_amdgcn_rcpf(d[1])};
}
DEVI void block_rt_j(int b, int &rt, int &j) {
  const int x = b & 7, q = b >> 3;
  rt = 8 * (x >> 1) + (q & 7);
  j = 4 * (x & 1) + (q >> 3);
}
DEVI int tile_row(int tau, int i) { return 32 * (tau >> 1) + 8 * (i >> 2) + 4 * (tau & 1) + (i & 3); }
DEVI f32x2 cmul(f32x2 z, f32x2 v, f32x2 vr) { return pk_fma(f32x2{z[1], z[1]}, vr, f32x2{z[0], z[0]} * v); }
DEVI f32x2 crot(f32x2 v) { return f32x2{-v[1], v[0]}; }
DEVI f32x2 cexp_m(float lr2, float rev, float m) {
  const float er = __builtin_amdgcn_exp2f(lr2 * m);
  const float fr = __builtin_amdgcn_fractf(rev * m);
  return f32x2{er * __builtin_amdgcn_cosf(fr), er * __builtin_amdgcn_sinf(fr)};
}

#define SB() __builtin_amdgcn_sched_barrier(0)
#define WAIT_VM(n) asm volatile("s_waitcnt vmcnt(" #n ")" ::: "memory")
#define WAIT_LGKM0() asm volatile("s_waitcnt lgkmcnt(0)" ::: "memory")
#define BAR() asm volatile("s_waitcnt lgkmcnt(0)\n\ts_barrier" ::: "memory")
constexpr int AUX_SC1 = 16;
constexpr unsigned SPIN_CAP = 1u << 18;

constexpr int B_RC = 4160;
constexpr int B_U = 8 * B_RC;
constexpr int U_CH = 95 * 80 + 16;
constexpr int B_RED = B_U + 2 * U_CH;
constexpr int KT_ROW = 208;
constexpr int B_KT = 4 * B_RC;
constexpr int B_KD = B_KT + 256 * KT_ROW;
constexpr int B_WT = B_KD + 4096;
constexpr int B_TR = B_WT + 4096;
constexpr int TR_WAVE = 16 * 1040;
constexpr int B_Z1 = B_TR + 4 * TR_WAVE;
constexpr int B_ST = B_Z1 + 14336;
constexpr int B_E = B_ST + 512;
constexpr int B_ZS = B_E + 2 * 224 * 4;
constexpr int B_GB = B_ZS + 2048;
constexpr int B_LDS = B_GB + 256;
constexpr int B_LN = 0;
constexpr int B_WO = 16384;
constexpr int B_MW = 24576;
constexpr int B_MS = B_MW + 32768;
constexpr int B_X = 0;
constexpr int B_ZT = 0;
static_assert(B_RED + 16384 <= B_WT && B_MS + 1024 + 512 <= B_WT && 65536 <= B_WT && (B_KD & 15) == 0 && (B_WT & 15) == 0 &&
              4 * 3 * 7 * 1024 <= B_Z1 && B_LDS <= 163840, "lds map");
constexpr int G1_WIN = 4;
constexpr int G2_WIN = 3;

DEVI rsrc_t mk_rsrc(const void *p, int bytes) { return __builtin_amdgcn_make_buffer_rsrc((void *)p, 0, bytes, 0x00020000); }
DEVI u32x4 ld128(rsrc_t rs, int voff, int soff) { return __builtin_amdgcn_raw_buffer_load_b128(rs, voff, soff, 0); }
DEVI unsigned ld16(rsrc_t rs, int voff, int soff) { return __builtin_amdgcn_raw_buffer_load_b16(rs, voff, soff, 0); }
DEVI u32x4 ld_sc1(rsrc_t rs, int voff, int soff) { return __builtin_amdgcn_raw_buffer_load_b128(rs, voff, soff, AUX_SC1); }
DEVI u32x2 ld_sc1_64(rsrc_t rs, int voff, int soff) { return __builtin_amdgcn_raw_buffer_load_b64(rs, voff, soff, AUX_SC1); }
DEVI unsigned ld_sc1_32(rsrc_t rs, int voff, int soff) { return __builtin_amdgcn_raw_buffer_load_b32(rs, voff, soff, AUX_SC1); }
DEVI void st_sc1(rsrc_t rs, int voff, int soff, u32x4 v) { __builtin_amdgcn_raw_buffer_store_b128(v, rs, voff, soff, AUX_SC1); }
DEVI void st_sc1_64(rsrc_t rs, int voff, int soff, u32x2 v) { __builtin_amdgcn_raw_buffer_store_b64(v, rs, voff, soff, AUX_SC1); }
DEVI void st_sc1_32(rsrc_t rs, int voff, int soff, unsigned v) { __builtin_amdgcn_raw_buffer_store_b32(v, rs, voff, soff, AUX_SC1); }
DEVI float pick4(f32x4 v, int r) { return r == 0 ? v[0] : r == 1 ? v[1] : r == 2 ? v[2] : v[3]; }
template <bool GE> DEVI void wave_poll(gu32 *word, bool on, unsigned want) {
  for (unsigned spins = 0; spins < SPIN_CAP; ++spins) {
    const unsigned v = on ? __hip_atomic_load(word, RLX_AGENT) : want;
    if (__all(GE ? v >= want : v == want)) break;
    __builtin_amdgcn_s_sleep(1);
  }
  __builtin_amdgcn_fence(__ATOMIC_ACQUIRE, "wavefront");
}

DEVI void mma16(f32x4 &c, u32x4 a, u32x4 b) { asm volatile("v_mfma_f32_16x16x32_bf16 %0, %1, %2, %0" : "+v"(c) : "v"(a), "v"(b)); }
DEVI void mma16a(f32x4 &c, u32x4 a, u32x4 b) { asm volatile("v_mfma_f32_16x16x32_bf16 %0, %1, %2, %0" : "+v"(c) : "v"(a), "a"(b)); }
DEVI void mma32(f32x16 &c, u32x4 a, u32x4 b) { asm volatile("v_mfma_f32_32x32x16_bf16 %0, %1, %2, %0" : "+v"(c) : "v"(a), "v"(b)); }
DEVI void mma16z(f32x4 &c, u32x4 a, u32x4 b) { asm volatile("v_mfma_f32_16x16x32_bf16 %0, %1, %2, 0" : "=&v"(c) : "v"(a), "v"(b)); }
DEVI void mma16n(f32x4 &c, u32x4 a, u32x4 b) { asm volatile("s_nop 1\n\tv_mfma_f32_16x16x32_bf16 %0, %1, %2, %0" : "+v"(c) : "v"(a), "v"(b)); }
DEVI void mma16zn(f32x4 &c, u32x4 a, u32x4 b) { asm volatile("s_nop 1\n\tv_mfma_f32_16x16x32_bf16 %0, %1, %2, 0" : "=&v"(c) : "v"(a), "v"(b)); }
DEVI void mma16az(f32x4 &c, u32x4 a, u32x4 b) { asm volatile("v_mfma_f32_16x16x32_bf16 %0, %1, %2, 0" : "=&v"(c) : "v"(a), "a"(b)); }
DEVI void mma16A(f32x4 &c, u32x4 a, u32x4 b) { asm volatile("v_mfma_f32_16x16x32_bf16 %0, %1, %2, %0" : "+v"(c) : "a"(a), "v"(b)); }
DEVI void mma16Az(f32x4 &c, u32x4 a, u32x4 b) { asm volatile("v_mfma_f32_16x16x32_bf16 %0, %1, %2, 0" : "=&v"(c) : "a"(a), "v"(b)); }
DEVI void mma32z(f32x16 &c, u32x4 a, u32x4 b) { asm volatile("s_nop 1\n\tv_mfma_f32_32x32x16_bf16 %0, %1, %2, 0" : "=&v"(c) : "v"(a), "v"(b)); }
DEVI void mma32n(f32x16 &c, u32x4 a, u32x4 b) { asm volatile("s_nop 1\n\tv_mfma_f32_32x32x16_bf16 %0, %1, %2, %0" : "+v"(c) : "v"(a), "v"(b)); }
#define MFMA_FENCE() do { SB(); asm volatile("s_nop 7\n\ts_nop 7" ::: "memory"); SB(); } while (0)
#define FRESH_IDS()                                   \
  int tid = threadIdx.x;                              \
  asm volatile("" : "+v"(tid));                       \
  const int lane = tid & 63, i16 = lane & 15, g = lane >> 4; \
  (void)i16; (void)g
DEVI u32x4 opaque_ones() {
  u32x4 o = u32x4{0x3f803f80u, 0x3f803f80u, 0x3f803f80u, 0x3f803f80u};
  asm volatile("" : "+v"(o));
  return o;
}
struct Transit {
  rsrc_t rw;
  int v[4], so, la;
  DEVI void begin(int lane, int w, int rt, int tau) {
#pragma unroll
    for (int c = 0; c < 4; ++c) v[c] = lane * 16 + c * 4096;
    so = ((64 * rt + 32 * (tau >> 1) + 4 * (tau & 1)) * SEQ + 512 * w) * 2;
    la = B_TR + w * TR_WAVE;
  }
  DEVI void step(int i) {
    asm volatile("s_mov_b32 m0, %0\n\ts_add_u32 %0, %0, 1040\n\tbuffer_load_dwordx4 %2, %3, %1 offen lds"
                 : "+s"(la) : "s"(so), "v"(v[i & 3]), "s"(rw) : "memory", "scc");
    if ((i & 3) == 3) so += 32768;
  }
  DEVI void all() {
#pragma unroll
    for (int i = 0; i < 16; ++i) step(i);
  }
};
DEVI void transit_read(LAS unsigned char *L, int lane, int w, u32x4 (&stash)[16][4], int slot) {
  LAS unsigned char *T = L + B_TR + w * TR_WAVE + (lane & 15) * 1040 + (lane >> 4) * 16;
  asm volatile("" : "+v"(T));
#pragma unroll
  for (int k = 0; k < 16; ++k) {
    const u32x4 v = *(LAS u32x4 *)(T + 64 * k);
    if (slot == 0) stash[k][0] = v; else if (slot == 1) stash[k][1] = v; else if (slot == 2) stash[k][2] = v; else stash[k][3] = v;
  }
  WAIT_LGKM0();
}

template <int VAR>
DEVI void gemm1_loop(rsrc_t rs, LAS unsigned char *L, int lane, const int (&wo)[4], u32x4 (&wf)[G1_WIN][4], f32x4 (&acc)[4][4],
                     f32x4 (&ssum)[2], f32x4 (&ssq)[2]) {
  const int g = lane >> 4, q = (lane & 15) >> 2, pq = lane & 3;
  LAS unsigned char *xb[4];
#pragma unroll
  for (int tau = 0; tau < 4; ++tau)
    xb[tau] = L + B_X + (8 * g + q) * 128 + (((4 * (tau >> 1) + pq) ^ (4 * (q >> 1))) * 16) + (((tau & 1) ^ (g & 1)) * 8);
  constexpr bool act[4] = {true, VAR != 3, true, VAR != 2};
  const u32x4 ones = opaque_ones();
  u32x2 xl[2][4], xh[2][4];
#pragma unroll
  for (int tau = 0; tau < 4; ++tau) { xl[0][tau] = tr_read(xb[tau]); xh[0][tau] = tr_read(xb[tau] + 512); }
  SB();
#pragma unroll
  for (int ks = 0; ks < 16; ++ks) {
    const int c = ks & 1, n = c ^ 1;
    u32x4 cur[4];
#pragma unroll
    for (int s = 0; s < 4; ++s) cur[s] = wf[ks % G1_WIN][s];
#pragma unroll
    for (int tau = 0; tau < 4; ++tau) {
      const u32x4 xf = cat2(xl[c][tau], xh[c][tau]);
      if (ks == 0) mma16z(acc[0][tau], xf, cur[0]); else mma16(acc[0][tau], xf, cur[0]);
      if (ks + 1 < 16) xl[n][tau] = tr_read(xb[tau] + (ks + 1) * 4096);
      SB();
      if (act[1]) { if (ks == 0) mma16z(acc[1][tau], xf, cur[1]); else mma16(acc[1][tau], xf, cur[1]); }
      else if (tau >= 2) { if (ks == 0) mma16z(ssum[tau - 2], xf, ones); else mma16(ssum[tau - 2], xf, ones); }
      if (ks + 1 < 16) xh[n][tau] = tr_read(xb[tau] + (ks + 1) * 4096 + 512);
      SB();
      if (ks == 0) mma16z(acc[2][tau], cur[2], xf); else mma16(acc[2][tau], cur[2], xf);
      SB();
      if (act[3]) { if (ks == 0) mma16z(acc[3][tau], cur[3], xf); else mma16(acc[3][tau], cur[3], xf); }
      else if (tau < 2) { if (ks == 0) mma16z(ssum[tau], xf, ones); else mma16(ssum[tau], xf, ones); }
      if (VAR == 2 && tau < 2) { if (ks == 0) mma16z(ssq[tau], xf, xf); else mma16(ssq[tau], xf, xf); }
      if (VAR == 3 && tau >= 2) { if (ks == 0) mma16z(ssq[tau - 2], xf, xf); else mma16(ssq[tau - 2], xf, xf); }
      if (ks + G1_WIN < 16 && act[tau]) wf[ks % G1_WIN][tau] = ld_sc1(rs, wo[tau], (ks + G1_WIN) * 1024);
      SB();
    }
  }
  MFMA_FENCE();
}

__global__ __launch_bounds__(256, 1) void kb_main(Params p) {
  extern __shared__ __attribute__((aligned(16))) unsigned char smem_raw[];
  LAS unsigned char *L = (LAS unsigned char *)smem_raw;
  const int b = blockIdx.x;
  const int w = __builtin_amdgcn_readfirstlane(threadIdx.x >> 6);
  const rsrc_t rs = mk_rsrc(p.ws, WS_USED);
  gu32 *ctl = (gu32 *)p.ws;
  int rt, j;
  block_rt_j(b, rt, j);
  const int d0 = 64 * (b & 7) + 2 * (b >> 3);
  const unsigned epoch_raw = __hip_atomic_load(ctl + C_EPOCH, RLX_AGENT);
  unsigned epoch;

  u32x4 stash[16][4];
  Transit tr;
  tr.rw = mk_rsrc(p.w_sp, SEQ * SEQ * 2);

  unsigned prm[2][6], ldt[2];
  int kidx[12];
  {
    FRESH_IDS();
    const int t0 = 8 * b;
    const rsrc_t rx = mk_rsrc(p.x, SEQ * DM * 2);
    u32x4 xr[2];
#pragma unroll
    for (int r = 0; r < 2; ++r) xr[r] = ld128(rx, lane * 16, (t0 + 2 * w + r) * (DM * 2));
    u32x4 jb[8];
    unsigned cv = 0;
    if (b < 96) {
      const rsrc_t rm = mk_rsrc(p.w_mod, DM * 3072 * 2);
#pragma unroll
      for (int q = 0; q < 8; ++q) jb[q] = ld128(rm, (lane >> 2) * (3072 * 2) + (lane & 3) * 16, ((128 * w + 16 * q) * 3072 + 32 * b) * 2);
      cv = ((const GAS unsigned *)p.cvec)[64 * w + lane];
    } else if (b < 208) {
      const rsrc_t ro = mk_rsrc(p.w_out, FH * DM * 2);
      const int bb = b - 96;
#pragma unroll
      for (int q = 0; q < 2; ++q) jb[q] = ld128(ro, (tid >> 4) * (DM * 2) + (tid & 15) * 16, ((32 * (bb >> 2) + 16 * q) * DM + 128 * (bb & 3)) * 2);
    }
    {
      const int po = d0 * NS + (w & 1) * 64 + lane;
      const u16 *pa[6] = {p.a_re, p.a_im, p.b_re, p.b_im, p.c_re, p.c_im};
#pragma unroll
      for (int a = 0; a < 6; ++a)
#pragma unroll
        for (int c = 0; c < 2; ++c) prm[c][a] = ((const GAS u16 *)pa[a])[po + c * NS];
#pragma unroll
      for (int c = 0; c < 2; ++c) ldt[c] = ((const GAS u16 *)p.log_dt)[d0 + c];
    }
    SB();
    tr.begin(lane, w, rt, w);
    tr.all();
    SB();
#pragma unroll
    for (int e = 0; e < 12; ++e) {
      const int l = 2047 - (8 * tid + e);
      const int lr = (int)rbf((float)(e < 8 ? l : max(l, 0)));
      kidx[e] = min(lr, 384 + (lr >> 2));
    }
    f32x2 xv[2][4], xn[2][4];
#pragma unroll
    for (int r = 0; r < 2; ++r) {
#pragma unroll
      for (int e = 0; e < 4; ++e) xv[r][e] = bf_2(xr[r][e]);
      const f32x2 s2 = (xv[r][0] + xv[r][1]) + (xv[r][2] + xv[r][3]);
      const float mean = wave_sum(s2[0] + s2[1]) * (1.f / DM);
      f32x2 q2 = {0.f, 0.f};
#pragma unroll
      for (int e = 0; e < 4; ++e) { const f32x2 c = xv[r][e] - f32x2{mean, mean}; q2 = pk_fma(c, c, q2); }
      const float rsd = ln_rstd(wave_sum(q2[0] + q2[1]) * (1.f / DM)), mb = -rbf(mean) * rsd;
#pragma unroll
      for (int e = 0; e < 4; ++e) xn[r][e] = pk_fma(xv[r][e], f32x2{rsd, rsd}, f32x2{mb, mb});
    }
    {
      u32x4 a0, a1, n0, n1;
#pragma unroll
      for (int e = 0; e < 4; ++e) {
        const unsigned lo = __builtin_amdgcn_perm(xr[1][e], xr[0][e], 0x05040100u), hi = __builtin_amdgcn_perm(xr[1][e], xr[0][e], 0x07060302u);
        const unsigned nl = pack_bf(xn[0][e][0], xn[1][e][0]), nh = pack_bf(xn[0][e][1], xn[1][e][1]);
        if (e < 2) { a0[2 * e] = lo; a0[2 * e + 1] = hi; n0[2 * e] = nl; n0[2 * e + 1] = nh; }
        else { a1[2 * e - 4] = lo; a1[2 * e - 3] = hi; n1[2 * e - 4] = nl; n1[2 * e - 3] = nh; }
      }
      LAS u32x4 *B0 = (LAS u32x4 *)(L + B_LN + w * 2048) + 2 * lane;
      B0[0] = a0; B0[1] = a1; B0[512] = n0; B0[513] = n1;
    }
    if (b < 96) {
      LAS unsigned char *MW = L + B_MW + w * 8192 + lane * 16;
#pragma unroll
      for (int q = 0; q < 8; ++q) *(LAS u32x4 *)(MW + q * 1024) = jb[q];
      const f32x2 c2 = bf_2(cv);
      const f32x2 e2 = f32x2{__builtin_amdgcn_exp2f(c2[0] * -1.4426950408889634f), __builtin_amdgcn_exp2f(c2[1] * -1.4426950408889634f)} + f32x2{1.f, 1.f};
      ((LAS unsigned *)(L + B_MS))[64 * w + lane] = pack2(c2 * f32x2{__builtin_amdgcn_rcpf(e2[0]), __builtin_amdgcn_rcpf(e2[1])});
      u32x4 sa4[4], wb[4][2];
      LAS unsigned char *A = L + B_MS + w * 256 + g * 16;
      LAS unsigned char *Bq = L + B_MW + w * 8192 + (8 * g + (i16 >> 2)) * 64 + (lane & 3) * 8;
#pragma unroll
      for (int ks = 0; ks < 4; ++ks) {
        sa4[ks] = *(LAS u32x4 *)(A + ks * 64);
#pragma unroll
        for (int ct = 0; ct < 2; ++ct) wb[ks][ct] = cat2(tr_read(Bq + ks * 2048 + ct * 32), tr_read(Bq + ks * 2048 + ct * 32 + 256));
      }
      f32x4 m0, m1;
      mma16zn(m0, sa4[0], wb[0][0]);
      mma16z(m1, sa4[0], wb[0][1]);
#pragma unroll
      for (int ks = 1; ks < 4; ++ks) { mma16n(m0, sa4[ks], wb[ks][0]); mma16(m1, sa4[ks], wb[ks][1]); }
      MFMA_FENCE();
      asm volatile("" :: "v"(m0), "v"(m1));
      if (g == 0) {
        LAS float *M = (LAS float *)(L + B_MS + 1024) + 32 * w + i16;
        M[0] = m0[0]; M[16] = m1[0];
      }
    } else if (b < 208) {
#pragma unroll
      for (int q = 0; q < 2; ++q) *(LAS u32x4 *)(L + B_WO + q * 4096 + tid * 16) = jb[q];
    }
    BAR();
    {
      LAS unsigned *S = (LAS unsigned *)(L + B_LN) + tid;
      u32x4 v[4];
#pragma unroll
      for (int a = 0; a < 4; ++a) { LAS unsigned *Sa = S + (a >> 1) * 2048 + (a & 1) * 256; v[a] = u32x4{Sa[0], Sa[512], Sa[1024], Sa[1536]}; }
#pragma unroll
      for (int a = 0; a < 4; ++a) st_sc1(rs, tid * 16 + (a & 1) * 4096, (int)((a >> 1) ? O_XNT : O_XT) + b * 8192, v[a]);
    }
    if (b < 96) {
      if (tid < 32) {
        LAS float *M = (LAS float *)(L + B_MS + 1024) + tid;
        st_sc1_32(rs, tid * 4, (int)O_MOD + 128 * b, f2u(rbf((M[0] + M[32]) + (M[64] + M[96]))));
      }
    } else if (b < 208) {
      const int bb = b - 96;
#pragma unroll
      for (int k = 0; k < 2; ++k) {
        const int dl = 2 * w + k;
        LAS unsigned char *A = L + B_WO + (4 * g + (i16 >> 2)) * 256 + (32 * (dl >> 1) + 8 * (lane & 3) + 4 * (dl & 1)) * 2;
        st_sc1(rs, lane * 16, (int)O_WOF + ((8 * (bb & 3) + dl) * 28 + (bb >> 2)) * 1024, cat2(tr_read(A), tr_read(A + 16 * 256)));
      }
    } else {
      const int idx = (b - 208) * 256 + tid;
      if (idx < 2 * FF / 2) st_sc1(rs, idx * 16, (int)O_E12, u32x4{0, 0, 0, 0});
      if (b == 255 && tid < 64) st_sc1_32(rs, tid * 128, C_E2 * 4, 0u);
    }
    SB();
    epoch = __builtin_amdgcn_readfirstlane(epoch_raw) + 1u;
    WAIT_VM(0);
    BAR();
    if (tid < 8) __hip_atomic_store(ctl + C_E1 + tid * 256 + b, epoch, RLX_AGENT);
    SB();
    transit_read(L, lane, w, stash, 0);
    BAR();
  }

  {
    FRESH_IDS();
    {
      const int part = w >> 1, n = (w & 1) * 64 + lane;
      LAS unsigned char *T0 = L + B_KT + n * (2 * KT_ROW) + part * 64, *T1 = L + B_TR + n * (2 * KT_ROW) + part * 64;
      asm volatile("" : "+v"(T0), "+v"(T1));
      f32x2 z[2][2], zm[2], zmr[2], y[2][2], ym[2], ymr[2];
#pragma unroll
      for (int c = 0; c < 2; ++c) {
        const float dt = rbf(expf(bf_lo(ldt[c])));
        const float lr2 = dt * bf_lo(prm[c][0]) * 1.4426950408889634f, rev = dt * bf_lo(prm[c][1]) * 0.15915494309189535f;
        const f32x2 cc = {bf_lo(prm[c][4]), bf_lo(prm[c][5])};
        const f32x2 beta = cmul(f32x2{dt * bf_lo(prm[c][2]), dt * bf_lo(prm[c][3])}, cc, crot(cc));
        const f32x2 e0 = cexp_m(lr2, rev, part ? 512.f : 0.f), e1 = cexp_m(lr2, rev, part ? 64.f : 16.f), w1 = cexp_m(lr2, rev, part ? 4.f : 1.f);
        const f32x2 e1r = crot(e1);
        z[c][0] = cmul(beta, e0, crot(e0));
        z[c][1] = cmul(z[c][0], e1, e1r);
        zm[c] = cmul(e1, e1, e1r); zmr[c] = crot(zm[c]);
        y[c][0] = f32x2{1.f, 0.f};
        y[c][1] = w1;
        ym[c] = cmul(w1, w1, crot(w1)); ymr[c] = crot(ym[c]);
      }
#pragma unroll
      for (int grp = 0; grp < 4; ++grp) {
#pragma unroll
        for (int c = 0; c < 2; ++c) {
          u32x4 re, im;
#pragma unroll
          for (int q = 0; q < 4; ++q) {
            re[q] = pack_bf(z[c][0][0], z[c][1][0]); im[q] = pack_bf(z[c][0][1], z[c][1][1]);
            z[c][0] = cmul(z[c][0], zm[c], zmr[c]); z[c][1] = cmul(z[c][1], zm[c], zmr[c]);
          }
          LAS unsigned char *T = c ? T1 : T0;
          *(LAS u32x4 *)(T + 16 * grp) = re;
          *(LAS u32x4 *)(T + 16 * grp + KT_ROW) = im;
        }
      }
#pragma unroll
      for (int grp = 0; grp < 2; ++grp) {
#pragma unroll
        for (int c = 0; c < 2; ++c) {
          u32x4 re, im;
#pragma unroll
          for (int q = 0; q < 4; ++q) {
            re[q] = pack_bf(y[c][0][0], y[c][1][0]); im[q] = pack_bf(-y[c][0][1], -y[c][1][1]);
            y[c][0] = cmul(y[c][0], ym[c], ymr[c]); y[c][1] = cmul(y[c][1], ym[c], ymr[c]);
          }
          LAS unsigned char *T = (c ? T1 : T0) + 128 - part * 32;
          *(LAS u32x4 *)(T + 16 * grp) = re;
          *(LAS u32x4 *)(T + 16 * grp + KT_ROW) = im;
        }
      }
      BAR();
      f32x4 acc[2];
      u32x4 a[2][8], bq[2][8];
#pragma unroll
      for (int c = 0; c < 2; ++c) {
        LAS unsigned char *TA = L + (c ? B_TR : B_KT) + (8 * g + (i16 >> 2)) * KT_ROW + 8 * (lane & 3) + 32 * w;
        LAS unsigned char *TB = L + (c ? B_TR : B_KT) + (8 * g + (i16 >> 2)) * KT_ROW + 8 * (lane & 3) + 128 + 32 * (w >> 1);
#pragma unroll
        for (int ks = 0; ks < 8; ++ks) {
          a[c][ks] = cat2(tr_read(TA + ks * 32 * KT_ROW), tr_read(TA + (ks * 32 + 4) * KT_ROW));
          bq[c][ks] = cat2(tr_read(TB + ks * 32 * KT_ROW), tr_read(TB + (ks * 32 + 4) * KT_ROW));
        }
      }
      mma16zn(acc[0], a[0][0], bq[0][0]);
      mma16zn(acc[1], a[1][0], bq[1][0]);
#pragma unroll
      for (int ks = 1; ks < 8; ++ks) { mma16n(acc[0], a[0][ks], bq[0][ks]); mma16n(acc[1], a[1][ks], bq[1][ks]); }
      MFMA_FENCE();
#pragma unroll
      for (int c = 0; c < 2; ++c) {
        LAS u16 *KD = (LAS u16 *)(L + B_KD + c * 2048) + (16 * w + 4 * g) * 16 + i16;
        const unsigned k01 = pack_bf(acc[c][0], acc[c][1]), k23 = pack_bf(acc[c][2], acc[c][3]);
        KD[0] = (u16)k01; KD[16] = (u16)(k01 >> 16); KD[32] = (u16)k23; KD[48] = (u16)(k23 >> 16);
      }
      if (w == 0) {
        for (unsigned spins = 0; spins < SPIN_CAP; ++spins) {
          const u32x4 f = ld_sc1(rs, lane * 16, (C_E1 + (b & 7) * 256) * 4);
          if (__all(((f[0] ^ epoch) | (f[1] ^ epoch) | (f[2] ^ epoch) | (f[3] ^ epoch)) == 0u)) break;
          __builtin_amdgcn_s_sleep(1);
        }
        __builtin_amdgcn_fence(__ATOMIC_ACQUIRE, "wavefront");
      }
      BAR();
    }
    const int ch = w >> 1, pp = w & 1;
    const int i32 = lane & 31, h = lane >> 5;
    const int wks = b & 15, wfg = b >> 4;
    u32x4 xn[2];
#pragma unroll
    for (int c = 0; c < 2; ++c) xn[c] = ld_sc1(rs, tid * 8192 + 16 * c, (int)O_XNT + d0 * 16);
    const unsigned mraw = ld_sc1_32(rs, ((lane >> 1) & 3) * 2048 + (lane & 1) * 4, (int)O_MOD + d0 * 4);
    const rsrc_t rin = mk_rsrc(p.w_in, DM * FF * 2);
    u32x4 wr[2], cb8[2], sb8[2];
#pragma unroll
    for (int k = 0; k < 2; ++k) {
      const int ph = (w + 4 * k < 7) ? w + 4 * k : w;
      wr[k] = ld128(rin, (lane >> 1) * (FF * 2) + (lane & 1) * 16, (32 * wks * FF + 112 * wfg + 16 * ph) * 2);
      cb8[k] = ld_sc1(rs, g * 32 + 16 * k, (int)O_MOD + (4 * 512 + 32 * wks) * 4);
      sb8[k] = ld_sc1(rs, g * 32 + 16 * k, (int)O_MOD + (3 * 512 + 32 * wks) * 4);
    }
    u32x2 xe[4];
#pragma unroll
    for (int rg = 0; rg < 4; ++rg) xe[rg] = ld_sc1_64(rs, (4 * i32 + rg) * 8192 + 8 * h, (int)O_XT + 128 * pp * 8192 + (d0 + ch) * 16);
    SB();
    {
      unsigned v[2][12];
#pragma unroll
      for (int c = 0; c < 2; ++c)
#pragma unroll
        for (int e = 0; e < 12; ++e) v[c][e] = ((LAS u16 *)(L + B_KD + c * 2048))[kidx[e]];
      SB();
#pragma unroll
      for (int c = 0; c < 2; ++c) {
#pragma unroll
        for (int e = 8; e < 12; ++e) v[c][e] = tid == 255 ? 0u : v[c][e];
        unsigned D[6];
#pragma unroll
        for (int e = 0; e < 6; ++e) D[e] = v[c][2 * e] | (v[c][2 * e + 1] << 16);
        unsigned A[5];
#pragma unroll
        for (int e = 0; e < 5; ++e) A[e] = __builtin_amdgcn_alignbit(D[e + 1], D[e], 16);
        LAS unsigned char *R = L + c * 4 * B_RC + 16 * tid;
        *(LAS u32x4 *)(R + 0 * B_RC) = u32x4{D[0], D[1], D[2], D[3]};
        *(LAS u32x4 *)(R + 1 * B_RC) = u32x4{A[0], A[1], A[2], A[3]};
        *(LAS u32x4 *)(R + 2 * B_RC) = u32x4{D[1], D[2], D[3], D[4]};
        *(LAS u32x4 *)(R + 3 * B_RC) = u32x4{A[1], A[2], A[3], A[4]};
      }
      if (tid < 32) *(LAS u32x4 *)(L + (tid >> 2) * B_RC + 4096 + 16 * (tid & 3)) = u32x4{0, 0, 0, 0};
    }
    SB();
    const float dsk = bf2f(((const CAS u16 *)p.dskip)[d0 + ch]);
    if (tid < 155) {
      *(LAS u32x4 *)(L + B_U + 16 * tid) = u32x4{0, 0, 0, 0};
      *(LAS u32x4 *)(L + B_U + U_CH + 16 * tid) = u32x4{0, 0, 0, 0};
    }
    const float gaw = u2f(__builtin_amdgcn_readlane(mraw, 4 + ch));
    {
      LAS unsigned char *PU = L + B_U + (31 + (tid >> 2)) * 80 + 16 * (tid & 3);
#pragma unroll
      for (int c = 0; c < 2; ++c) {
        const float sa = u2f(__builtin_amdgcn_readlane(mraw, c)), c1 = rbf(1.f + u2f(__builtin_amdgcn_readlane(mraw, 2 + c)));
        u32x4 hv;
#pragma unroll
        for (int e = 0; e < 4; ++e) hv[e] = pack2(pk_fma(bf_2(xn[c][e]), f32x2{c1, c1}, f32x2{sa, sa}));
        *(LAS u32x4 *)(PU + c * U_CH) = hv;
      }
    }
    LAS unsigned char *WT = L + B_WT + w * 1024;
    *(LAS u32x4 *)(WT + lane * 16) = wr[0];
    u32x4 sbv = u32x4{pack_bf(u2f(sb8[0][0]), u2f(sb8[0][1])), pack_bf(u2f(sb8[0][2]), u2f(sb8[0][3])),
                      pack_bf(u2f(sb8[1][0]), u2f(sb8[1][1])), pack_bf(u2f(sb8[1][2]), u2f(sb8[1][3]))};
    f32x2 c2[4];
#pragma unroll
    for (int e = 0; e < 4; ++e) {
      const unsigned a0 = cb8[e >> 1][2 * (e & 1)], a1 = cb8[e >> 1][2 * (e & 1) + 1];
      c2[e] = bf_2(pack2(f32x2{u2f(a0), u2f(a1)} + f32x2{1.f, 1.f}));
    }
    u32x4 ones = opaque_ones();
    asm volatile("" : "+v"(sbv));
    tr.begin(lane, w, rt, (w + 1) & 3);
    BAR();

    f32x16 acc0, acc1;
    u32x4 raw[2], scl[2];
    f32x4 e1[2], e2[2];
    int ei[4];
    {
      LAS unsigned char *Al = L + ch * 4 * B_RC + (3 - (i32 & 3)) * B_RC + (2044 - 4 * (i32 >> 2) + 8 * h) * 2 - 64 * pp - 128 * 15 - 2048;
      LAS unsigned char *Ah = Al + 8;
      asm volatile("" : "+v"(Ah));
      LAS unsigned char *Bb = L + B_U + ch * U_CH + 80 * i32 + 16 * h - 80 * pp;
      LAS unsigned char *WA = WT + (8 * g + (i16 >> 2)) * 32 + 8 * (lane & 3);
      u32x2 a0l[3], a0h[3], a1l[3], a1h[3];
      u32x4 b0[3], b1[3];
#define CONV_LOAD_A(n, sl)                                                                          \
  do {                                                                                              \
    const int o_ = 128 * (15 - ((n) >> 1)) + 32 * ((n) & 1);                                        \
    a0l[sl] = *(LAS u32x2 *)(Al + o_ + 2048); a0h[sl] = *(LAS u32x2 *)(Ah + o_ + 2048);             \
  } while (0)
#define CONV_LOAD_A1(n, sl)                                                                         \
  do {                                                                                              \
    const int o_ = 128 * (15 - ((n) >> 1)) + 32 * ((n) & 1);                                        \
    a1l[sl] = *(LAS u32x2 *)(Al + o_); a1h[sl] = *(LAS u32x2 *)(Ah + o_);                           \
  } while (0)
#define CONV_LOAD_B(n, sl)                                                                          \
  do {                                                                                              \
    b0[sl] = *(LAS u32x4 *)(Bb + (31 - 2 * ((n) >> 1)) * 80 + 32 * ((n) & 1));                      \
    b1[sl] = *(LAS u32x4 *)(Bb + (63 - 2 * ((n) >> 1)) * 80 + 32 * ((n) & 1));                      \
  } while (0)
#pragma unroll
      for (int n = 0; n < 2; ++n) { CONV_LOAD_A(n, n); CONV_LOAD_B(n, n); CONV_LOAD_A1(n, n); }
      SB();
#pragma unroll
      for (int n = 0; n < 32; ++n) {
        const int c = n % 3, nx = (n + 2) % 3;
        const u32x4 A0 = cat2(a0l[c], a0h[c]), A1 = cat2(a1l[c], a1h[c]);
        if (n == 0) mma32z(acc0, A0, b0[c]); else if (n == 1) mma32n(acc0, A0, b0[c]); else mma32(acc0, A0, b0[c]);
        if (n + 2 < 32) CONV_LOAD_A(n + 2, nx);
        SB();
        if (n == 0) mma32z(acc1, A0, b1[c]); else mma32(acc1, A0, b1[c]);
        if (n + 2 < 32) CONV_LOAD_B(n + 2, nx);
        SB();
        if (n < 2) mma32n(acc1, A1, b0[c]); else mma32(acc1, A1, b0[c]);
        if (n + 2 < 32) CONV_LOAD_A1(n + 2, nx);
        if (n < 16) tr.step(n);
        if (n == 2 || n == 8) raw[n == 8] = cat2(tr_read(WA), tr_read(WA + 128));
        if (n == 5) *(LAS u32x4 *)(WT + lane * 16) = wr[1];
        if (n == 4 || n == 5 || n == 10 || n == 11) {
          const int k = n >= 10, e0 = 2 * (n & 1);
          scl[k][e0] = pack2(bf_2(raw[k][e0]) * c2[e0]);
          scl[k][e0 + 1] = pack2(bf_2(raw[k][e0 + 1]) * c2[e0 + 1]);
        }
        if (n == 6 || n == 12) { const int k = n == 12; mma16zn(e1[k], ones, scl[k]); mma16z(e2[k], sbv, raw[k]); }
        if (n == 9 || n == 15) {
          const int k = n == 15;
          asm volatile("" :: "v"(e1[k]), "v"(e2[k]));
          ei[2 * k] = (int)__builtin_rintf(e1[k][0] * 65536.f);
          ei[2 * k + 1] = (int)__builtin_rintf(e2[k][0] * 65536.f);
        }
        SB();
      }
      MFMA_FENCE();
    }
    if (pp) { const f32x16 t = acc0; acc0 = acc1; acc1 = t; }
    {
      LAS f32x4 *R = (LAS f32x4 *)(L + B_RED + (ch * 2 + (1 - pp)) * 4096) + lane;
#pragma unroll
      for (int rg = 0; rg < 4; ++rg) R[rg * 64] = f32x4{acc1[4 * rg], acc1[4 * rg + 1], acc1[4 * rg + 2], acc1[4 * rg + 3]};
    }
#pragma unroll
    for (int k = 0; k < 2; ++k) {
      const int ph = w + 4 * k;
      if (ph < 7) {
        const int F = 7 * wfg + ph;
        st_sc1(rs, lane * 16, (int)O_WP + (F * 16 + wks) * 1024, scl[k]);
        if (g == 0) {
          GAS u64 *E = (GAS u64 *)(p.ws + O_E12) + 16 * F + i16;
          __hip_atomic_fetch_add(E, (u64)(long long)ei[2 * k], RLX_AGENT);
          __hip_atomic_fetch_add(E + FF, (u64)(long long)ei[2 * k + 1], RLX_AGENT);
        }
      }
    }
    BAR();
    {
      const int d = d0 + ch;
      LAS f32x4 *R = (LAS f32x4 *)(L + B_RED + (ch * 2 + pp) * 4096) + lane;
      LAS unsigned char *U = L + B_U + ch * U_CH + (31 + 32 * pp + i32) * 80 + 8 * h;
      f32x4 o[4];
      u32x2 hv[4];
#pragma unroll
      for (int rg = 0; rg < 4; ++rg) { o[rg] = R[rg * 64]; hv[rg] = *(LAS u32x2 *)(U + 16 * rg); }
      SB();
      unsigned o0[4], o1[4];
      const f32x2 dk2 = {dsk, dsk}, ga2 = {gaw, gaw};
#pragma unroll
      for (int rg = 0; rg < 4; ++rg) {
        const f32x2 ya = lo2(o[rg]) + f32x2{acc0[4 * rg], acc0[4 * rg + 1]}, yb = hi2(o[rg]) + f32x2{acc0[4 * rg + 2], acc0[4 * rg + 3]};
        o0[rg] = pack2(pk_fma(ga2, pk_fma(bf_2(hv[rg].x), dk2, ya), bf_2(xe[rg].x)));
        o1[rg] = pack2(pk_fma(ga2, pk_fma(bf_2(hv[rg].y), dk2, yb), bf_2(xe[rg].y)));
      }
      const bool sw = (d >> 3) & 1;
      const int vo = (i32 >> 1) * 65536 + (i32 & 1) * 64 + 16 * h;
#pragma unroll
      for (int k = 0; k < 2; ++k) {
        auto s0 = __builtin_amdgcn_permlane32_swap(o0[2 * k], o0[2 * k + 1], false, false);
        auto s1 = __builtin_amdgcn_permlane32_swap(o1[2 * k], o1[2 * k + 1], false, false);
        const unsigned l0 = s0[0], h0 = s0[1], l1 = s1[0], h1 = s1[1];
        const u32x4 v = sw ? u32x4{h0, h1, l0, l1} : u32x4{l0, l1, h0, h1};
        st_sc1(rs, vo + 32 * k, (int)O_X1T + 16 * pp * 65536 + d * 128, v);
      }
    }
    SB();
    WAIT_VM(0);
    BAR();
    if (tid < 8) __hip_atomic_fetch_add(ctl + C_E2 + (tid * 8 + (b & 7)) * 32, 1u, RLX_AGENT);
    SB();
    transit_read(L, lane, w, stash, 1);
    tr.begin(lane, w, rt, (w + 2) & 3);
    tr.all();
    if (w == 0) wave_poll<true>(ctl + C_E2 + ((b & 7) * 8 + (lane & 7)) * 32, lane < 8, 32u);
    WAIT_VM(0);
    BAR();
  }

  u32x4 xres[2];
  {
    FRESH_IDS();
    const int ph2[2] = {2 * w, 2 * w + 1};
    const int ph1[2] = {w < 3 ? 2 * w : 5, w < 3 ? 2 * w + 1 : 6};
    const int Fs[4] = {56 + 7 * j + ph2[0], 56 + 7 * j + (ph2[1] < 7 ? ph2[1] : 0), 7 * j + ph1[0], 7 * j + (w == 2 ? 0 : ph1[1])};
    int wo[4];
#pragma unroll
    for (int s = 0; s < 4; ++s) wo[s] = (int)O_WP + Fs[s] * 16 * 1024 + lane * 16;
    u32x4 wf[G1_WIN][4];
    {
      u32x4 xs[16];
      const int xo = (int)O_X1T + rt * 65536 + tid * 16;
#pragma unroll
      for (int i = 0; i < 16; ++i) xs[i] = ld_sc1(rs, xo, i * 4096);
#pragma unroll
      for (int ks = 0; ks < G1_WIN; ++ks)
#pragma unroll
        for (int s = 0; s < 4; ++s) wf[ks][s] = ld_sc1(rs, wo[s], ks * 1024);
      const int tf = tid < 224 ? tid : 0, f = tf < 112 ? 112 * j + tf : FH + 112 * j + (tf - 112);
      const u32x2 ea = ld_sc1_64(rs, f * 8, (int)O_E12), eb = ld_sc1_64(rs, f * 8, (int)O_E12 + FF * 8);
      const unsigned gbv = ld_sc1_32(rs, (tid & 63) * 4, (int)O_MOD + (5 * 512 + 64 * j) * 4);
      SB();
      transit_read(L, lane, w, stash, 2);
      SB();
      const int r0 = tid >> 3, cc = tid & 7;
      LAS unsigned char *X = L + B_X + r0 * 128 + ((cc ^ (4 * ((r0 >> 1) & 1))) * 16);
#pragma unroll
      for (int i = 0; i < 16; ++i) *(LAS u32x4 *)(X + i * 4096) = xs[i];
      if (tid < 224) {
        const int ax = ea.x, ay = ea.y, bx = eb.x, by = eb.y;
        LAS float *E = (LAS float *)(L + B_E) + tid;
        E[0] = __builtin_fmaf((float)(ay + (ax < 0)), 65536.f, (float)ax * (1.f / 65536.f));
        E[224] = __builtin_fmaf((float)(by + (bx < 0)), 65536.f, (float)bx * (1.f / 65536.f));
      }
      if (tid < 64) ((LAS unsigned *)(L + B_GB))[tid] = gbv;
    }
    BAR();
    f32x4 acc[4][4], ssum[2], ssq[2];
    if (w < 2) gemm1_loop<0>(rs, L, lane, wo, wf, acc, ssum, ssq);
    else if (w == 2) gemm1_loop<2>(rs, L, lane, wo, wf, acc, ssum, ssq);
    else gemm1_loop<3>(rs, L, lane, wo, wf, acc, ssum, ssq);

    LAS float *SM = (LAS float *)(L + B_ST), *SR = SM + 64;
    asm volatile("" : "+v"(SM));
    SR = SM + 64;
    if (w >= 2 && g == (i16 >> 2)) {
#pragma unroll
      for (int k = 0; k < 2; ++k) {
        const int tau = (w == 2 ? 0 : 2) + k;
        const float mean = pick4(ssum[k], i16 & 3) * (1.f / DM);
        const float var = pick4(ssq[k], i16 & 3) * (1.f / DM) - mean * mean;
        const int s = tile_row(tau, i16);
        const float r = ln_rstd(fmaxf(var, 0.f));
        SM[s] = -rbf(mean) * r;
        SR[s] = r;
      }
    }
    {
      const int q = i16 >> 2, pq = lane & 3;
      LAS unsigned char *xb = L + B_X + (8 * g + q) * 128 + (((4 * (w >> 1) + pq) ^ (4 * (q >> 1))) * 16) + (((w & 1) ^ (g & 1)) * 8) + 2 * j * 4096;
#pragma unroll
      for (int dl = 0; dl < 2; ++dl) xres[dl] = cat2(tr_read(xb + dl * 4096), tr_read(xb + dl * 4096 + 512));
    }
    tr.begin(lane, w, rt, (w + 3) & 3);
    BAR();

    const int nz2 = w == 3 ? 1 : 2, nz1 = w == 2 ? 1 : 2;
    u32x4 zf[2][2];
    f32x4 e1z[2], e2z[2];
    float m1[4], r1[4];
    {
      LAS float *E1 = (LAS float *)(L + B_E);
      asm volatile("" : "+v"(E1));
      f32x4 m4[4], r4[4];
      float e1[2], e2[2];
#pragma unroll
      for (int t = 0; t < 4; ++t) { m4[t] = *(LAS f32x4 *)(SM + 32 * (t >> 1) + 8 * g + 4 * (t & 1)); r4[t] = *(LAS f32x4 *)(SR + 32 * (t >> 1) + 8 * g + 4 * (t & 1)); }
#pragma unroll
      for (int s = 0; s < 2; ++s) {
        const int p2 = s < nz2 ? ph2[s] : 0, p1 = s < nz1 ? ph1[s] : 0;
        e1[s] = E1[112 + 16 * p2 + i16]; e2[s] = E1[224 + 112 + 16 * p2 + i16];
        e1z[s] = *(LAS f32x4 *)(E1 + 16 * p1 + 4 * g); e2z[s] = *(LAS f32x4 *)(E1 + 224 + 16 * p1 + 4 * g);
      }
#pragma unroll
      for (int t = 0; t < 4; ++t) { const int sr = tile_row(t, i16); m1[t] = SM[sr]; r1[t] = SR[sr]; }
      SB();
      LAS unsigned char *ZW = L + B_ZT + i16 * 128 + 16 * g;
#pragma unroll
      for (int s = 0; s < 2; ++s) {
        if (s < nz2) {
          const f32x2 e12 = {e1[s], e1[s]}, e22 = {e2[s], e2[s]};
#pragma unroll
          for (int t = 0; t < 4; ++t) {
            const f32x4 a = acc[s][t];
            zf[s][t >> 1][2 * (t & 1)] = pack2(gelu2(pk_fma(lo2(a), lo2(r4[t]), pk_fma(lo2(m4[t]), e12, e22))));
            zf[s][t >> 1][2 * (t & 1) + 1] = pack2(gelu2(pk_fma(hi2(a), hi2(r4[t]), pk_fma(hi2(m4[t]), e12, e22))));
            if (s == 0) {
#pragma unroll
              for (int q = 0; q < 4; ++q) tr.step(4 * t + q);
              SB();
            }
          }
#pragma unroll
          for (int kp = 0; kp < 2; ++kp) *(LAS u32x4 *)(ZW + ph2[s] * 2048 + 64 * kp) = zf[s][kp];
        }
      }
    }
    if (tid < 128) *(LAS u32x4 *)(L + B_ZT + 112 * 128 + 16 * tid) = u32x4{0, 0, 0, 0};
    BAR();
    {
      const int q = i16 >> 2, pq = lane & 3;
      const u32x4 ones = opaque_ones();
      f32x4 su, sq;
      u32x4 za[4];
      LAS unsigned char *A = L + B_ZT + (8 * g + q) * 128 + (16 * w + 4 * pq) * 2;
#pragma unroll
      for (int st = 0; st < 4; ++st) za[st] = cat2(tr_read(A + st * 4096), tr_read(A + st * 4096 + 512));
      mma16zn(su, za[0], ones);
      mma16z(sq, za[0], za[0]);
#pragma unroll
      for (int st = 1; st < 4; ++st) { mma16(su, za[st], ones); mma16(sq, za[st], za[st]); }
      MFMA_FENCE();
      if (g == q) {
        const u32x4 gr = {f2u(pick4(su, i16 & 3)), epoch, f2u(pick4(sq, i16 & 3)), epoch};
        st_sc1(rs, (16 * w + i16) * 16, (int)O_GRAN + (rt * 8 + j) * 1024, gr);
      }
    }
    {
      LAS unsigned char *Z1 = L + B_Z1 + lane * 8;
      asm volatile("" : "+v"(Z1));
#pragma unroll
      for (int s = 0; s < 2; ++s) {
        if (s < nz1) {
          u32x2 z[4];
#pragma unroll
          for (int t = 0; t < 4; ++t) {
            const f32x2 m2 = {m1[t], m1[t]}, r2 = {r1[t], r1[t]};
            const f32x4 a = acc[2 + s][t];
            z[t] = u32x2{pack2(gelu2(pk_fma(lo2(a), r2, pk_fma(m2, lo2(e1z[s]), lo2(e2z[s]))))),
                         pack2(gelu2(pk_fma(hi2(a), r2, pk_fma(m2, hi2(e1z[s]), hi2(e2z[s])))))};
          }
#pragma unroll
          for (int t = 0; t < 4; ++t) *(LAS u32x2 *)(Z1 + ph1[s] * 2048 + t * 512) = z[t];
        }
      }
    }
    {
      float su, sq;
      for (unsigned spins = 0; spins < SPIN_CAP; ++spins) {
        u32x4 gr[8];
#pragma unroll
        for (int q = 0; q < 8; ++q) gr[q] = ld_sc1(rs, lane * 16, (int)O_GRAN + (rt * 8 + q) * 1024);
        unsigned bad = 0u;
        su = 0.f; sq = 0.f;
#pragma unroll
        for (int q = 0; q < 8; ++q) { bad |= (gr[q][1] ^ epoch) | (gr[q][3] ^ epoch); su += u2f(gr[q][0]); sq += u2f(gr[q][2]); }
        if (__all(bad == 0u)) break;
        __builtin_amdgcn_s_sleep(1);
      }
      const float mean = su * (1.f / FH), var = sq * (1.f / FH) - mean * mean;
      const float r = ln_rstd(fmaxf(var, 0.f));
      LAS float *ZR = (LAS float *)(L + B_ZS + w * 512);
      asm volatile("" : "+v"(ZR));
      ZR[lane] = r;
      ZR[64 + lane] = -rbf(mean) * r;
      f32x4 r4[4], m4[4];
#pragma unroll
      for (int t = 0; t < 4; ++t) { r4[t] = *(LAS f32x4 *)(ZR + 32 * (t >> 1) + 8 * g + 4 * (t & 1)); m4[t] = *(LAS f32x4 *)(ZR + 64 + 32 * (t >> 1) + 8 * g + 4 * (t & 1)); }
      SB();
#pragma unroll
      for (int s = 0; s < 2; ++s) {
        if (s < nz2) {
#pragma unroll
          for (int kp = 0; kp < 2; ++kp) {
            u32x4 o;
#pragma unroll
            for (int hf = 0; hf < 2; ++hf) {
              o[2 * hf] = pack2(pk_fma(bf_2(zf[s][kp][2 * hf]), lo2(r4[2 * kp + hf]), lo2(m4[2 * kp + hf])));
              o[2 * hf + 1] = pack2(pk_fma(bf_2(zf[s][kp][2 * hf + 1]), hi2(r4[2 * kp + hf]), hi2(m4[2 * kp + hf])));
            }
            st_sc1(rs, lane * 16, (int)O_Z2F + ((7 * j + ph2[s]) * 64 + 2 * rt + kp) * 1024, o);
          }
        }
      }
    }
    SB();
    WAIT_VM(0);
    BAR();
    if (tid == 0) __hip_atomic_store(ctl + C_F3 + (rt * 8 + j) * 32, epoch, RLX_AGENT);
    SB();
    transit_read(L, lane, w, stash, 3);
  }

  u32x4 wof[4][7];
  {
    FRESH_IDS();
    const float bsp = bf2f(p.b_sp[64 * rt + tile_row(w, i16)]);
    wave_poll<false>(ctl + C_F3 + ((8 * w + (lane & 7)) * 8 + j) * 32, lane < 8, epoch);
    const int ao = (int)O_Z2F + (7 * j * 64 + 16 * w) * 1024, vo = lane * 16;
    u32x4 af[G2_WIN][7];
#pragma unroll
    for (int k = 0; k < G2_WIN; ++k)
#pragma unroll
      for (int ph = 0; ph < 7; ++ph) af[k][ph] = ld_sc1(rs, vo, ao + (ph * 64 + k) * 1024);
    SB();
    f32x4 acc[7][4];
#pragma unroll
    for (int k = 0; k < 16; ++k) {
#pragma unroll
      for (int ph = 0; ph < 7; ++ph) {
        const u32x4 cur = af[k % G2_WIN][ph];
#pragma unroll
        for (int s = 0; s < 4; ++s) { if (k == 0) mma16az(acc[ph][s], cur, stash[k][s]); else mma16a(acc[ph][s], cur, stash[k][s]); }
        if (k + G2_WIN < 16) af[k % G2_WIN][ph] = ld_sc1(rs, vo, ao + (ph * 64 + k + G2_WIN) * 1024);
        SB();
      }
    }
    MFMA_FENCE();
#pragma unroll
    for (int dd = 0; dd < 4; ++dd)
#pragma unroll
      for (int k = 0; k < 7; ++k) wof[dd][k] = ld_sc1(rs, vo, (int)O_WOF + ((4 * j + dd) * 28 + 7 * w + k) * 1024);
    SB();
    BAR();
    LAS f32x4 *R = (LAS f32x4 *)L + lane;
#pragma unroll
    for (int s = 1; s < 4; ++s) {
      LAS f32x4 *Rs = R + (((s + w) & 3) * 3 + (s - 1)) * 7 * 64;
#pragma unroll
      for (int ph = 0; ph < 7; ++ph) Rs[ph * 64] = acc[ph][s];
    }
    BAR();
    unsigned vv[7][2];
    {
      const f32x2 b2 = {bsp, bsp};
      LAS f32x4 *Rw = R + w * 3 * 7 * 64;
      LAS unsigned char *Z1 = L + B_Z1 + (w * 64 + lane) * 8;
      asm volatile("" : "+v"(Z1));
      f32x4 ra[7][3];
      u32x2 z1[7];
#pragma unroll
      for (int ph = 0; ph < 7; ++ph) {
#pragma unroll
        for (int q = 0; q < 3; ++q) ra[ph][q] = Rw[(q * 7 + ph) * 64];
        z1[ph] = *(LAS u32x2 *)(Z1 + ph * 2048);
      }
      SB();
#pragma unroll
      for (int ph = 0; ph < 7; ++ph) {
        const f32x4 s = (acc[ph][0] + ra[ph][0]) + (ra[ph][1] + ra[ph][2]);
        vv[ph][0] = pack2(bf_2(z1[ph].x) * (lo2(s) + b2));
        vv[ph][1] = pack2(bf_2(z1[ph].y) * (hi2(s) + b2));
      }
    }
    const int vb = (int)O_VF + (4 * rt + w) * 28 * 1024;
    if ((j & 1) == 0) {
      const int p0 = 7 * j / 2;
#pragma unroll
      for (int k = 0; k < 3; ++k) st_sc1(rs, vo, vb + (p0 + k) * 1024, u32x4{vv[2 * k][0], vv[2 * k][1], vv[2 * k + 1][0], vv[2 * k + 1][1]});
      st_sc1_64(rs, vo, vb + (p0 + 3) * 1024, u32x2{vv[6][0], vv[6][1]});
    } else {
      const int p0 = (7 * j - 1) / 2;
      st_sc1_64(rs, vo + 8, vb + p0 * 1024, u32x2{vv[0][0], vv[0][1]});
#pragma unroll
      for (int k = 0; k < 3; ++k)
        st_sc1(rs, vo, vb + (p0 + 1 + k) * 1024, u32x4{vv[2 * k + 1][0], vv[2 * k + 1][1], vv[2 * k + 2][0], vv[2 * k + 2][1]});
    }
    SB();
    WAIT_VM(0);
    BAR();
    if (tid == 0) __hip_atomic_store(ctl + C_F4 + (rt * 8 + j) * 32, epoch, RLX_AGENT);
  }

  {
    FRESH_IDS();
    wave_poll<false>(ctl + C_F4 + (rt * 8 + 2 * w + (lane & 1)) * 32, lane < 2, epoch);
    u32x4 vf[7][4];
#pragma unroll
    for (int k = 0; k < 7; ++k)
#pragma unroll
      for (int s = 0; s < 4; ++s) vf[k][s] = ld_sc1(rs, lane * 16, (int)O_VF + ((4 * rt + ((s + w) & 3)) * 28 + 7 * w + k) * 1024);
    SB();
    f32x4 acc[4][4];
#pragma unroll
    for (int k = 0; k < 7; ++k)
#pragma unroll
      for (int s = 0; s < 4; ++s) {
#pragma unroll
        for (int dd = 0; dd < 4; ++dd) { if (k == 0) mma16Az(acc[dd][s], wof[dd][k], vf[k][s]); else mma16A(acc[dd][s], wof[dd][k], vf[k][s]); }
        SB();
      }
    MFMA_FENCE();
    LAS f32x4 *R = (LAS f32x4 *)L + lane;
#pragma unroll
    for (int s = 1; s < 4; ++s) {
      LAS f32x4 *Rs = R + (((s + w) & 3) * 3 + (s - 1)) * 4 * 64;
#pragma unroll
      for (int dd = 0; dd < 4; ++dd) Rs[dd * 64] = acc[dd][s];
    }
    BAR();
    const int t = 64 * rt + tile_row(w, i16);
    {
      LAS f32x4 *Rw = R + w * 3 * 4 * 64;
      LAS unsigned char *GB = L + B_GB + 32 * g;
      asm volatile("" : "+v"(GB));
      f32x4 ra[4][3], gb[4];
#pragma unroll
      for (int dd = 0; dd < 4; ++dd) {
#pragma unroll
        for (int q = 0; q < 3; ++q) ra[dd][q] = Rw[(q * 4 + dd) * 64];
        gb[dd] = *(LAS f32x4 *)(GB + 128 * (dd >> 1) + 16 * (dd & 1));
      }
      SB();
#pragma unroll
      for (int dl = 0; dl < 2; ++dl) {
        u32x4 o;
#pragma unroll
        for (int hf = 0; hf < 2; ++hf) {
          const int dd = 2 * dl + hf;
          const f32x4 s = (acc[dd][0] + ra[dd][0]) + (ra[dd][1] + ra[dd][2]);
          o[2 * hf] = pack2(pk_fma(lo2(gb[dd]), lo2(s), bf_2(xres[dl][2 * hf])));
          o[2 * hf + 1] = pack2(pk_fma(hi2(gb[dd]), hi2(s), bf_2(xres[dl][2 * hf + 1])));
        }
        *(GAS u32x4 *)(p.out + (size_t)t * DM + 64 * j + 32 * dl + 8 * g) = o;
      }
    }
    if (b == 0 && tid == 0) __hip_atomic_store(ctl + C_EPOCH, epoch, RLX_AGENT);
  }
}

extern "C" void kernel_launch(void *const *d_in, const int *in_sizes, int n_in, void *d_out, int out_size, void *d_ws,
                              size_t ws_size, hipStream_t stream) {
  Params p;
  p.x = (const u16 *)d_in[0];
  p.a_re = (const u16 *)d_in[1]; p.a_im = (const u16 *)d_in[2];
  p.b_re = (const u16 *)d_in[3]; p.b_im = (const u16 *)d_in[4];
  p.c_re = (const u16 *)d_in[5]; p.c_im = (const u16 *)d_in[6];
  p.log_dt = (const u16 *)d_in[7]; p.dskip = (const u16 *)d_in[8];
  p.w_in = (const u16 *)d_in[10]; p.w_sp = (const u16 *)d_in[11]; p.b_sp = (const u16 *)d_in[12];
  p.w_out = (const u16 *)d_in[13]; p.cvec = (const u16 *)d_in[14]; p.w_mod = (const u16 *)d_in[15];
  p.out = (u16 *)d_out;
  p.ws = (unsigned char *)d_ws;
  (void)hipFuncSetAttribute((const void *)kb_main, hipFuncAttributeMaxDynamicSharedMemorySize, B_LDS);
  kb_main<<<256, 256, B_LDS, stream>>>(p);
}
```

```cpp
#include <hip/hip_runtime.h>
#include <stdint.h>
#include <string.h>

#define GAS __attribute__((address_space(1)))
#define LAS __attribute__((address_space(3)))
#define CAS __attribute__((address_space(4)))
#define DEVI __device__ __forceinline__
typedef unsigned short u16;
typedef unsigned long long u64;
typedef float f32x2 __attribute__((ext_vector_type(2)));
typedef float f32x4 __attribute__((ext_vector_type(4)));
typedef float f32x16 __attribute__((ext_vector_type(16)));
typedef unsigned u32x2 __attribute__((ext_vector_type(2)));
typedef unsigned u32x4 __attribute__((ext_vector_type(4)));
typedef __bf16 bf16x2_t __attribute__((ext_vector_type(2)));
typedef GAS unsigned gu32;
typedef GAS u64 gu64;
typedef __amdgpu_buffer_rsrc_t rsrc_t;
#define RLX_AGENT __ATOMIC_RELAXED, __HIP_MEMORY_SCOPE_AGENT

constexpr int SEQ = 2048, DM = 512, NS = 128, FF = 1792, FH = 896;

constexpr size_t O_GRAN = 131072;
constexpr size_t O_E12 = O_GRAN + 32 * 8 * 64 * 16;
constexpr size_t O_MOD = 1 << 20;
constexpr size_t O_XT = 2 << 20;
constexpr size_t O_XNT = 4 << 20;
constexpr size_t O_X1T = 9 << 20;
constexpr size_t O_WP = 11 << 20;
constexpr size_t O_WOF = 13 << 20;
constexpr size_t O_Z2F = 14 << 20;
constexpr size_t O_VF = 18 << 20;
constexpr int WS_USED = 22 << 20;
constexpr int C_EPOCH = 0;
constexpr int C_E1 = 1024;
constexpr int C_E2 = 4096;
constexpr int C_F3 = C_E2 + 64 * 32;
constexpr int C_F4 = C_F3 + 256 * 32;
static_assert((C_F4 + 256 * 32) * 4 <= (int)O_GRAN, "control block");

struct Params {
  const u16 *x, *a_re, *a_im, *b_re, *b_im, *c_re, *c_im, *log_dt, *dskip;
  const u16 *w_in, *w_sp, *b_sp, *w_out, *cvec, *w_mod;
  u16 *out;
  unsigned char *ws;
};

#define STAMP_DECL
#define STAMP(kid, slot) do {} while (0)
#define STAMP_FLUSH(kid, n) do {} while (0)
DEVI float u2f(unsigned u) { return __builtin_bit_cast(float, u); }
DEVI unsigned f2u(float f) { return __builtin_bit_cast(unsigned, f); }
DEVI float bf_lo(unsigned u) { return u2f(u << 16); }
DEVI float bf_hi(unsigned u) { return u2f(u & 0xffff0000u); }
DEVI f32x2 bf_2(unsigned u) { return f32x2{bf_lo(u), bf_hi(u)}; }
DEVI float bf2f(u16 h) { return u2f((unsigned)h << 16); }
DEVI unsigned pack2(f32x2 v) { return __builtin_bit_cast(unsigned, __builtin_convertvector(v, bf16x2_t)); }
DEVI unsigned pack_bf(float lo, float hi) { return pack2(f32x2{lo, hi}); }
DEVI float rbf(float f) { return bf_lo(pack_bf(f, f)); }
DEVI f32x2 pk_fma(f32x2 a, f32x2 b, f32x2 c) { return __builtin_elementwise_fma(a, b, c); }
DEVI f32x2 lo2(f32x4 v) { return f32x2{v[0], v[1]}; }
DEVI f32x2 hi2(f32x4 v) { return f32x2{v[2], v[3]}; }
template <int CTRL> DEVI float dpp_f(float x) {
  return __builtin_bit_cast(float, __builtin_amdgcn_update_dpp(0, __builtin_bit_cast(int, x), CTRL, 0xf, 0xf, true));
}
DEVI float wave_sum(float x) {
  x += dpp_f<0xB1>(x);
  x += dpp_f<0x4E>(x);
  x += dpp_f<0x141>(x);
  x += dpp_f<0x140>(x);
  auto s = __builtin_amdgcn_permlane16_swap(f2u(x), f2u(x), false, false);
  x = u2f(s[0]) + u2f(s[1]);
  auto t = __builtin_amdgcn_permlane32_swap(f2u(x), f2u(x), false, false);
  return u2f(t[0]) + u2f(t[1]);
}
DEVI u32x2 tr_read(LAS unsigned char *p) {
  typedef short v4i16_t __attribute__((ext_vector_type(4)));
  return __builtin_bit_cast(u32x2, __builtin_amdgcn_ds_read_tr16_b64_v4i16((LAS v4i16_t *)p));
}
DEVI u32x4 cat2(u32x2 a, u32x2 b) { return u32x4{a.x, a.y, b.x, b.y}; }
DEVI float ln_rstd(float var) {
  return __builtin_amdgcn_rcpf(rbf(__builtin_amdgcn_sqrtf(rbf(rbf(var) + 1e-5f))));
}
DEVI f32x2 gelu2(f32x2 x) {
  constexpr float K0 = -0.7978845608028654f * 2.0f * 1.4426950408889634f, K1 = K0 * 0.044715f;
  const f32x2 u = x * pk_fma(x * x, f32x2{K1, K1}, f32x2{K0, K0});
  const f32x2 d = f32x2{__builtin_amdgcn_exp2f(u[0]), __builtin_amdgcn_exp2f(u[1])} + f32x2{1.f, 1.f};
  return x * f32x2{__builtin_amdgcn_rcpf(d[0]), __builtin_amdgcn_rcpf(d[1])};
}
DEVI void block_rt_j(int b, int &rt, int &j) {
  const int x = b & 7, q = b >> 3;
  rt = 8 * (x >> 1) + (q & 7);
  j = 4 * (x & 1) + (q >> 3);
}
DEVI int tile_row(int tau, int i) { return 32 * (tau >> 1) + 8 * (i >> 2) + 4 * (tau & 1) + (i & 3); }
DEVI f32x2 cmul(f32x2 z, f32x2 v, f32x2 vr) { return pk_fma(f32x2{z[1], z[1]}, vr, f32x2{z[0], z[0]} * v); }
DEVI f32x2 crot(f32x2 v) { return f32x2{-v[1], v[0]}; }
DEVI f32x2 cexp_m(float lr2, float rev, float m) {
  const float er = __builtin_amdgcn_exp2f(lr2 * m);
  const float fr = __builtin_amdgcn_fractf(rev * m);
  return f32x2{er * __builtin_amdgcn_cosf(fr), er * __builtin_amdgcn_sinf(fr)};
}

#define SB() __builtin_amdgcn_sched_barrier(0)
#define WAIT_VM(n) asm volatile("s_waitcnt vmcnt(" #n ")" ::: "memory")
#define WAIT_LGKM0() asm volatile("s_waitcnt lgkmcnt(0)" ::: "memory")
#define BAR() asm volatile("s_waitcnt lgkmcnt(0)\n\ts_barrier" ::: "memory")
constexpr int AUX_SC1 = 16;
constexpr unsigned SPIN_CAP = 1u << 18;

constexpr int B_RC = 4160;
constexpr int B_U = 8 * B_RC;
constexpr int U_CH = 95 * 80 + 16;
constexpr int B_RED = B_U + 2 * U_CH;
constexpr int KT_ROW = 208;
constexpr int B_KT = 4 * B_RC;
constexpr int B_KD = B_KT + 256 * KT_ROW;
constexpr int B_WT = B_KD + 4096;
constexpr int B_TR = B_WT + 4096;
constexpr int TR_WAVE = 16 * 1040;
constexpr int B_Z1 = B_TR + 4 * TR_WAVE;
constexpr int B_ST = B_Z1 + 14336;
constexpr int B_E = B_ST + 512;
constexpr int B_ZS = B_E + 2 * 224 * 4;
constexpr int B_GB = B_ZS + 2048;
constexpr int B_LDS = B_GB + 256;
constexpr int B_LN = 0;
constexpr int B_WO = 16384;
constexpr int B_MW = 24576;
constexpr int B_MS = B_MW + 32768;
constexpr int B_X = 0;
constexpr int B_ZT = 0;
static_assert(B_RED + 16384 <= B_WT && B_MS + 1024 + 512 <= B_WT && 65536 <= B_WT && (B_KD & 15) == 0 && (B_WT & 15) == 0 &&
              4 * 3 * 7 * 1024 <= B_Z1 && B_LDS <= 163840, "lds map");
constexpr int G1_WIN = 4;
constexpr int G2_WIN = 3;

DEVI rsrc_t mk_rsrc(const void *p, int bytes) { return __builtin_amdgcn_make_buffer_rsrc((void *)p, 0, bytes, 0x00020000); }
DEVI u32x4 ld128(rsrc_t rs, int voff, int soff) { return __builtin_amdgcn_raw_buffer_load_b128(rs, voff, soff, 0); }
DEVI unsigned ld16(rsrc_t rs, int voff, int soff) { return __builtin_amdgcn_raw_buffer_load_b16(rs, voff, soff, 0); }
DEVI u32x4 ld_sc1(rsrc_t rs, int voff, int soff) { return __builtin_amdgcn_raw_buffer_load_b128(rs, voff, soff, AUX_SC1); }
DEVI u32x2 ld_sc1_64(rsrc_t rs, int voff, int soff) { return __builtin_amdgcn_raw_buffer_load_b64(rs, voff, soff, AUX_SC1); }
DEVI unsigned ld_sc1_32(rsrc_t rs, int voff, int soff) { return __builtin_amdgcn_raw_buffer_load_b32(rs, voff, soff, AUX_SC1); }
DEVI void st_sc1(rsrc_t rs, int voff, int soff, u32x4 v) { __builtin_amdgcn_raw_buffer_store_b128(v, rs, voff, soff, AUX_SC1); }
DEVI void st_sc1_64(rsrc_t rs, int voff, int soff, u32x2 v) { __builtin_amdgcn_raw_buffer_store_b64(v, rs, voff, soff, AUX_SC1); }
DEVI void st_sc1_32(rsrc_t rs, int voff, int soff, unsigned v) { __builtin_amdgcn_raw_buffer_store_b32(v, rs, voff, soff, AUX_SC1); }
DEVI float pick4(f32x4 v, int r) { return r == 0 ? v[0] : r == 1 ? v[1] : r == 2 ? v[2] : v[3]; }
template <bool GE> DEVI void wave_poll(gu32 *word, bool on, unsigned want) {
  for (unsigned spins = 0; spins < SPIN_CAP; ++spins) {
    const unsigned v = on ? __hip_atomic_load(word, RLX_AGENT) : want;
    if (__all(GE ? v >= want : v == want)) break;
    __builtin_amdgcn_s_sleep(1);
  }
  __builtin_amdgcn_fence(__ATOMIC_ACQUIRE, "wavefront");
}

DEVI void mma16(f32x4 &c, u32x4 a, u32x4 b) { asm volatile("v_mfma_f32_16x16x32_bf16 %0, %1, %2, %0" : "+v"(c) : "v"(a), "v"(b)); }
DEVI void mma16a(f32x4 &c, u32x4 a, u32x4 b) { asm volatile("v_mfma_f32_16x16x32_bf16 %0, %1, %2, %0" : "+v"(c) : "v"(a), "a"(b)); }
DEVI void mma32(f32x16 &c, u32x4 a, u32x4 b) { asm volatile("v_mfma_f32_32x32x16_bf16 %0, %1, %2, %0" : "+v"(c) : "v"(a), "v"(b)); }
DEVI void mma16z(f32x4 &c, u32x4 a, u32x4 b) { asm volatile("v_mfma_f32_16x16x32_bf16 %0, %1, %2, 0" : "=&v"(c) : "v"(a), "v"(b)); }
DEVI void mma16n(f32x4 &c, u32x4 a, u32x4 b) { asm volatile("s_nop 1\n\tv_mfma_f32_16x16x32_bf16 %0, %1, %2, %0" : "+v"(c) : "v"(a), "v"(b)); }
DEVI void mma16zn(f32x4 &c, u32x4 a, u32x4 b) { asm volatile("s_nop 1\n\tv_mfma_f32_16x16x32_bf16 %0, %1, %2, 0" : "=&v"(c) : "v"(a), "v"(b)); }
DEVI void mma16az(f32x4 &c, u32x4 a, u32x4 b) { asm volatile("v_mfma_f32_16x16x32_bf16 %0, %1, %2, 0" : "=&v"(c) : "v"(a), "a"(b)); }
DEVI void mma16A(f32x4 &c, u32x4 a, u32x4 b) { asm volatile("v_mfma_f32_16x16x32_bf16 %0, %1, %2, %0" : "+v"(c) : "a"(a), "v"(b)); }
DEVI void mma16Az(f32x4 &c, u32x4 a, u32x4 b) { asm volatile("v_mfma_f32_16x16x32_bf16 %0, %1, %2, 0" : "=&v"(c) : "a"(a), "v"(b)); }
DEVI void mma32z(f32x16 &c, u32x4 a, u32x4 b) { asm volatile("s_nop 1\n\tv_mfma_f32_32x32x16_bf16 %0, %1, %2, 0" : "=&v"(c) : "v"(a), "v"(b)); }
DEVI void mma32n(f32x16 &c, u32x4 a, u32x4 b) { asm volatile("s_nop 1\n\tv_mfma_f32_32x32x16_bf16 %0, %1, %2, %0" : "+v"(c) : "v"(a), "v"(b)); }
#define MFMA_FENCE() do { SB(); asm volatile("s_nop 7\n\ts_nop 7" ::: "memory"); SB(); } while (0)
#define FRESH_IDS()                                   \
  int tid = threadIdx.x;                              \
  asm volatile("" : "+v"(tid));                       \
  const int lane = tid & 63, i16 = lane & 15, g = lane >> 4; \
  (void)i16; (void)g
DEVI u32x4 opaque_ones() {
  u32x4 o = u32x4{0x3f803f80u, 0x3f803f80u, 0x3f803f80u, 0x3f803f80u};
  asm volatile("" : "+v"(o));
  return o;
}
struct Transit {
  rsrc_t rw;
  int v[4], so, la;
  DEVI void begin(int lane, int w, int rt, int tau) {
#pragma unroll
    for (int c = 0; c < 4; ++c) v[c] = lane * 16 + c * 4096;
    so = ((64 * rt + 32 * (tau >> 1) + 4 * (tau & 1)) * SEQ + 512 * w) * 2;
    la = B_TR + w * TR_WAVE;
  }
  DEVI void step(int i) {
    asm volatile("s_mov_b32 m0, %0\n\ts_add_u32 %0, %0, 1040\n\tbuffer_load_dwordx4 %2, %3, %1 offen lds"
                 : "+s"(la) : "s"(so), "v"(v[i & 3]), "s"(rw) : "memory", "scc");
    if ((i & 3) == 3) so += 32768;
  }
  DEVI void all() {
#pragma unroll
    for (int i = 0; i < 16; ++i) step(i);
  }
};
DEVI void transit_read(LAS unsigned char *L, int lane, int w, u32x4 (&stash)[16][4], int slot) {
  LAS unsigned char *T = L + B_TR + w * TR_WAVE + (lane & 15) * 1040 + (lane >> 4) * 16;
  asm volatile("" : "+v"(T));
#pragma unroll
  for (int k = 0; k < 16; ++k) {
    const u32x4 v = *(LAS u32x4 *)(T + 64 * k);
    if (slot == 0) stash[k][0] = v; else if (slot == 1) stash[k][1] = v; else if (slot == 2) stash[k][2] = v; else stash[k][3] = v;
  }
  WAIT_LGKM0();
}

template <int VAR>
DEVI void gemm1_loop(rsrc_t rs, LAS unsigned char *L, int lane, const int (&wo)[4], u32x4 (&wf)[G1_WIN][4], f32x4 (&acc)[4][4],
                     f32x4 (&ssum)[2], f32x4 (&ssq)[2]) {
  const int g = lane >> 4, q = (lane & 15) >> 2, pq = lane & 3;
  LAS unsigned char *xb[4];
#pragma unroll
  for (int tau = 0; tau < 4; ++tau)
    xb[tau] = L + B_X + (8 * g + q) * 128 + (((4 * (tau >> 1) + pq) ^ (4 * (q >> 1))) * 16) + (((tau & 1) ^ (g & 1)) * 8);
  constexpr bool act[4] = {true, VAR != 3, true, VAR != 2};
  const u32x4 ones = opaque_ones();
  u32x2 xl[2][4], xh[2][4];
#pragma unroll
  for (int tau = 0; tau < 4; ++tau) { xl[0][tau] = tr_read(xb[tau]); xh[0][tau] = tr_read(xb[tau] + 512); }
  SB();
#pragma unroll
  for (int ks = 0; ks < 16; ++ks) {
    const int c = ks & 1, n = c ^ 1;
    u32x4 cur[4];
#pragma unroll
    for (int s = 0; s < 4; ++s) cur[s] = wf[ks % G1_WIN][s];
#pragma unroll
    for (int tau = 0; tau < 4; ++tau) {
      const u32x4 xf = cat2(xl[c][tau], xh[c][tau]);
      if (ks == 0) mma16z(acc[0][tau], xf, cur[0]); else mma16(acc[0][tau], xf, cur[0]);
      if (ks + 1 < 16) xl[n][tau] = tr_read(xb[tau] + (ks + 1) * 4096);
      SB();
      if (act[1]) { if (ks == 0) mma16z(acc[1][tau], xf, cur[1]); else mma16(acc[1][tau], xf, cur[1]); }
      else if (tau >= 2) { if (ks == 0) mma16z(ssum[tau - 2], xf, ones); else mma16(ssum[tau - 2], xf, ones); }
      if (ks + 1 < 16) xh[n][tau] = tr_read(xb[tau] + (ks + 1) * 4096 + 512);
      SB();
      if (ks == 0) mma16z(acc[2][tau], cur[2], xf); else mma16(acc[2][tau], cur[2], xf);
      SB();
      if (act[3]) { if (ks == 0) mma16z(acc[3][tau], cur[3], xf); else mma16(acc[3][tau], cur[3], xf); }
      else if (tau < 2) { if (ks == 0) mma16z(ssum[tau], xf, ones); else mma16(ssum[tau], xf, ones); }
      if (VAR == 2 && tau < 2) { if (ks == 0) mma16z(ssq[tau], xf, xf); else mma16(ssq[tau], xf, xf); }
      if (VAR == 3 && tau >= 2) { if (ks == 0) mma16z(ssq[tau - 2], xf, xf); else mma16(ssq[tau - 2], xf, xf); }
      if (ks + G1_WIN < 16 && act[tau]) wf[ks % G1_WIN][tau] = ld_sc1(rs, wo[tau], (ks + G1_WIN) * 1024);
      SB();
    }
  }
  MFMA_FENCE();
}

__global__ __launch_bounds__(256, 1) void kb_main(Params p) {
  extern __shared__ __attribute__((aligned(16))) unsigned char smem_raw[];
  LAS unsigned char *L = (LAS unsigned char *)smem_raw;
  const int b = blockIdx.x;
  const int w = __builtin_amdgcn_readfirstlane(threadIdx.x >> 6);
  const rsrc_t rs = mk_rsrc(p.ws, WS_USED);
  gu32 *ctl = (gu32 *)p.ws;
  int rt, j;
  block_rt_j(b, rt, j);
  const int d0 = 64 * (b & 7) + 2 * (b >> 3);
  const unsigned epoch_raw = __hip_atomic_load(ctl + C_EPOCH, RLX_AGENT);
  unsigned epoch;

  u32x4 stash[16][4];
  Transit tr;
  tr.rw = mk_rsrc(p.w_sp, SEQ * SEQ * 2);

  {
    u32x4 tv_[40]; int nt_ = 0;
    const int gi_ = blockIdx.x * 256 + threadIdx.x;
    unsigned long long pc;
    asm volatile("s_getpc_b64 %0" : "=s"(pc));
    const GAS unsigned char *c_ = (const GAS unsigned char *)pc + (threadIdx.x & 255) * 64;
    unsigned cv0_ = *(const GAS unsigned *)(c_), cv1_ = *(const GAS unsigned *)(c_ + 16384), cv2_ = *(const GAS unsigned *)(c_ + 32768);
    __builtin_amdgcn_sched_barrier(0);
    float xx = 1.0f + threadIdx.x;
    for (int it = 0; it < 300; ++it) asm volatile(".rept 16\n\tv_fma_f32 %0, %0, %0, %0\n\t.endr" : "+v"(xx));
    if (xx == 1234.5f) p.out[0] = 0;
    __builtin_amdgcn_sched_barrier(0);
    asm volatile("" :: "v"(cv0_), "v"(cv1_), "v"(cv2_));
    (void)nt_;
  }
  unsigned prm[2][6], ldt[2];
  int kidx[12];
  {
    FRESH_IDS();
    const int t0 = 8 * b;
    const rsrc_t rx = mk_rsrc(p.x, SEQ * DM * 2);
    u32x4 xr[2];
#pragma unroll
    for (int r = 0; r < 2; ++r) xr[r] = ld128(rx, lane * 16, (t0 + 2 * w + r) * (DM * 2));
    u32x4 jb[8];
    unsigned cv = 0;
    if (b < 96) {
      const rsrc_t rm = mk_rsrc(p.w_mod, DM * 3072 * 2);
#pragma unroll
      for (int q = 0; q < 8; ++q) jb[q] = ld128(rm, (lane >> 2) * (3072 * 2) + (lane & 3) * 16, ((128 * w + 16 * q) * 3072 + 32 * b) * 2);
      cv = ((const GAS unsigned *)p.cvec)[64 * w + lane];
    } else if (b < 208) {
      const rsrc_t ro = mk_rsrc(p.w_out, FH * DM * 2);
      const int bb = b - 96;
#pragma unroll
      for (int q = 0; q < 2; ++q) jb[q] = ld128(ro, (tid >> 4) * (DM * 2) + (tid & 15) * 16, ((32 * (bb >> 2) + 16 * q) * DM + 128 * (bb & 3)) * 2);
    }
    {
      const int po = d0 * NS + (w & 1) * 64 + lane;
      const u16 *pa[6] = {p.a_re, p.a_im, p.b_re, p.b_im, p.c_re, p.c_im};
#pragma unroll
      for (int a = 0; a < 6; ++a)
#pragma unroll
        for (int c = 0; c < 2; ++c) prm[c][a] = ((const GAS u16 *)pa[a])[po + c * NS];
#pragma unroll
      for (int c = 0; c < 2; ++c) ldt[c] = ((const GAS u16 *)p.log_dt)[d0 + c];
    }
    SB();
    tr.begin(lane, w, rt, w);
    tr.all();
    SB();
#pragma unroll
    for (int e = 0; e < 12; ++e) {
      const int l = 2047 - (8 * tid + e);
      const int lr = (int)rbf((float)(e < 8 ? l : max(l, 0)));
      kidx[e] = min(lr, 384 + (lr >> 2));
    }
    f32x2 xv[2][4], xn[2][4];
#pragma unroll
    for (int r = 0; r < 2; ++r) {
#pragma unroll
      for (int e = 0; e < 4; ++e) xv[r][e] = bf_2(xr[r][e]);
      const f32x2 s2 = (xv[r][0] + xv[r][1]) + (xv[r][2] + xv[r][3]);
      const float mean = wave_sum(s2[0] + s2[1]) * (1.f / DM);
      f32x2 q2 = {0.f, 0.f};
#pragma unroll
      for (int e = 0; e < 4; ++e) { const f32x2 c = xv[r][e] - f32x2{mean, mean}; q2 = pk_fma(c, c, q2); }
      const float rsd = ln_rstd(wave_sum(q2[0] + q2[1]) * (1.f / DM)), mb = -rbf(mean) * rsd;
#pragma unroll
      for (int e = 0; e < 4; ++e) xn[r][e] = pk_fma(xv[r][e], f32x2{rsd, rsd}, f32x2{mb, mb});
    }
    {
      u32x4 a0, a1, n0, n1;
#pragma unroll
      for (int e = 0; e < 4; ++e) {
        const unsigned lo = __builtin_amdgcn_perm(xr[1][e], xr[0][e], 0x05040100u), hi = __builtin_amdgcn_perm(xr[1][e], xr[0][e], 0x07060302u);
        const unsigned nl = pack_bf(xn[0][e][0], xn[1][e][0]), nh = pack_bf(xn[0][e][1], xn[1][e][1]);
        if (e < 2) { a0[2 * e] = lo; a0[2 * e + 1] = hi; n0[2 * e] = nl; n0[2 * e + 1] = nh; }
        else { a1[2 * e - 4] = lo; a1[2 * e - 3] = hi; n1[2 * e - 4] = nl; n1[2 * e - 3] = nh; }
      }
      LAS u32x4 *B0 = (LAS u32x4 *)(L + B_LN + w * 2048) + 2 * lane;
      B0[0] = a0; B0[1] = a1; B0[512] = n0; B0[513] = n1;
    }
    if (b < 96) {
      LAS unsigned char *MW = L + B_MW + w * 8192 + lane * 16;
#pragma unroll
      for (int q = 0; q < 8; ++q) *(LAS u32x4 *)(MW + q * 1024) = jb[q];
      const f32x2 c2 = bf_2(cv);
      const f32x2 e2 = f32x2{__builtin_amdgcn_exp2f(c2[0] * -1.4426950408889634f), __builtin_amdgcn_exp2f(c2[1] * -1.4426950408889634f)} + f32x2{1.f, 1.f};
      ((LAS unsigned *)(L + B_MS))[64 * w + lane] = pack2(c2 * f32x2{__builtin_amdgcn_rcpf(e2[0]), __builtin_amdgcn_rcpf(e2[1])});
      u32x4 sa4[4], wb[4][2];
      LAS unsigned char *A = L + B_MS + w * 256 + g * 16;
      LAS unsigned char *Bq = L + B_MW + w * 8192 + (8 * g + (i16 >> 2)) * 64 + (lane & 3) * 8;
#pragma unroll
      for (int ks = 0; ks < 4; ++ks) {
        sa4[ks] = *(LAS u32x4 *)(A + ks * 64);
#pragma unroll
        for (int ct = 0; ct < 2; ++ct) wb[ks][ct] = cat2(tr_read(Bq + ks * 2048 + ct * 32), tr_read(Bq + ks * 2048 + ct * 32 + 256));
      }
      f32x4 m0, m1;
      mma16zn(m0, sa4[0], wb[0][0]);
      mma16z(m1, sa4[0], wb[0][1]);
#pragma unroll
      for (int ks = 1; ks < 4; ++ks) { mma16n(m0, sa4[ks], wb[ks][0]); mma16(m1, sa4[ks], wb[ks][1]); }
      MFMA_FENCE();
      asm volatile("" :: "v"(m0), "v"(m1));
      if (g == 0) {
        LAS float *M = (LAS float *)(L + B_MS + 1024) + 32 * w + i16;
        M[0] = m0[0]; M[16] = m1[0];
      }
    } else if (b < 208) {
#pragma unroll
      for (int q = 0; q < 2; ++q) *(LAS u32x4 *)(L + B_WO + q * 4096 + tid * 16) = jb[q];
    }
    BAR();
    {
      LAS unsigned *S = (LAS unsigned *)(L + B_LN) + tid;
      u32x4 v[4];
#pragma unroll
      for (int a = 0; a < 4; ++a) { LAS unsigned *Sa = S + (a >> 1) * 2048 + (a & 1) * 256; v[a] = u32x4{Sa[0], Sa[512], Sa[1024], Sa[1536]}; }
#pragma unroll
      for (int a = 0; a < 4; ++a) st_sc1(rs, tid * 16 + (a & 1) * 4096, (int)((a >> 1) ? O_XNT : O_XT) + b * 8192, v[a]);
    }
    if (b < 96) {
      if (tid < 32) {
        LAS float *M = (LAS float *)(L + B_MS + 1024) + tid;
        st_sc1_32(rs, tid * 4, (int)O_MOD + 128 * b, f2u(rbf((M[0] + M[32]) + (M[64] + M[96]))));
      }
    } else if (b < 208) {
      const int bb = b - 96;
#pragma unroll
      for (int k = 0; k < 2; ++k) {
        const int dl = 2 * w + k;
        LAS unsigned char *A = L + B_WO + (4 * g + (i16 >> 2)) * 256 + (32 * (dl >> 1) + 8 * (lane & 3) + 4 * (dl & 1)) * 2;
        st_sc1(rs, lane * 16, (int)O_WOF + ((8 * (bb & 3) + dl) * 28 + (bb >> 2)) * 1024, cat2(tr_read(A), tr_read(A + 16 * 256)));
      }
    } else {
      const int idx = (b - 208) * 256 + tid;
      if (idx < 2 * FF / 2) st_sc1(rs, idx * 16, (int)O_E12, u32x4{0, 0, 0, 0});
      if (b == 255 && tid < 64) st_sc1_32(rs, tid * 128, C_E2 * 4, 0u);
    }
    SB();
    epoch = __builtin_amdgcn_readfirstlane(epoch_raw) + 1u;
    WAIT_VM(0);
    BAR();
    if (tid < 8) __hip_atomic_store(ctl + C_E1 + tid * 256 + b, epoch, RLX_AGENT);
    SB();
    transit_read(L, lane, w, stash, 0);
    BAR();
  }

  {
    FRESH_IDS();
    {
      const int part = w >> 1, n = (w & 1) * 64 + lane;
      LAS unsigned char *T0 = L + B_KT + n * (2 * KT_ROW) + part * 64, *T1 = L + B_TR + n * (2 * KT_ROW) + part * 64;
      asm volatile("" : "+v"(T0), "+v"(T1));
      f32x2 z[2][2], zm[2], zmr[2], y[2][2], ym[2], ymr[2];
#pragma unroll
      for (int c = 0; c < 2; ++c) {
        const float dt = rbf(expf(bf_lo(ldt[c])));
        const float lr2 = dt * bf_lo(prm[c][0]) * 1.4426950408889634f, rev = dt * bf_lo(prm[c][1]) * 0.15915494309189535f;
        const f32x2 cc = {bf_lo(prm[c][4]), bf_lo(prm[c][5])};
        const f32x2 beta = cmul(f32x2{dt * bf_lo(prm[c][2]), dt * bf_lo(prm[c][3])}, cc, crot(cc));
        const f32x2 e0 = cexp_m(lr2, rev, part ? 512.f : 0.f), e1 = cexp_m(lr2, rev, part ? 64.f : 16.f), w1 = cexp_m(lr2, rev, part ? 4.f : 1.f);
        const f32x2 e1r = crot(e1);
        z[c][0] = cmul(beta, e0, crot(e0));
        z[c][1] = cmul(z[c][0], e1, e1r);
        zm[c] = cmul(e1, e1, e1r); zmr[c] = crot(zm[c]);
        y[c][0] = f32x2{1.f, 0.f};
        y[c][1] = w1;
        ym[c] = cmul(w1, w1, crot(w1)); ymr[c] = crot(ym[c]);
      }
#pragma unroll
      for (int grp = 0; grp < 4; ++grp) {
#pragma unroll
        for (int c = 0; c < 2; ++c) {
          u32x4 re, im;
#pragma unroll
          for (int q = 0; q < 4; ++q) {
            re[q] = pack_bf(z[c][0][0], z[c][1][0]); im[q] = pack_bf(z[c][0][1], z[c][1][1]);
            z[c][0] = cmul(z[c][0], zm[c], zmr[c]); z[c][1] = cmul(z[c][1], zm[c], zmr[c]);
          }
          LAS unsigned char *T = c ? T1 : T0;
          *(LAS u32x4 *)(T + 16 * grp) = re;
          *(LAS u32x4 *)(T + 16 * grp + KT_ROW) = im;
        }
      }
#pragma unroll
      for (int grp = 0; grp < 2; ++grp) {
#pragma unroll
        for (int c = 0; c < 2; ++c) {
          u32x4 re, im;
#pragma unroll
          for (int q = 0; q < 4; ++q) {
            re[q] = pack_bf(y[c][0][0], y[c][1][0]); im[q] = pack_bf(-y[c][0][1], -y[c][1][1]);
            y[c][0] = cmul(y[c][0], ym[c], ymr[c]); y[c][1] = cmul(y[c][1], ym[c], ymr[c]);
          }
          LAS unsigned char *T = (c ? T1 : T0) + 128 - part * 32;
          *(LAS u32x4 *)(T + 16 * grp) = re;
          *(LAS u32x4 *)(T + 16 * grp + KT_ROW) = im;
        }
      }
      BAR();
      f32x4 acc[2];
      u32x4 a[2][8], bq[2][8];
#pragma unroll
      for (int c = 0; c < 2; ++c) {
        LAS unsigned char *TA = L + (c ? B_TR : B_KT) + (8 * g + (i16 >> 2)) * KT_ROW + 8 * (lane & 3) + 32 * w;
        LAS unsigned char *TB = L + (c ? B_TR : B_KT) + (8 * g + (i16 >> 2)) * KT_ROW + 8 * (lane & 3) + 128 + 32 * (w >> 1);
#pragma unroll
        for (int ks = 0; ks < 8; ++ks) {
          a[c][ks] = cat2(tr_read(TA + ks * 32 * KT_ROW), tr_read(TA + (ks * 32 + 4) * KT_ROW));
          bq[c][ks] = cat2(tr_read(TB + ks * 32 * KT_ROW), tr_read(TB + (ks * 32 + 4) * KT_ROW));
        }
      }
      mma16zn(acc[0], a[0][0], bq[0][0]);
      mma16zn(acc[1], a[1][0], bq[1][0]);
#pragma unroll
      for (int ks = 1; ks < 8; ++ks) { mma16n(acc[0], a[0][ks], bq[0][ks]); mma16n(acc[1], a[1][ks], bq[1][ks]); }
      MFMA_FENCE();
#pragma unroll
      for (int c = 0; c < 2; ++c) {
        LAS u16 *KD = (LAS u16 *)(L + B_KD + c * 2048) + (16 * w + 4 * g) * 16 + i16;
        const unsigned k01 = pack_bf(acc[c][0], acc[c][1]), k23 = pack_bf(acc[c][2], acc[c][3]);
        KD[0] = (u16)k01; KD[16] = (u16)(k01 >> 16); KD[32] = (u16)k23; KD[48] = (u16)(k23 >> 16);
      }
      if (w == 0) {
        for (unsigned spins = 0; spins < SPIN_CAP; ++spins) {
          const u32x4 f = ld_sc1(rs, lane * 16, (C_E1 + (b & 7) * 256) * 4);
          if (__all(((f[0] ^ epoch) | (f[1] ^ epoch) | (f[2] ^ epoch) | (f[3] ^ epoch)) == 0u)) break;
          __builtin_amdgcn_s_sleep(1);
        }
        __builtin_amdgcn_fence(__ATOMIC_ACQUIRE, "wavefront");
      }
      BAR();
    }
    const int ch = w >> 1, pp = w & 1;
    const int i32 = lane & 31, h = lane >> 5;
    const int wks = b & 15, wfg = b >> 4;
    u32x4 xn[2];
#pragma unroll
    for (int c = 0; c < 2; ++c) xn[c] = ld_sc1(rs, tid * 8192 + 16 * c, (int)O_XNT + d0 * 16);
    const unsigned mraw = ld_sc1_32(rs, ((lane >> 1) & 3) * 2048 + (lane & 1) * 4, (int)O_MOD + d0 * 4);
    const rsrc_t rin = mk_rsrc(p.w_in, DM * FF * 2);
    u32x4 wr[2], cb8[2], sb8[2];
#pragma unroll
    for (int k = 0; k < 2; ++k) {
      const int ph = (w + 4 * k < 7) ? w + 4 * k : w;
      wr[k] = ld128(rin, (lane >> 1) * (FF * 2) + (lane & 1) * 16, (32 * wks * FF + 112 * wfg + 16 * ph) * 2);
      cb8[k] = ld_sc1(rs, g * 32 + 16 * k, (int)O_MOD + (4 * 512 + 32 * wks) * 4);
      sb8[k] = ld_sc1(rs, g * 32 + 16 * k, (int)O_MOD + (3 * 512 + 32 * wks) * 4);
    }
    u32x2 xe[4];
#pragma unroll
    for (int rg = 0; rg < 4; ++rg) xe[rg] = ld_sc1_64(rs, (4 * i32 + rg) * 8192 + 8 * h, (int)O_XT + 128 * pp * 8192 + (d0 + ch) * 16);
    SB();
    {
      unsigned v[2][12];
#pragma unroll
      for (int c = 0; c < 2; ++c)
#pragma unroll
        for (int e = 0; e < 12; ++e) v[c][e] = ((LAS u16 *)(L + B_KD + c * 2048))[kidx[e]];
      SB();
#pragma unroll
      for (int c = 0; c < 2; ++c) {
#pragma unroll
        for (int e = 8; e < 12; ++e) v[c][e] = tid == 255 ? 0u : v[c][e];
        unsigned D[6];
#pragma unroll
        for (int e = 0; e < 6; ++e) D[e] = v[c][2 * e] | (v[c][2 * e + 1] << 16);
        unsigned A[5];
#pragma unroll
        for (int e = 0; e < 5; ++e) A[e] = __builtin_amdgcn_alignbit(D[e + 1], D[e], 16);
        LAS unsigned char *R = L + c * 4 * B_RC + 16 * tid;
        *(LAS u32x4 *)(R + 0 * B_RC) = u32x4{D[0], D[1], D[2], D[3]};
        *(LAS u32x4 *)(R + 1 * B_RC) = u32x4{A[0], A[1], A[2], A[3]};
        *(LAS u32x4 *)(R + 2 * B_RC) = u32x4{D[1], D[2], D[3], D[4]};
        *(LAS u32x4 *)(R + 3 * B_RC) = u32x4{A[1], A[2], A[3], A[4]};
      }
      if (tid < 32) *(LAS u32x4 *)(L + (tid >> 2) * B_RC + 4096 + 16 * (tid & 3)) = u32x4{0, 0, 0, 0};
    }
    SB();
    const float dsk = bf2f(((const CAS u16 *)p.dskip)[d0 + ch]);
    if (tid < 155) {
      *(LAS u32x4 *)(L + B_U + 16 * tid) = u32x4{0, 0, 0, 0};
      *(LAS u32x4 *)(L + B_U + U_CH + 16 * tid) = u32x4{0, 0, 0, 0};
    }
    const float gaw = u2f(__builtin_amdgcn_readlane(mraw, 4 + ch));
    {
      LAS unsigned char *PU = L + B_U + (31 + (tid >> 2)) * 80 + 16 * (tid & 3);
#pragma unroll
      for (int c = 0; c < 2; ++c) {
        const float sa = u2f(__builtin_amdgcn_readlane(mraw, c)), c1 = rbf(1.f + u2f(__builtin_amdgcn_readlane(mraw, 2 + c)));
        u32x4 hv;
#pragma unroll
        for (int e = 0; e < 4; ++e) hv[e] = pack2(pk_fma(bf_2(xn[c][e]), f32x2{c1, c1}, f32x2{sa, sa}));
        *(LAS u32x4 *)(PU + c * U_CH) = hv;
      }
    }
    LAS unsigned char *WT = L + B_WT + w * 1024;
    *(LAS u32x4 *)(WT + lane * 16) = wr[0];
    u32x4 sbv = u32x4{pack_bf(u2f(sb8[0][0]), u2f(sb8[0][1])), pack_bf(u2f(sb8[0][2]), u2f(sb8[0][3])),
                      pack_bf(u2f(sb8[1][0]), u2f(sb8[1][1])), pack_bf(u2f(sb8[1][2]), u2f(sb8[1][3]))};
    f32x2 c2[4];
#pragma unroll
    for (int e = 0; e < 4; ++e) {
      const unsigned a0 = cb8[e >> 1][2 * (e & 1)], a1 = cb8[e >> 1][2 * (e & 1) + 1];
      c2[e] = bf_2(pack2(f32x2{u2f(a0), u2f(a1)} + f32x2{1.f, 1.f}));
    }
    u32x4 ones = opaque_ones();
    asm volatile("" : "+v"(sbv));
    tr.begin(lane, w, rt, (w + 1) & 3);
    BAR();

    f32x16 acc0, acc1;
    u32x4 raw[2], scl[2];
    f32x4 e1[2], e2[2];
    int ei[4];
    {
      LAS unsigned char *Al = L + ch * 4 * B_RC + (3 - (i32 & 3)) * B_RC + (2044 - 4 * (i32 >> 2) + 8 * h) * 2 - 64 * pp - 128 * 15 - 2048;
      LAS unsigned char *Ah = Al + 8;
      asm volatile("" : "+v"(Ah));
      LAS unsigned char *Bb = L + B_U + ch * U_CH + 80 * i32 + 16 * h - 80 * pp;
      LAS unsigned char *WA = WT + (8 * g + (i16 >> 2)) * 32 + 8 * (lane & 3);
      u32x2 a0l[3], a0h[3], a1l[3], a1h[3];
      u32x4 b0[3], b1[3];
#define CONV_LOAD_A(n, sl)                                                                          \
  do {                                                                                              \
    const int o_ = 128 * (15 - ((n) >> 1)) + 32 * ((n) & 1);                                        \
    a0l[sl] = *(LAS u32x2 *)(Al + o_ + 2048); a0h[sl] = *(LAS u32x2 *)(Ah + o_ + 2048);             \
  } while (0)
#define CONV_LOAD_A1(n, sl)                                                                         \
  do {                                                                                              \
    const int o_ = 128 * (15 - ((n) >> 1)) + 32 * ((n) & 1);                                        \
    a1l[sl] = *(LAS u32x2 *)(Al + o_); a1h[sl] = *(LAS u32x2 *)(Ah + o_);                           \
  } while (0)
#define CONV_LOAD_B(n, sl)                                                                          \
  do {                                                                                              \
    b0[sl] = *(LAS u32x4 *)(Bb + (31 - 2 * ((n) >> 1)) * 80 + 32 * ((n) & 1));                      \
    b1[sl] = *(LAS u32x4 *)(Bb + (63 - 2 * ((n) >> 1)) * 80 + 32 * ((n) & 1));                      \
  } while (0)
#pragma unroll
      for (int n = 0; n < 2; ++n) { CONV_LOAD_A(n, n); CONV_LOAD_B(n, n); CONV_LOAD_A1(n, n); }
      SB();
#pragma unroll
      for (int n = 0; n < 32; ++n) {
        const int c = n % 3, nx = (n + 2) % 3;
        const u32x4 A0 = cat2(a0l[c], a0h[c]), A1 = cat2(a1l[c], a1h[c]);
        if (n == 0) mma32z(acc0, A0, b0[c]); else if (n == 1) mma32n(acc0, A0, b0[c]); else mma32(acc0, A0, b0[c]);
        if (n + 2 < 32) CONV_LOAD_A(n + 2, nx);
        SB();
        if (n == 0) mma32z(acc1, A0, b1[c]); else mma32(acc1, A0, b1[c]);
        if (n + 2 < 32) CONV_LOAD_B(n + 2, nx);
        SB();
        if (n < 2) mma32n(acc1, A1, b0[c]); else mma32(acc1, A1, b0[c]);
        if (n + 2 < 32) CONV_LOAD_A1(n + 2, nx);
        if (n < 16) tr.step(n);
        if (n == 2 || n == 8) raw[n == 8] = cat2(tr_read(WA), tr_read(WA + 128));
        if (n == 5) *(LAS u32x4 *)(WT + lane * 16) = wr[1];
        if (n == 4 || n == 5 || n == 10 || n == 11) {
          const int k = n >= 10, e0 = 2 * (n & 1);
          scl[k][e0] = pack2(bf_2(raw[k][e0]) * c2[e0]);
          scl[k][e0 + 1] = pack2(bf_2(raw[k][e0 + 1]) * c2[e0 + 1]);
        }
        if (n == 6 || n == 12) { const int k = n == 12; mma16zn(e1[k], ones, scl[k]); mma16z(e2[k], sbv, raw[k]); }
        if (n == 9 || n == 15) {
          const int k = n == 15;
          asm volatile("" :: "v"(e1[k]), "v"(e2[k]));
          ei[2 * k] = (int)__builtin_rintf(e1[k][0] * 65536.f);
          ei[2 * k + 1] = (int)__builtin_rintf(e2[k][0] * 65536.f);
        }
        SB();
      }
      MFMA_FENCE();
    }
    if (pp) { const f32x16 t = acc0; acc0 = acc1; acc1 = t; }
    {
      LAS f32x4 *R = (LAS f32x4 *)(L + B_RED + (ch * 2 + (1 - pp)) * 4096) + lane;
#pragma unroll
      for (int rg = 0; rg < 4; ++rg) R[rg * 64] = f32x4{acc1[4 * rg], acc1[4 * rg + 1], acc1[4 * rg + 2], acc1[4 * rg + 3]};
    }
#pragma unroll
    for (int k = 0; k < 2; ++k) {
      const int ph = w + 4 * k;
      if (ph < 7) {
        const int F = 7 * wfg + ph;
        st_sc1(rs, lane * 16, (int)O_WP + (F * 16 + wks) * 1024, scl[k]);
        if (g == 0) {
          GAS u64 *E = (GAS u64 *)(p.ws + O_E12) + 16 * F + i16;
          __hip_atomic_fetch_add(E, (u64)(long long)ei[2 * k], RLX_AGENT);
          __hip_atomic_fetch_add(E + FF, (u64)(long long)ei[2 * k + 1], RLX_AGENT);
        }
      }
    }
    BAR();
    {
      const int d = d0 + ch;
      LAS f32x4 *R = (LAS f32x4 *)(L + B_RED + (ch * 2 + pp) * 4096) + lane;
      LAS unsigned char *U = L + B_U + ch * U_CH + (31 + 32 * pp + i32) * 80 + 8 * h;
      f32x4 o[4];
      u32x2 hv[4];
#pragma unroll
      for (int rg = 0; rg < 4; ++rg) { o[rg] = R[rg * 64]; hv[rg] = *(LAS u32x2 *)(U + 16 * rg); }
      SB();
      unsigned o0[4], o1[4];
      const f32x2 dk2 = {dsk, dsk}, ga2 = {gaw, gaw};
#pragma unroll
      for (int rg = 0; rg < 4; ++rg) {
        const f32x2 ya = lo2(o[rg]) + f32x2{acc0[4 * rg], acc0[4 * rg + 1]}, yb = hi2(o[rg]) + f32x2{acc0[4 * rg + 2], acc0[4 * rg + 3]};
        o0[rg] = pack2(pk_fma(ga2, pk_fma(bf_2(hv[rg].x), dk2, ya), bf_2(xe[rg].x)));
        o1[rg] = pack2(pk_fma(ga2, pk_fma(bf_2(hv[rg].y), dk2, yb), bf_2(xe[rg].y)));
      }
      const bool sw = (d >> 3) & 1;
      const int vo = (i32 >> 1) * 65536 + (i32 & 1) * 64 + 16 * h;
#pragma unroll
      for (int k = 0; k < 2; ++k) {
        auto s0 = __builtin_amdgcn_permlane32_swap(o0[2 * k], o0[2 * k + 1], false, false);
        auto s1 = __builtin_amdgcn_permlane32_swap(o1[2 * k], o1[2 * k + 1], false, false);
        const unsigned l0 = s0[0], h0 = s0[1], l1 = s1[0], h1 = s1[1];
        const u32x4 v = sw ? u32x4{h0, h1, l0, l1} : u32x4{l0, l1, h0, h1};
        st_sc1(rs, vo + 32 * k, (int)O_X1T + 16 * pp * 65536 + d * 128, v);
      }
    }
    SB();
    WAIT_VM(0);
    BAR();
    if (tid < 8) __hip_atomic_fetch_add(ctl + C_E2 + (tid * 8 + (b & 7)) * 32, 1u, RLX_AGENT);
    SB();
    transit_read(L, lane, w, stash, 1);
    tr.begin(lane, w, rt, (w + 2) & 3);
    tr.all();
    if (w == 0) wave_poll<true>(ctl + C_E2 + ((b & 7) * 8 + (lane & 7)) * 32, lane < 8, 32u);
    WAIT_VM(0);
    BAR();
  }

  u32x4 xres[2];
  {
    FRESH_IDS();
    const int ph2[2] = {2 * w, 2 * w + 1};
    const int ph1[2] = {w < 3 ? 2 * w : 5, w < 3 ? 2 * w + 1 : 6};
    const int Fs[4] = {56 + 7 * j + ph2[0], 56 + 7 * j + (ph2[1] < 7 ? ph2[1] : 0), 7 * j + ph1[0], 7 * j + (w == 2 ? 0 : ph1[1])};
    int wo[4];
#pragma unroll
    for (int s = 0; s < 4; ++s) wo[s] = (int)O_WP + Fs[s] * 16 * 1024 + lane * 16;
    u32x4 wf[G1_WIN][4];
    {
      u32x4 xs[16];
      const int xo = (int)O_X1T + rt * 65536 + tid * 16;
#pragma unroll
      for (int i = 0; i < 16; ++i) xs[i] = ld_sc1(rs, xo, i * 4096);
#pragma unroll
      for (int ks = 0; ks < G1_WIN; ++ks)
#pragma unroll
        for (int s = 0; s < 4; ++s) wf[ks][s] = ld_sc1(rs, wo[s], ks * 1024);
      const int tf = tid < 224 ? tid : 0, f = tf < 112 ? 112 * j + tf : FH + 112 * j + (tf - 112);
      const u32x2 ea = ld_sc1_64(rs, f * 8, (int)O_E12), eb = ld_sc1_64(rs, f * 8, (int)O_E12 + FF * 8);
      const unsigned gbv = ld_sc1_32(rs, (tid & 63) * 4, (int)O_MOD + (5 * 512 + 64 * j) * 4);
      SB();
      transit_read(L, lane, w, stash, 2);
      SB();
      const int r0 = tid >> 3, cc = tid & 7;
      LAS unsigned char *X = L + B_X + r0 * 128 + ((cc ^ (4 * ((r0 >> 1) & 1))) * 16);
#pragma unroll
      for (int i = 0; i < 16; ++i) *(LAS u32x4 *)(X + i * 4096) = xs[i];
      if (tid < 224) {
        const int ax = ea.x, ay = ea.y, bx = eb.x, by = eb.y;
        LAS float *E = (LAS float *)(L + B_E) + tid;
        E[0] = __builtin_fmaf((float)(ay + (ax < 0)), 65536.f, (float)ax * (1.f / 65536.f));
        E[224] = __builtin_fmaf((float)(by + (bx < 0)), 65536.f, (float)bx * (1.f / 65536.f));
      }
      if (tid < 64) ((LAS unsigned *)(L + B_GB))[tid] = gbv;
    }
    BAR();
    f32x4 acc[4][4], ssum[2], ssq[2];
    if (w < 2) gemm1_loop<0>(rs, L, lane, wo, wf, acc, ssum, ssq);
    else if (w == 2) gemm1_loop<2>(rs, L, lane, wo, wf, acc, ssum, ssq);
    else gemm1_loop<3>(rs, L, lane, wo, wf, acc, ssum, ssq);

    LAS float *SM = (LAS float *)(L + B_ST), *SR = SM + 64;
    asm volatile("" : "+v"(SM));
    SR = SM + 64;
    if (w >= 2 && g == (i16 >> 2)) {
#pragma unroll
      for (int k = 0; k < 2; ++k) {
        const int tau = (w == 2 ? 0 : 2) + k;
        const float mean = pick4(ssum[k], i16 & 3) * (1.f / DM);
        const float var = pick4(ssq[k], i16 & 3) * (1.f / DM) - mean * mean;
        const int s = tile_row(tau, i16);
        const float r = ln_rstd(fmaxf(var, 0.f));
        SM[s] = -rbf(mean) * r;
        SR[s] = r;
      }
    }
    {
      const int q = i16 >> 2, pq = lane & 3;
      LAS unsigned char *xb = L + B_X + (8 * g + q) * 128 + (((4 * (w >> 1) + pq) ^ (4 * (q >> 1))) * 16) + (((w & 1) ^ (g & 1)) * 8) + 2 * j * 4096;
#pragma unroll
      for (int dl = 0; dl < 2; ++dl) xres[dl] = cat2(tr_read(xb + dl * 4096), tr_read(xb + dl * 4096 + 512));
    }
    tr.begin(lane, w, rt, (w + 3) & 3);
    BAR();

    const int nz2 = w == 3 ? 1 : 2, nz1 = w == 2 ? 1 : 2;
    u32x4 zf[2][2];
    f32x4 e1z[2], e2z[2];
    float m1[4], r1[4];
    {
      LAS float *E1 = (LAS float *)(L + B_E);
      asm volatile("" : "+v"(E1));
      f32x4 m4[4], r4[4];
      float e1[2], e2[2];
#pragma unroll
      for (int t = 0; t < 4; ++t) { m4[t] = *(LAS f32x4 *)(SM + 32 * (t >> 1) + 8 * g + 4 * (t & 1)); r4[t] = *(LAS f32x4 *)(SR + 32 * (t >> 1) + 8 * g + 4 * (t & 1)); }
#pragma unroll
      for (int s = 0; s < 2; ++s) {
        const int p2 = s < nz2 ? ph2[s] : 0, p1 = s < nz1 ? ph1[s] : 0;
        e1[s] = E1[112 + 16 * p2 + i16]; e2[s] = E1[224 + 112 + 16 * p2 + i16];
        e1z[s] = *(LAS f32x4 *)(E1 + 16 * p1 + 4 * g); e2z[s] = *(LAS f32x4 *)(E1 + 224 + 16 * p1 + 4 * g);
      }
#pragma unroll
      for (int t = 0; t < 4; ++t) { const int sr = tile_row(t, i16); m1[t] = SM[sr]; r1[t] = SR[sr]; }
      SB();
      LAS unsigned char *ZW = L + B_ZT + i16 * 128 + 16 * g;
#pragma unroll
      for (int s = 0; s < 2; ++s) {
        if (s < nz2) {
          const f32x2 e12 = {e1[s], e1[s]}, e22 = {e2[s], e2[s]};
#pragma unroll
          for (int t = 0; t < 4; ++t) {
            const f32x4 a = acc[s][t];
            zf[s][t >> 1][2 * (t & 1)] = pack2(gelu2(pk_fma(lo2(a), lo2(r4[t]), pk_fma(lo2(m4[t]), e12, e22))));
            zf[s][t >> 1][2 * (t & 1) + 1] = pack2(gelu2(pk_fma(hi2(a), hi2(r4[t]), pk_fma(hi2(m4[t]), e12, e22))));
            if (s == 0) {
#pragma unroll
              for (int q = 0; q < 4; ++q) tr.step(4 * t + q);
              SB();
            }
          }
#pragma unroll
          for (int kp = 0; kp < 2; ++kp) *(LAS u32x4 *)(ZW + ph2[s] * 2048 + 64 * kp) = zf[s][kp];
        }
      }
    }
    if (tid < 128) *(LAS u32x4 *)(L + B_ZT + 112 * 128 + 16 * tid) = u32x4{0, 0, 0, 0};
    BAR();
    {
      const int q = i16 >> 2, pq = lane & 3;
      const u32x4 ones = opaque_ones();
      f32x4 su, sq;
      u32x4 za[4];
      LAS unsigned char *A = L + B_ZT + (8 * g + q) * 128 + (16 * w + 4 * pq) * 2;
#pragma unroll
      for (int st = 0; st < 4; ++st) za[st] = cat2(tr_read(A + st * 4096), tr_read(A + st * 4096 + 512));
      mma16zn(su, za[0], ones);
      mma16z(sq, za[0], za[0]);
#pragma unroll
      for (int st = 1; st < 4; ++st) { mma16(su, za[st], ones); mma16(sq, za[st], za[st]); }
      MFMA_FENCE();
      if (g == q) {
        const u32x4 gr = {f2u(pick4(su, i16 & 3)), epoch, f2u(pick4(sq, i16 & 3)), epoch};
        st_sc1(rs, (16 * w + i16) * 16, (int)O_GRAN + (rt * 8 + j) * 1024, gr);
      }
    }
    {
      LAS unsigned char *Z1 = L + B_Z1 + lane * 8;
      asm volatile("" : "+v"(Z1));
#pragma unroll
      for (int s = 0; s < 2; ++s) {
        if (s < nz1) {
          u32x2 z[4];
#pragma unroll
          for (int t = 0; t < 4; ++t) {
            const f32x2 m2 = {m1[t], m1[t]}, r2 = {r1[t], r1[t]};
            const f32x4 a = acc[2 + s][t];
            z[t] = u32x2{pack2(gelu2(pk_fma(lo2(a), r2, pk_fma(m2, lo2(e1z[s]), lo2(e2z[s]))))),
                         pack2(gelu2(pk_fma(hi2(a), r2, pk_fma(m2, hi2(e1z[s]), hi2(e2z[s])))))};
          }
#pragma unroll
          for (int t = 0; t < 4; ++t) *(LAS u32x2 *)(Z1 + ph1[s] * 2048 + t * 512) = z[t];
        }
      }
    }
    {
      float su, sq;
      for (unsigned spins = 0; spins < SPIN_CAP; ++spins) {
        u32x4 gr[8];
#pragma unroll
        for (int q = 0; q < 8; ++q) gr[q] = ld_sc1(rs, lane * 16, (int)O_GRAN + (rt * 8 + q) * 1024);
        unsigned bad = 0u;
        su = 0.f; sq = 0.f;
#pragma unroll
        for (int q = 0; q < 8; ++q) { bad |= (gr[q][1] ^ epoch) | (gr[q][3] ^ epoch); su += u2f(gr[q][0]); sq += u2f(gr[q][2]); }
        if (__all(bad == 0u)) break;
        __builtin_amdgcn_s_sleep(1);
      }
      const float mean = su * (1.f / FH), var = sq * (1.f / FH) - mean * mean;
      const float r = ln_rstd(fmaxf(var, 0.f));
      LAS float *ZR = (LAS float *)(L + B_ZS + w * 512);
      asm volatile("" : "+v"(ZR));
      ZR[lane] = r;
      ZR[64 + lane] = -rbf(mean) * r;
      f32x4 r4[4], m4[4];
#pragma unroll
      for (int t = 0; t < 4; ++t) { r4[t] = *(LAS f32x4 *)(ZR + 32 * (t >> 1) + 8 * g + 4 * (t & 1)); m4[t] = *(LAS f32x4 *)(ZR + 64 + 32 * (t >> 1) + 8 * g + 4 * (t & 1)); }
      SB();
#pragma unroll
      for (int s = 0; s < 2; ++s) {
        if (s < nz2) {
#pragma unroll
          for (int kp = 0; kp < 2; ++kp) {
            u32x4 o;
#pragma unroll
            for (int hf = 0; hf < 2; ++hf) {
              o[2 * hf] = pack2(pk_fma(bf_2(zf[s][kp][2 * hf]), lo2(r4[2 * kp + hf]), lo2(m4[2 * kp + hf])));
              o[2 * hf + 1] = pack2(pk_fma(bf_2(zf[s][kp][2 * hf + 1]), hi2(r4[2 * kp + hf]), hi2(m4[2 * kp + hf])));
            }
            st_sc1(rs, lane * 16, (int)O_Z2F + ((7 * j + ph2[s]) * 64 + 2 * rt + kp) * 1024, o);
          }
        }
      }
    }
    SB();
    WAIT_VM(0);
    BAR();
    if (tid == 0) __hip_atomic_store(ctl + C_F3 + (rt * 8 + j) * 32, epoch, RLX_AGENT);
    SB();
    transit_read(L, lane, w, stash, 3);
  }

  u32x4 wof[4][7];
  {
    FRESH_IDS();
    const float bsp = bf2f(p.b_sp[64 * rt + tile_row(w, i16)]);
    wave_poll<false>(ctl + C_F3 + ((8 * w + (lane & 7)) * 8 + j) * 32, lane < 8, epoch);
    const int ao = (int)O_Z2F + (7 * j * 64 + 16 * w) * 1024, vo = lane * 16;
    u32x4 af[G2_WIN][7];
#pragma unroll
    for (int k = 0; k < G2_WIN; ++k)
#pragma unroll
      for (int ph = 0; ph < 7; ++ph) af[k][ph] = ld_sc1(rs, vo, ao + (ph * 64 + k) * 1024);
    SB();
    f32x4 acc[7][4];
#pragma unroll
    for (int k = 0; k < 16; ++k) {
#pragma unroll
      for (int ph = 0; ph < 7; ++ph) {
        const u32x4 cur = af[k % G2_WIN][ph];
#pragma unroll
        for (int s = 0; s < 4; ++s) { if (k == 0) mma16az(acc[ph][s], cur, stash[k][s]); else mma16a(acc[ph][s], cur, stash[k][s]); }
        if (k + G2_WIN < 16) af[k % G2_WIN][ph] = ld_sc1(rs, vo, ao + (ph * 64 + k + G2_WIN) * 1024);
        SB();
      }
    }
    MFMA_FENCE();
#pragma unroll
    for (int dd = 0; dd < 4; ++dd)
#pragma unroll
      for (int k = 0; k < 7; ++k) wof[dd][k] = ld_sc1(rs, vo, (int)O_WOF + ((4 * j + dd) * 28 + 7 * w + k) * 1024);
    SB();
    BAR();
    LAS f32x4 *R = (LAS f32x4 *)L + lane;
#pragma unroll
    for (int s = 1; s < 4; ++s) {
      LAS f32x4 *Rs = R + (((s + w) & 3) * 3 + (s - 1)) * 7 * 64;
#pragma unroll
      for (int ph = 0; ph < 7; ++ph) Rs[ph * 64] = acc[ph][s];
    }
    BAR();
    unsigned vv[7][2];
    {
      const f32x2 b2 = {bsp, bsp};
      LAS f32x4 *Rw = R + w * 3 * 7 * 64;
      LAS unsigned char *Z1 = L + B_Z1 + (w * 64 + lane) * 8;
      asm volatile("" : "+v"(Z1));
      f32x4 ra[7][3];
      u32x2 z1[7];
#pragma unroll
      for (int ph = 0; ph < 7; ++ph) {
#pragma unroll
        for (int q = 0; q < 3; ++q) ra[ph][q] = Rw[(q * 7 + ph) * 64];
        z1[ph] = *(LAS u32x2 *)(Z1 + ph * 2048);
      }
      SB();
#pragma unroll
      for (int ph = 0; ph < 7; ++ph) {
        const f32x4 s = (acc[ph][0] + ra[ph][0]) + (ra[ph][1] + ra[ph][2]);
        vv[ph][0] = pack2(bf_2(z1[ph].x) * (lo2(s) + b2));
        vv[ph][1] = pack2(bf_2(z1[ph].y) * (hi2(s) + b2));
      }
    }
    const int vb = (int)O_VF + (4 * rt + w) * 28 * 1024;
    if ((j & 1) == 0) {
      const int p0 = 7 * j / 2;
#pragma unroll
      for (int k = 0; k < 3; ++k) st_sc1(rs, vo, vb + (p0 + k) * 1024, u32x4{vv[2 * k][0], vv[2 * k][1], vv[2 * k + 1][0], vv[2 * k + 1][1]});
      st_sc1_64(rs, vo, vb + (p0 + 3) * 1024, u32x2{vv[6][0], vv[6][1]});
    } else {
      const int p0 = (7 * j - 1) / 2;
      st_sc1_64(rs, vo + 8, vb + p0 * 1024, u32x2{vv[0][0], vv[0][1]});
#pragma unroll
      for (int k = 0; k < 3; ++k)
        st_sc1(rs, vo, vb + (p0 + 1 + k) * 1024, u32x4{vv[2 * k + 1][0], vv[2 * k + 1][1], vv[2 * k + 2][0], vv[2 * k + 2][1]});
    }
    SB();
    WAIT_VM(0);
    BAR();
    if (tid == 0) __hip_atomic_store(ctl + C_F4 + (rt * 8 + j) * 32, epoch, RLX_AGENT);
  }

  {
    FRESH_IDS();
    wave_poll<false>(ctl + C_F4 + (rt * 8 + 2 * w + (lane & 1)) * 32, lane < 2, epoch);
    u32x4 vf[7][4];
#pragma unroll
    for (int k = 0; k < 7; ++k)
#pragma unroll
      for (int s = 0; s < 4; ++s) vf[k][s] = ld_sc1(rs, lane * 16, (int)O_VF + ((4 * rt + ((s + w) & 3)) * 28 + 7 * w + k) * 1024);
    SB();
    f32x4 acc[4][4];
#pragma unroll
    for (int k = 0; k < 7; ++k)
#pragma unroll
      for (int s = 0; s < 4; ++s) {
#pragma unroll
        for (int dd = 0; dd < 4; ++dd) { if (k == 0) mma16Az(acc[dd][s], wof[dd][k], vf[k][s]); else mma16A(acc[dd][s], wof[dd][k], vf[k][s]); }
        SB();
      }
    MFMA_FENCE();
    LAS f32x4 *R = (LAS f32x4 *)L + lane;
#pragma unroll
    for (int s = 1; s < 4; ++s) {
      LAS f32x4 *Rs = R + (((s + w) & 3) * 3 + (s - 1)) * 4 * 64;
#pragma unroll
      for (int dd = 0; dd < 4; ++dd) Rs[dd * 64] = acc[dd][s];
    }
    BAR();
    const int t = 64 * rt + tile_row(w, i16);
    {
      LAS f32x4 *Rw = R + w * 3 * 4 * 64;
      LAS unsigned char *GB = L + B_GB + 32 * g;
      asm volatile("" : "+v"(GB));
      f32x4 ra[4][3], gb[4];
#pragma unroll
      for (int dd = 0; dd < 4; ++dd) {
#pragma unroll
        for (int q = 0; q < 3; ++q) ra[dd][q] = Rw[(q * 4 + dd) * 64];
        gb[dd] = *(LAS f32x4 *)(GB + 128 * (dd >> 1) + 16 * (dd & 1));
      }
      SB();
#pragma unroll
      for (int dl = 0; dl < 2; ++dl) {
        u32x4 o;
#pragma unroll
        for (int hf = 0; hf < 2; ++hf) {
          const int dd = 2 * dl + hf;
          const f32x4 s = (acc[dd][0] + ra[dd][0]) + (ra[dd][1] + ra[dd][2]);
          o[2 * hf] = pack2(pk_fma(lo2(gb[dd]), lo2(s), bf_2(xres[dl][2 * hf])));
          o[2 * hf + 1] = pack2(pk_fma(hi2(gb[dd]), hi2(s), bf_2(xres[dl][2 * hf + 1])));
        }
        *(GAS u32x4 *)(p.out + (size_t)t * DM + 64 * j + 32 * dl + 8 * g) = o;
      }
    }
    if (b == 0 && tid == 0) __hip_atomic_store(ctl + C_EPOCH, epoch, RLX_AGENT);
  }
}

extern "C" void kernel_launch(void *const *d_in, const int *in_sizes, int n_in, void *d_out, int out_size, void *d_ws,
                              size_t ws_size, hipStream_t stream) {
  Params p;
  p.x = (const u16 *)d_in[0];
  p.a_re = (const u16 *)d_in[1]; p.a_im = (const u16 *)d_in[2];
  p.b_re = (const u16 *)d_in[3]; p.b_im = (const u16 *)d_in[4];
  p.c_re = (const u16 *)d_in[5]; p.c_im = (const u16 *)d_in[6];
  p.log_dt = (const u16 *)d_in[7]; p.dskip = (const u16 *)d_in[8];
  p.w_in = (const u16 *)d_in[10]; p.w_sp = (const u16 *)d_in[11]; p.b_sp = (const u16 *)d_in[12];
  p.w_out = (const u16 *)d_in[13]; p.cvec = (const u16 *)d_in[14]; p.w_mod = (const u16 *)d_in[15];
  p.out = (u16 *)d_out;
  p.ws = (unsigned char *)d_ws;
  (void)hipFuncSetAttribute((const void *)kb_main, hipFuncAttributeMaxDynamicSharedMemorySize, B_LDS);
  kb_main<<<256, 256, B_LDS, stream>>>(p);
}
```

```cpp
#include <hip/hip_runtime.h>
#include <stdint.h>
#include <string.h>

#define GAS __attribute__((address_space(1)))
#define LAS __attribute__((address_space(3)))
#define CAS __attribute__((address_space(4)))
#define DEVI __device__ __forceinline__
typedef unsigned short u16;
typedef unsigned long long u64;
typedef float f32x2 __attribute__((ext_vector_type(2)));
typedef float f32x4 __attribute__((ext_vector_type(4)));
typedef float f32x16 __attribute__((ext_vector_type(16)));
typedef unsigned u32x2 __attribute__((ext_vector_type(2)));
typedef unsigned u32x4 __attribute__((ext_vector_type(4)));
typedef __bf16 bf16x2_t __attribute__((ext_vector_type(2)));
typedef GAS unsigned gu32;
typedef GAS u64 gu64;
typedef __amdgpu_buffer_rsrc_t rsrc_t;
#define RLX_AGENT __ATOMIC_RELAXED, __HIP_MEMORY_SCOPE_AGENT

constexpr int SEQ = 2048, DM = 512, NS = 128, FF = 1792, FH = 896;

constexpr size_t O_GRAN = 131072;
constexpr size_t O_E12 = O_GRAN + 32 * 8 * 64 * 16;
constexpr size_t O_MOD = 1 << 20;
constexpr size_t O_XT = 2 << 20;
constexpr size_t O_X1T = 9 << 20;
constexpr size_t O_WP = 11 << 20;
constexpr size_t O_WOF = 13 << 20;
constexpr size_t O_Z2F = 14 << 20;
constexpr size_t O_VF = 18 << 20;
constexpr int WS_USED = 22 << 20;
constexpr int C_EPOCH = 0;
constexpr int C_E1 = 1024;
constexpr int C_E2 = 4096;
constexpr int C_F3 = C_E2 + 64 * 32;
constexpr int C_F4 = C_F3 + 256 * 32;
static_assert((C_F4 + 256 * 32) * 4 <= (int)O_GRAN, "control block");

struct Params {
  const u16 *x, *a_re, *a_im, *b_re, *b_im, *c_re, *c_im, *log_dt, *dskip;
  const u16 *w_in, *w_sp, *b_sp, *w_out, *cvec, *w_mod;
  u16 *out;
  unsigned char *ws;
};

#define STAMP_DECL
#define STAMP(kid, slot) do {} while (0)
#define STAMP_FLUSH(kid, n) do {} while (0)
DEVI float u2f(unsigned u) { return __builtin_bit_cast(float, u); }
DEVI unsigned f2u(float f) { return __builtin_bit_cast(unsigned, f); }
DEVI float bf_lo(unsigned u) { return u2f(u << 16); }
DEVI float bf_hi(unsigned u) { return u2f(u & 0xffff0000u); }
DEVI f32x2 bf_2(unsigned u) { return f32x2{bf_lo(u), bf_hi(u)}; }
DEVI float bf2f(u16 h) { return u2f((unsigned)h << 16); }
DEVI unsigned pack2(f32x2 v) { return __builtin_bit_cast(unsigned, __builtin_convertvector(v, bf16x2_t)); }
DEVI unsigned pack_bf(float lo, float hi) { return pack2(f32x2{lo, hi}); }
DEVI float rbf(float f) { return bf_lo(pack_bf(f, f)); }
DEVI f32x2 pk_fma(f32x2 a, f32x2 b, f32x2 c) { return __builtin_elementwise_fma(a, b, c); }
DEVI f32x2 lo2(f32x4 v) { return f32x2{v[0], v[1]}; }
DEVI f32x2 hi2(f32x4 v) { return f32x2{v[2], v[3]}; }
template <int CTRL> DEVI float dpp_f(float x) {
  return __builtin_bit_cast(float, __builtin_amdgcn_update_dpp(0, __builtin_bit_cast(int, x), CTRL, 0xf, 0xf, true));
}
DEVI float wave_sum(float x) {
  x += dpp_f<0xB1>(x);
  x += dpp_f<0x4E>(x);
  x += dpp_f<0x141>(x);
  x += dpp_f<0x140>(x);
  auto s = __builtin_amdgcn_permlane16_swap(f2u(x), f2u(x), false, false);
  x = u2f(s[0]) + u2f(s[1]);
  auto t = __builtin_amdgcn_permlane32_swap(f2u(x), f2u(x), false, false);
  return u2f(t[0]) + u2f(t[1]);
}
DEVI u32x2 tr_read(LAS unsigned char *p) {
  typedef short v4i16_t __attribute__((ext_vector_type(4)));
  return __builtin_bit_cast(u32x2, __builtin_amdgcn_ds_read_tr16_b64_v4i16((LAS v4i16_t *)p));
}
DEVI u32x4 cat2(u32x2 a, u32x2 b) { return u32x4{a.x, a.y, b.x, b.y}; }
DEVI float ln_rstd(float var) {
  return __builtin_amdgcn_rcpf(rbf(__builtin_amdgcn_sqrtf(rbf(rbf(var) + 1e-5f))));
}
DEVI f32x2 gelu2(f32x2 x) {
  constexpr float K0 = -0.7978845608028654f * 2.0f * 1.4426950408889634f, K1 = K0 * 0.044715f;
  const f32x2 u = x * pk_fma(x * x, f32x2{K1, K1}, f32x2{K0, K0});
  const f32x2 d = f32x2{__builtin_amdgcn_exp2f(u[0]), __builtin_amdgcn_exp2f(u[1])} + f32x2{1.f, 1.f};
  return x * f32x2{__builtin_amdgcn_rcpf(d[0]), __builtin_amdgcn_rcpf(d[1])};
}
DEVI void block_rt_j(int b, int &rt, int &j) {
  const int x = b & 7, q = b >> 3;
  rt = 8 * (x >> 1) + (q & 7);
  j = 4 * (x & 1) + (q >> 3);
}
DEVI int tile_row(int tau, int i) { return 32 * (tau >> 1) + 8 * (i >> 2) + 4 * (tau & 1) + (i & 3); }
DEVI f32x2 cmul(f32x2 z, f32x2 v, f32x2 vr) { return pk_fma(f32x2{z[1], z[1]}, vr, f32x2{z[0], z[0]} * v); }
DEVI f32x2 crot(f32x2 v) { return f32x2{-v[1], v[0]}; }
DEVI f32x2 cexp_m(float lr2, float rev, float m) {
  const float er = __builtin_amdgcn_exp2f(lr2 * m);
  const float fr = __builtin_amdgcn_fractf(rev * m);
  return f32x2{er * __builtin_amdgcn_cosf(fr), er * __builtin_amdgcn_sinf(fr)};
}

#define SB() __builtin_amdgcn_sched_barrier(0)
#define WAIT_VM(n) asm volatile("s_waitcnt vmcnt(" #n ")" ::: "memory")
#define WAIT_LGKM0() asm volatile("s_waitcnt lgkmcnt(0)" ::: "memory")
#define BAR() asm volatile("s_waitcnt lgkmcnt(0)\n\ts_barrier" ::: "memory")
constexpr int AUX_SC1 = 16;
constexpr unsigned SPIN_CAP = 1u << 18;
#define POLL_SLEEP 2
#define R2_SLEEP 2

constexpr int B_RC = 4160;
constexpr int B_U = 8 * B_RC;
constexpr int U_CH = 95 * 80 + 16;
constexpr int B_RED = B_U + 2 * U_CH;
constexpr int KT_ROW = 208;
constexpr int B_KT = 4 * B_RC;
constexpr int B_KD = B_KT + 256 * KT_ROW;
constexpr int B_WT = B_KD + 4096;
constexpr int B_TR = B_WT + 4096;
constexpr int TR_WAVE = 16 * 1040;
constexpr int B_Z1 = B_TR + 4 * TR_WAVE;
constexpr int B_ST = B_Z1 + 14336;
constexpr int B_E = B_ST + 512;
constexpr int B_ZS = B_E + 2 * 224 * 4;
constexpr int B_GB = B_ZS + 2048;
constexpr int B_LDS = B_GB + 256;
constexpr int B_CS = B_Z1 + 10240;
constexpr int B_XU = B_Z1;
constexpr int B_LN = 0;
constexpr int B_WO = 16384;
constexpr int B_MW = 24576;
constexpr int B_MS = B_MW + 64 * 272;
constexpr int B_X = 0;
constexpr int B_ZT = 0;
static_assert(B_RED + 16384 <= B_WT && B_MS + 128 <= B_WT && 65536 <= B_WT && (B_KD & 15) == 0 && (B_WT & 15) == 0 &&
              4 * 3 * 7 * 1024 <= B_Z1 && B_LDS <= 163840, "lds map");
constexpr int G1_WIN = 4;
constexpr int G2_WIN = 24;

DEVI rsrc_t mk_rsrc(const void *p, int bytes) { return __builtin_amdgcn_make_buffer_rsrc((void *)p, 0, bytes, 0x00020000); }
DEVI u32x4 ld128(rsrc_t rs, int voff, int soff) { return __builtin_amdgcn_raw_buffer_load_b128(rs, voff, soff, 0); }
DEVI unsigned ld16(rsrc_t rs, int voff, int soff) { return __builtin_amdgcn_raw_buffer_load_b16(rs, voff, soff, 0); }
DEVI u32x4 ld_sc1(rsrc_t rs, int voff, int soff) { return __builtin_amdgcn_raw_buffer_load_b128(rs, voff, soff, AUX_SC1); }
DEVI u32x2 ld_sc1_64(rsrc_t rs, int voff, int soff) { return __builtin_amdgcn_raw_buffer_load_b64(rs, voff, soff, AUX_SC1); }
DEVI unsigned ld_sc1_32(rsrc_t rs, int voff, int soff) { return __builtin_amdgcn_raw_buffer_load_b32(rs, voff, soff, AUX_SC1); }
DEVI void st_sc1(rsrc_t rs, int voff, int soff, u32x4 v) { __builtin_amdgcn_raw_buffer_store_b128(v, rs, voff, soff, AUX_SC1); }
DEVI void st_sc1_64(rsrc_t rs, int voff, int soff, u32x2 v) { __builtin_amdgcn_raw_buffer_store_b64(v, rs, voff, soff, AUX_SC1); }
DEVI void st_sc1_32(rsrc_t rs, int voff, int soff, unsigned v) { __builtin_amdgcn_raw_buffer_store_b32(v, rs, voff, soff, AUX_SC1); }
DEVI float pick4(f32x4 v, int r) { return r == 0 ? v[0] : r == 1 ? v[1] : r == 2 ? v[2] : v[3]; }
template <bool GE> DEVI void wave_poll(gu32 *word, bool on, unsigned want) {
  for (unsigned spins = 0; spins < SPIN_CAP; ++spins) {
    const unsigned v = on ? __hip_atomic_load(word, RLX_AGENT) : want;
    if (__all(GE ? v >= want : v == want)) break;
    __builtin_amdgcn_s_sleep(POLL_SLEEP);
  }
  __builtin_amdgcn_fence(__ATOMIC_ACQUIRE, "wavefront");
}

DEVI void mma16(f32x4 &c, u32x4 a, u32x4 b) { asm volatile("v_mfma_f32_16x16x32_bf16 %0, %1, %2, %0" : "+v"(c) : "v"(a), "v"(b)); }
DEVI void mma16a(f32x4 &c, u32x4 a, u32x4 b) { asm volatile("v_mfma_f32_16x16x32_bf16 %0, %1, %2, %0" : "+v"(c) : "v"(a), "a"(b)); }
DEVI void mma32(f32x16 &c, u32x4 a, u32x4 b) { asm volatile("v_mfma_f32_32x32x16_bf16 %0, %1, %2, %0" : "+v"(c) : "v"(a), "v"(b)); }
DEVI void mma16z(f32x4 &c, u32x4 a, u32x4 b) { asm volatile("v_mfma_f32_16x16x32_bf16 %0, %1, %2, 0" : "=&v"(c) : "v"(a), "v"(b)); }
DEVI void mma16n(f32x4 &c, u32x4 a, u32x4 b) { asm volatile("s_nop 1\n\tv_mfma_f32_16x16x32_bf16 %0, %1, %2, %0" : "+v"(c) : "v"(a), "v"(b)); }
DEVI void mma16zn(f32x4 &c, u32x4 a, u32x4 b) { asm volatile("s_nop 1\n\tv_mfma_f32_16x16x32_bf16 %0, %1, %2, 0" : "=&v"(c) : "v"(a), "v"(b)); }
DEVI void mma16az(f32x4 &c, u32x4 a, u32x4 b) { asm volatile("v_mfma_f32_16x16x32_bf16 %0, %1, %2, 0" : "=&v"(c) : "v"(a), "a"(b)); }
DEVI void mma16A(f32x4 &c, u32x4 a, u32x4 b) { asm volatile("v_mfma_f32_16x16x32_bf16 %0, %1, %2, %0" : "+v"(c) : "a"(a), "v"(b)); }
DEVI void mma16Az(f32x4 &c, u32x4 a, u32x4 b) { asm volatile("v_mfma_f32_16x16x32_bf16 %0, %1, %2, 0" : "=&v"(c) : "a"(a), "v"(b)); }
DEVI void mma32z(f32x16 &c, u32x4 a, u32x4 b) { asm volatile("s_nop 1\n\tv_mfma_f32_32x32x16_bf16 %0, %1, %2, 0" : "=&v"(c) : "v"(a), "v"(b)); }
DEVI void mma32n(f32x16 &c, u32x4 a, u32x4 b) { asm volatile("s_nop 1\n\tv_mfma_f32_32x32x16_bf16 %0, %1, %2, %0" : "+v"(c) : "v"(a), "v"(b)); }
#define MFMA_FENCE() do { SB(); asm volatile("s_nop 7\n\ts_nop 7" ::: "memory"); SB(); } while (0)
#define FRESH_IDS()                                   \
  int tid = threadIdx.x;                              \
  asm volatile("" : "+v"(tid));                       \
  const int lane = tid & 63, i16 = lane & 15, g = lane >> 4; \
  (void)i16; (void)g
DEVI u32x4 opaque_ones() {
  u32x4 o = u32x4{0x3f803f80u, 0x3f803f80u, 0x3f803f80u, 0x3f803f80u};
  asm volatile("" : "+v"(o));
  return o;
}
struct Transit {
  rsrc_t rw;
  int v[4], so, la;
  DEVI void begin(int lane, int w, int rt, int tau) {
#pragma unroll
    for (int c = 0; c < 4; ++c) v[c] = lane * 16 + c * 4096;
    so = ((64 * rt + 32 * (tau >> 1) + 4 * (tau & 1)) * SEQ + 512 * w) * 2;
    la = B_TR + w * TR_WAVE;
  }
  DEVI void step(int i) {
    asm volatile("s_mov_b32 m0, %0\n\ts_add_u32 %0, %0, 1040\n\tbuffer_load_dwordx4 %2, %3, %1 offen lds"
                 : "+s"(la) : "s"(so), "v"(v[i & 3]), "s"(rw) : "memory", "scc");
    if ((i & 3) == 3) so += 32768;
  }
  DEVI void all() {
#pragma unroll
    for (int i = 0; i < 16; ++i) step(i);
  }
};
DEVI void transit_read(LAS unsigned char *L, int lane, int w, u32x4 (&stash)[16][4], int slot) {
  LAS unsigned char *T = L + B_TR + w * TR_WAVE + (lane & 15) * 1040 + (lane >> 4) * 16;
  asm volatile("" : "+v"(T));
#pragma unroll
  for (int k = 0; k < 16; ++k) {
    const u32x4 v = *(LAS u32x4 *)(T + 64 * k);
    if (slot == 0) stash[k][0] = v; else if (slot == 1) stash[k][1] = v; else if (slot == 2) stash[k][2] = v; else stash[k][3] = v;
  }
  WAIT_LGKM0();
}

template <int VAR>
DEVI void gemm1_loop(rsrc_t rs, LAS unsigned char *L, int lane, const int (&wo)[4], u32x4 (&wf)[G1_WIN][4], f32x4 (&acc)[4][4],
                     f32x4 (&ssum)[2], f32x4 (&ssq)[2]) {
  const int g = lane >> 4, q = (lane & 15) >> 2, pq = lane & 3;
  LAS unsigned char *xb[4];
#pragma unroll
  for (int tau = 0; tau < 4; ++tau)
    xb[tau] = L + B_X + (8 * g + q) * 128 + (((4 * (tau >> 1) + pq) ^ (4 * (q >> 1))) * 16) + (((tau & 1) ^ (g & 1)) * 8);
  constexpr bool act[4] = {true, VAR != 3, true, VAR != 2};
  const u32x4 ones = opaque_ones();
  u32x2 xl[2][4], xh[2][4];
#pragma unroll
  for (int tau = 0; tau < 4; ++tau) { xl[0][tau] = tr_read(xb[tau]); xh[0][tau] = tr_read(xb[tau] + 512); }
  SB();
#pragma unroll
  for (int ks = 0; ks < 16; ++ks) {
    const int c = ks & 1, n = c ^ 1;
    u32x4 cur[4];
#pragma unroll
    for (int s = 0; s < 4; ++s) cur[s] = wf[ks % G1_WIN][s];
#pragma unroll
    for (int tau = 0; tau < 4; ++tau) {
      const u32x4 xf = cat2(xl[c][tau], xh[c][tau]);
      if (ks == 0) mma16z(acc[0][tau], xf, cur[0]); else mma16(acc[0][tau], xf, cur[0]);
      if (ks + 1 < 16) xl[n][tau] = tr_read(xb[tau] + (ks + 1) * 4096);
      SB();
      if (act[1]) { if (ks == 0) mma16z(acc[1][tau], xf, cur[1]); else mma16(acc[1][tau], xf, cur[1]); }
      else if (tau >= 2) { if (ks == 0) mma16z(ssum[tau - 2], xf, ones); else mma16(ssum[tau - 2], xf, ones); }
      if (ks + 1 < 16) xh[n][tau] = tr_read(xb[tau] + (ks + 1) * 4096 + 512);
      SB();
      if (ks == 0) mma16z(acc[2][tau], cur[2], xf); else mma16(acc[2][tau], cur[2], xf);
      SB();
      if (act[3]) { if (ks == 0) mma16z(acc[3][tau], cur[3], xf); else mma16(acc[3][tau], cur[3], xf); }
      else if (tau < 2) { if (ks == 0) mma16z(ssum[tau], xf, ones); else mma16(ssum[tau], xf, ones); }
      if (VAR == 2 && tau < 2) { if (ks == 0) mma16z(ssq[tau], xf, xf); else mma16(ssq[tau], xf, xf); }
      if (VAR == 3 && tau >= 2) { if (ks == 0) mma16z(ssq[tau - 2], xf, xf); else mma16(ssq[tau - 2], xf, xf); }
      if (ks + G1_WIN < 16 && act[tau]) wf[ks % G1_WIN][tau] = ld_sc1(rs, wo[tau], (ks + G1_WIN) * 1024);
      SB();
    }
  }
  MFMA_FENCE();
}

__global__ __launch_bounds__(256, 1) void kb_main(Params p) {
  extern __shared__ __attribute__((aligned(16))) unsigned char smem_raw[];
  LAS unsigned char *L = (LAS unsigned char *)smem_raw;
  const int b = blockIdx.x;
  const int w = __builtin_amdgcn_readfirstlane(threadIdx.x >> 6);
  const rsrc_t rs = mk_rsrc(p.ws, WS_USED);
  gu32 *ctl = (gu32 *)p.ws;
  int rt, j;
  block_rt_j(b, rt, j);
  const int d0 = 64 * (b & 7) + 2 * (b >> 3);
  const unsigned epoch_raw = __hip_atomic_load(ctl + C_EPOCH, RLX_AGENT);
  unsigned epoch;

  u32x4 stash[16][4];
  Transit tr;
  tr.rw = mk_rsrc(p.w_sp, SEQ * SEQ * 2);

  unsigned prm[2][6], ldt[2];
  f32x2 z[2][2], zm[2], zmr[2], y[2][2], ym[2], ymr[2];
  int kidx[12];
  {
    FRESH_IDS();
    const int t0 = 8 * b;
    const rsrc_t rx = mk_rsrc(p.x, SEQ * DM * 2);
    u32x4 xr[2];
#pragma unroll
    for (int r = 0; r < 2; ++r) xr[r] = ld128(rx, lane * 16, (t0 + 2 * w + r) * (DM * 2));
    u32x4 jm[4], jo[2];
    unsigned cv = 0;
    const int mcg = b % 24, mkb = b / 24;
    if (b < 192) {
      const rsrc_t rm = mk_rsrc(p.w_mod, DM * 3072 * 2);
#pragma unroll
      for (int q = 0; q < 4; ++q) jm[q] = ld128(rm, g * (3072 * 2) + i16 * 16, ((64 * mkb + 16 * w + 4 * q) * 3072 + 128 * mcg) * 2);
      cv = ((const GAS unsigned *)p.cvec)[32 * mkb + (lane & 31)];
    }
    if (b >= 144) {
      const rsrc_t ro = mk_rsrc(p.w_out, FH * DM * 2);
      const int bb = b - 144;
#pragma unroll
      for (int q = 0; q < 2; ++q) jo[q] = ld128(ro, (tid >> 4) * (DM * 2) + (tid & 15) * 16, ((32 * (bb >> 2) + 16 * q) * DM + 128 * (bb & 3)) * 2);
    }
    {
      const int po = d0 * NS + (w & 1) * 64 + lane;
      const u16 *pa[6] = {p.a_re, p.a_im, p.b_re, p.b_im, p.c_re, p.c_im};
#pragma unroll
      for (int a = 0; a < 6; ++a)
#pragma unroll
        for (int c = 0; c < 2; ++c) prm[c][a] = ((const GAS u16 *)pa[a])[po + c * NS];
#pragma unroll
      for (int c = 0; c < 2; ++c) ldt[c] = ((const GAS u16 *)p.log_dt)[d0 + c];
    }
    SB();
#pragma unroll
    for (int e = 0; e < 12; ++e) {
      const int l = 2047 - (8 * tid + e);
      const int lr = (int)rbf((float)(e < 8 ? l : max(l, 0)));
      kidx[e] = min(lr, 384 + (lr >> 2));
    }
    f32x2 xv[2][4], xn[2][4];
#pragma unroll
    for (int r = 0; r < 2; ++r) {
#pragma unroll
      for (int e = 0; e < 4; ++e) xv[r][e] = bf_2(xr[r][e]);
      const f32x2 s2 = (xv[r][0] + xv[r][1]) + (xv[r][2] + xv[r][3]);
      const float mean = wave_sum(s2[0] + s2[1]) * (1.f / DM);
      f32x2 q2 = {0.f, 0.f};
#pragma unroll
      for (int e = 0; e < 4; ++e) { const f32x2 c = xv[r][e] - f32x2{mean, mean}; q2 = pk_fma(c, c, q2); }
      const float rsd = ln_rstd(wave_sum(q2[0] + q2[1]) * (1.f / DM)), mb = -rbf(mean) * rsd;
#pragma unroll
      for (int e = 0; e < 4; ++e) xn[r][e] = pk_fma(xv[r][e], f32x2{rsd, rsd}, f32x2{mb, mb});
    }
    {
      u32x4 a0, a1, n0, n1;
#pragma unroll
      for (int e = 0; e < 4; ++e) {
        const unsigned lo = __builtin_amdgcn_perm(xr[1][e], xr[0][e], 0x05040100u), hi = __builtin_amdgcn_perm(xr[1][e], xr[0][e], 0x07060302u);
        const unsigned nl = pack_bf(xn[0][e][0], xn[1][e][0]), nh = pack_bf(xn[0][e][1], xn[1][e][1]);
        if (e < 2) { a0[2 * e] = lo; a0[2 * e + 1] = hi; n0[2 * e] = nl; n0[2 * e + 1] = nh; }
        else { a1[2 * e - 4] = lo; a1[2 * e - 3] = hi; n1[2 * e - 4] = nl; n1[2 * e - 3] = nh; }
      }
      LAS u32x4 *B0 = (LAS u32x4 *)(L + B_LN + w * 2048) + 2 * lane;
      B0[0] = a0; B0[1] = a1; B0[512] = n0; B0[513] = n1;
    }
    if (b < 192) {
      LAS unsigned char *MW = L + B_MW + (16 * w + g) * 272 + i16 * 16;
#pragma unroll
      for (int q = 0; q < 4; ++q) *(LAS u32x4 *)(MW + q * 4 * 272) = jm[q];
      if (w == 0 && lane < 32) {
        const f32x2 c2 = bf_2(cv);
        const f32x2 e2 = f32x2{__builtin_amdgcn_exp2f(c2[0] * -1.4426950408889634f), __builtin_amdgcn_exp2f(c2[1] * -1.4426950408889634f)} + f32x2{1.f, 1.f};
        ((LAS unsigned *)(L + B_MS))[lane] = pack2(c2 * f32x2{__builtin_amdgcn_rcpf(e2[0]), __builtin_amdgcn_rcpf(e2[1])});
      }
    }
    if (b >= 144) {
#pragma unroll
      for (int q = 0; q < 2; ++q) *(LAS u32x4 *)(L + B_WO + q * 4096 + tid * 16) = jo[q];
    }
    BAR();
    {
      LAS unsigned *S = (LAS unsigned *)(L + B_LN) + ((tid & 2) ? 0 : 2048) + 2 * (tid >> 2) + (tid & 1);
      u32x4 v[4];
#pragma unroll
      for (int a = 0; a < 4; ++a) { LAS unsigned *Sa = S + a * 128; v[a] = u32x4{Sa[0], Sa[512], Sa[1024], Sa[1536]}; }
#pragma unroll
      for (int a = 0; a < 4; ++a) st_sc1(rs, tid * 16 + a * 4096, (int)O_XT + b * 16384, v[a]);
    }
    if (b < 192) {
      u32x4 sa4[2], wb[2][2];
      LAS unsigned char *Bq = L + B_MW + (8 * g + (i16 >> 2)) * 272 + (lane & 3) * 8 + 64 * w;
#pragma unroll
      for (int ks = 0; ks < 2; ++ks) {
        sa4[ks] = *(LAS u32x4 *)(L + B_MS + ks * 64 + g * 16);
#pragma unroll
        for (int ct = 0; ct < 2; ++ct) wb[ks][ct] = cat2(tr_read(Bq + ks * 32 * 272 + ct * 32), tr_read(Bq + (ks * 32 + 4) * 272 + ct * 32));
      }
      f32x4 m0, m1;
      mma16zn(m0, sa4[0], wb[0][0]);
      mma16zn(m1, sa4[0], wb[0][1]);
      mma16n(m0, sa4[1], wb[1][0]);
      mma16n(m1, sa4[1], wb[1][1]);
      MFMA_FENCE();
      asm volatile("" :: "v"(m0), "v"(m1));
      if (lane < 32) st_sc1_32(rs, lane * 4, (int)O_MOD + (mkb * 3072 + 128 * mcg + 32 * w) * 4, f2u(g == 0 ? m0[0] : m1[0]));
    }
    if (b >= 144) {
      const int bb = b - 144;
#pragma unroll
      for (int k = 0; k < 2; ++k) {
        const int dl = 2 * w + k;
        LAS unsigned char *A = L + B_WO + (4 * g + (i16 >> 2)) * 256 + (32 * (dl >> 1) + 8 * (lane & 3) + 4 * (dl & 1)) * 2;
        st_sc1(rs, lane * 16, (int)O_WOF + ((8 * (bb & 3) + dl) * 28 + (bb >> 2)) * 1024, cat2(tr_read(A), tr_read(A + 16 * 256)));
      }
    }
    if (b == 255 && tid < 64) st_sc1_32(rs, tid * 128, C_E2 * 4, 0u);
    SB();
    {
    const int part = w >> 1;
#pragma unroll
    for (int c = 0; c < 2; ++c) {
      const float dt = rbf(expf(bf_lo(ldt[c])));
      const float lr2 = dt * bf_lo(prm[c][0]) * 1.4426950408889634f, rev = dt * bf_lo(prm[c][1]) * 0.15915494309189535f;
      const f32x2 cc = {bf_lo(prm[c][4]), bf_lo(prm[c][5])};
      const f32x2 beta = cmul(f32x2{dt * bf_lo(prm[c][2]), dt * bf_lo(prm[c][3])}, cc, crot(cc));
      const f32x2 e0 = cexp_m(lr2, rev, part ? 512.f : 0.f), e1 = cexp_m(lr2, rev, part ? 64.f : 16.f), w1 = cexp_m(lr2, rev, part ? 4.f : 1.f);
      const f32x2 e1r = crot(e1);
      z[c][0] = cmul(beta, e0, crot(e0));
      z[c][1] = cmul(z[c][0], e1, e1r);
      zm[c] = cmul(e1, e1, e1r); zmr[c] = crot(zm[c]);
      y[c][0] = f32x2{1.f, 0.f};
      y[c][1] = w1;
      ym[c] = cmul(w1, w1, crot(w1)); ymr[c] = crot(ym[c]);
    }
#pragma unroll
    for (int c = 0; c < 2; ++c)
      asm volatile("" : "+v"(z[c][0]), "+v"(z[c][1]), "+v"(zm[c]), "+v"(zmr[c]), "+v"(y[c][1]), "+v"(ym[c]), "+v"(ymr[c]));
    }
    SB();
    epoch = __builtin_amdgcn_readfirstlane(epoch_raw) + 1u;
    WAIT_VM(0);
    BAR();
    if (tid < 8) __hip_atomic_store(ctl + C_E1 + tid * 256 + b, epoch, RLX_AGENT);
    SB();
  }

  {
    FRESH_IDS();
    {
      const int part = w >> 1, n = (w & 1) * 64 + lane;
      const int s0o = (64 * rt + tile_row(w, i16)) * (SEQ * 2) + (512 * w + 8 * g) * 2;
      LAS unsigned char *T0 = L + B_KT + n * (2 * KT_ROW) + part * 64, *T1 = L + B_TR + n * (2 * KT_ROW) + part * 64;
      asm volatile("" : "+v"(T0), "+v"(T1));
#pragma unroll
      for (int grp = 0; grp < 4; ++grp) {
#pragma unroll
        for (int c = 0; c < 2; ++c) {
          u32x4 re, im;
#pragma unroll
          for (int q = 0; q < 4; ++q) {
            re[q] = pack_bf(z[c][0][0], z[c][1][0]); im[q] = pack_bf(z[c][0][1], z[c][1][1]);
            z[c][0] = cmul(z[c][0], zm[c], zmr[c]); z[c][1] = cmul(z[c][1], zm[c], zmr[c]);
          }
          LAS unsigned char *T = c ? T1 : T0;
          *(LAS u32x4 *)(T + 16 * grp) = re;
          *(LAS u32x4 *)(T + 16 * grp + KT_ROW) = im;
          stash[2 * grp + c][0] = ld128(tr.rw, s0o, (2 * grp + c) * 64);
          SB();
        }
      }
#pragma unroll
      for (int grp = 0; grp < 2; ++grp) {
#pragma unroll
        for (int c = 0; c < 2; ++c) {
          u32x4 re, im;
#pragma unroll
          for (int q = 0; q < 4; ++q) {
            re[q] = pack_bf(y[c][0][0], y[c][1][0]); im[q] = pack_bf(-y[c][0][1], -y[c][1][1]);
            y[c][0] = cmul(y[c][0], ym[c], ymr[c]); y[c][1] = cmul(y[c][1], ym[c], ymr[c]);
          }
          LAS unsigned char *T = (c ? T1 : T0) + 128 - part * 32;
          *(LAS u32x4 *)(T + 16 * grp) = re;
          *(LAS u32x4 *)(T + 16 * grp + KT_ROW) = im;
          stash[8 + 2 * grp + c][0] = ld128(tr.rw, s0o, (8 + 2 * grp + c) * 64);
          SB();
        }
      }
      BAR();
      f32x4 acc[2];
      u32x4 a[2][8], bq[2][8];
#pragma unroll
      for (int c = 0; c < 2; ++c) {
        LAS unsigned char *TA = L + (c ? B_TR : B_KT) + (8 * g + (i16 >> 2)) * KT_ROW + 8 * (lane & 3) + 32 * w;
        LAS unsigned char *TB = L + (c ? B_TR : B_KT) + (8 * g + (i16 >> 2)) * KT_ROW + 8 * (lane & 3) + 128 + 32 * (w >> 1);
#pragma unroll
        for (int ks = 0; ks < 8; ++ks) {
          a[c][ks] = cat2(tr_read(TA + ks * 32 * KT_ROW), tr_read(TA + (ks * 32 + 4) * KT_ROW));
          bq[c][ks] = cat2(tr_read(TB + ks * 32 * KT_ROW), tr_read(TB + (ks * 32 + 4) * KT_ROW));
        }
      }
      SB();
#pragma unroll
      for (int q = 12; q < 14; ++q) stash[q][0] = ld128(tr.rw, s0o, q * 64);
      mma16zn(acc[0], a[0][0], bq[0][0]);
      mma16zn(acc[1], a[1][0], bq[1][0]);
#pragma unroll
      for (int ks = 1; ks < 8; ++ks) { mma16n(acc[0], a[0][ks], bq[0][ks]); mma16n(acc[1], a[1][ks], bq[1][ks]); }
#pragma unroll
      for (int q = 14; q < 16; ++q) stash[q][0] = ld128(tr.rw, s0o, q * 64);
      MFMA_FENCE();
#pragma unroll
      for (int c = 0; c < 2; ++c) {
        LAS u16 *KD = (LAS u16 *)(L + B_KD + c * 2048) + (16 * w + 4 * g) * 16 + i16;
        const unsigned k01 = pack_bf(acc[c][0], acc[c][1]), k23 = pack_bf(acc[c][2], acc[c][3]);
        KD[0] = (u16)k01; KD[16] = (u16)(k01 >> 16); KD[32] = (u16)k23; KD[48] = (u16)(k23 >> 16);
      }
      if (w == 0) {
        for (unsigned spins = 0; spins < SPIN_CAP; ++spins) {
          const u32x4 f = ld_sc1(rs, lane * 16, (C_E1 + (b & 7) * 256) * 4);
          if (__all(((f[0] ^ epoch) | (f[1] ^ epoch) | (f[2] ^ epoch) | (f[3] ^ epoch)) == 0u)) break;
          __builtin_amdgcn_s_sleep(POLL_SLEEP);
        }
        __builtin_amdgcn_fence(__ATOMIC_ACQUIRE, "wavefront");
      }
      BAR();
    }
    const int ch = w >> 1, pp = w & 1;
    const int i32 = lane & 31, h = lane >> 5;
    const int wF = b < 224 ? b >> 1 : 0, wks = 8 * (b & 1) + 2 * w;
    u32x4 xn[4];
#pragma unroll
    for (int i = 0; i < 4; ++i) xn[i] = ld_sc1(rs, (lane >> 2) * 16384 + (lane & 3) * 16, (int)O_XT + (64 * w + 16 * i) * 16384 + d0 * 32);
    float mraw = u2f(ld_sc1_32(rs, (lane & 7) * (3072 * 4) + ((lane >> 4) % 3) * 2048 + ((lane >> 3) & 1) * 4, (int)O_MOD + d0 * 4));
    float cbs[2][8];
#pragma unroll
    for (int q = 0; q < 8; ++q) {
      cbs[0][q] = u2f(ld_sc1_32(rs, lane * 4, (int)O_MOD + (q * 3072 + 4 * 512 + 32 * wks) * 4));
      cbs[1][q] = u2f(ld_sc1_32(rs, lane * 4, (int)O_MOD + (q * 3072 + 3 * 512 + 32 * wks) * 4));
    }
    const rsrc_t rin = mk_rsrc(p.w_in, DM * FF * 2);
    u32x4 wr[2];
#pragma unroll
    for (int k = 0; k < 2; ++k) wr[k] = ld128(rin, (lane >> 1) * (FF * 2) + (lane & 1) * 16, (32 * (wks + k) * FF + 16 * wF) * 2);
    SB();
    {
      unsigned v[2][12];
#pragma unroll
      for (int c = 0; c < 2; ++c)
#pragma unroll
        for (int e = 0; e < 12; ++e) v[c][e] = ((LAS u16 *)(L + B_KD + c * 2048))[kidx[e]];
      SB();
#pragma unroll
      for (int c = 0; c < 2; ++c) {
#pragma unroll
        for (int e = 8; e < 12; ++e) v[c][e] = tid == 255 ? 0u : v[c][e];
        unsigned D[6];
#pragma unroll
        for (int e = 0; e < 6; ++e) D[e] = v[c][2 * e] | (v[c][2 * e + 1] << 16);
        unsigned A[5];
#pragma unroll
        for (int e = 0; e < 5; ++e) A[e] = __builtin_amdgcn_alignbit(D[e + 1], D[e], 16);
        LAS unsigned char *R = L + c * 4 * B_RC + 16 * tid;
        *(LAS u32x4 *)(R + 0 * B_RC) = u32x4{D[0], D[1], D[2], D[3]};
        *(LAS u32x4 *)(R + 1 * B_RC) = u32x4{A[0], A[1], A[2], A[3]};
        *(LAS u32x4 *)(R + 2 * B_RC) = u32x4{D[1], D[2], D[3], D[4]};
        *(LAS u32x4 *)(R + 3 * B_RC) = u32x4{A[1], A[2], A[3], A[4]};
      }
      if (tid < 32) *(LAS u32x4 *)(L + (tid >> 2) * B_RC + 4096 + 16 * (tid & 3)) = u32x4{0, 0, 0, 0};
    }
    SB();
    const float dsk = bf2f(((const CAS u16 *)p.dskip)[d0 + ch]);
    if (tid < 155) {
      *(LAS u32x4 *)(L + B_U + 16 * tid) = u32x4{0, 0, 0, 0};
      *(LAS u32x4 *)(L + B_U + U_CH + 16 * tid) = u32x4{0, 0, 0, 0};
    }
    mraw += dpp_f<0xB1>(mraw); mraw += dpp_f<0x4E>(mraw); mraw += dpp_f<0x141>(mraw);
    const unsigned mval = f2u(rbf(mraw));
    const float gaw = u2f(__builtin_amdgcn_readlane(mval, 32 + 8 * ch));
    {
      const int c = lane & 1;
      const bool isx = lane & 2;
      const float sa0 = u2f(__builtin_amdgcn_readlane(mval, 0)), sa1 = u2f(__builtin_amdgcn_readlane(mval, 8));
      const float ca0 = u2f(__builtin_amdgcn_readlane(mval, 16)), ca1 = u2f(__builtin_amdgcn_readlane(mval, 24));
      const float sa = c ? sa1 : sa0, c1 = rbf(1.f + (c ? ca1 : ca0));
      LAS unsigned char *PU = L + (isx ? B_XU + c * 5120 : B_U + c * U_CH + 31 * 80) + (16 * w + g) * 80 + 16 * ((lane >> 2) & 3);
#pragma unroll
      for (int i = 0; i < 4; ++i) {
        u32x4 hv;
#pragma unroll
        for (int e = 0; e < 4; ++e) { const unsigned m = pack2(pk_fma(bf_2(xn[i][e]), f32x2{c1, c1}, f32x2{sa, sa})); hv[e] = isx ? xn[i][e] : m; }
        *(LAS u32x4 *)(PU + i * 320) = hv;
      }
    }
    LAS unsigned char *WT = L + B_WT + w * 1024;
    *(LAS u32x4 *)(WT + lane * 16) = wr[0];
    u32x4 sbv[2];
    f32x2 c2[2][4];
    {
      float t[2];
#pragma unroll
      for (int a = 0; a < 2; ++a) t[a] = ((cbs[a][0] + cbs[a][1]) + (cbs[a][2] + cbs[a][3])) + ((cbs[a][4] + cbs[a][5]) + (cbs[a][6] + cbs[a][7]));
      LAS u16 *CS = (LAS u16 *)(L + B_CS + w * 256);
      const unsigned pk = pack_bf(1.f + rbf(t[0]), t[1]);
      CS[lane] = (u16)pk; CS[64 + lane] = (u16)(pk >> 16);
#pragma unroll
      for (int k = 0; k < 2; ++k) {
        const u32x4 cq = *(LAS u32x4 *)(L + B_CS + w * 256 + 64 * k + 16 * g);
        sbv[k] = *(LAS u32x4 *)(L + B_CS + w * 256 + 128 + 64 * k + 16 * g);
#pragma unroll
        for (int e = 0; e < 4; ++e) c2[k][e] = bf_2(cq[e]);
      }
    }
    u32x4 ones = opaque_ones();
    asm volatile("" : "+v"(sbv[0]), "+v"(sbv[1]));
    tr.begin(lane, w, rt, (w + 1) & 3);
    BAR();

    f32x16 acc0, acc1;
    u32x4 raw[2], scl[2];
    f32x4 e1, e2;
    {
      LAS unsigned char *Al = L + ch * 4 * B_RC + (3 - (i32 & 3)) * B_RC + (2044 - 4 * (i32 >> 2) + 8 * h) * 2 - 64 * pp - 128 * 15 - 2048;
      LAS unsigned char *Ah = Al + 8;
      asm volatile("" : "+v"(Ah));
      LAS unsigned char *Bb = L + B_U + ch * U_CH + 80 * i32 + 16 * h - 80 * pp;
      LAS unsigned char *WA = WT + (8 * g + (i16 >> 2)) * 32 + 8 * (lane & 3);
      u32x2 a0l[3], a0h[3], a1l[3], a1h[3];
      u32x4 b0[3], b1[3];
#define CONV_LOAD_A(n, sl)                                                                          \
  do {                                                                                              \
    const int o_ = 128 * (15 - ((n) >> 1)) + 32 * ((n) & 1);                                        \
    a0l[sl] = *(LAS u32x2 *)(Al + o_ + 2048); a0h[sl] = *(LAS u32x2 *)(Ah + o_ + 2048);             \
  } while (0)
#define CONV_LOAD_A1(n, sl)                                                                         \
  do {                                                                                              \
    const int o_ = 128 * (15 - ((n) >> 1)) + 32 * ((n) & 1);                                        \
    a1l[sl] = *(LAS u32x2 *)(Al + o_); a1h[sl] = *(LAS u32x2 *)(Ah + o_);                           \
  } while (0)
#define CONV_LOAD_B(n, sl)                                                                          \
  do {                                                                                              \
    b0[sl] = *(LAS u32x4 *)(Bb + (31 - 2 * ((n) >> 1)) * 80 + 32 * ((n) & 1));                      \
    b1[sl] = *(LAS u32x4 *)(Bb + (63 - 2 * ((n) >> 1)) * 80 + 32 * ((n) & 1));                      \
  } while (0)
#pragma unroll
      for (int n = 0; n < 2; ++n) { CONV_LOAD_A(n, n); CONV_LOAD_B(n, n); CONV_LOAD_A1(n, n); }
      SB();
#pragma unroll
      for (int n = 0; n < 32; ++n) {
        const int c = n % 3, nx = (n + 2) % 3;
        const u32x4 A0 = cat2(a0l[c], a0h[c]), A1 = cat2(a1l[c], a1h[c]);
        if (n == 0) mma32z(acc0, A0, b0[c]); else if (n == 1) mma32n(acc0, A0, b0[c]); else mma32(acc0, A0, b0[c]);
        if (n + 2 < 32) CONV_LOAD_A(n + 2, nx);
        SB();
        if (n == 0) mma32z(acc1, A0, b1[c]); else mma32(acc1, A0, b1[c]);
        if (n + 2 < 32) CONV_LOAD_B(n + 2, nx);
        SB();
        if (n < 2) mma32n(acc1, A1, b0[c]); else mma32(acc1, A1, b0[c]);
        if (n + 2 < 32) CONV_LOAD_A1(n + 2, nx);
        if (n < 16) tr.step(n);
        if (n == 2 || n == 8) raw[n == 8] = cat2(tr_read(WA), tr_read(WA + 128));
        if (n == 5) *(LAS u32x4 *)(WT + lane * 16) = wr[1];
        if (n == 4 || n == 5 || n == 10 || n == 11) {
          const int k = n >= 10, e0 = 2 * (n & 1);
          scl[k][e0] = pack2(bf_2(raw[k][e0]) * c2[k][e0]);
          scl[k][e0 + 1] = pack2(bf_2(raw[k][e0 + 1]) * c2[k][e0 + 1]);
        }
        if (n == 6) { mma16zn(e1, ones, scl[0]); mma16z(e2, sbv[0], raw[0]); }
        if (n == 12) { mma16n(e1, ones, scl[1]); mma16(e2, sbv[1], raw[1]); }
        SB();
      }
      MFMA_FENCE();
    }
    if (pp) { const f32x16 t = acc0; acc0 = acc1; acc1 = t; }
    {
      LAS f32x4 *R = (LAS f32x4 *)(L + B_RED + (ch * 2 + (1 - pp)) * 4096) + lane;
#pragma unroll
      for (int rg = 0; rg < 4; ++rg) R[rg * 64] = f32x4{acc1[4 * rg], acc1[4 * rg + 1], acc1[4 * rg + 2], acc1[4 * rg + 3]};
    }
    asm volatile("" :: "v"(e1), "v"(e2));
    if (b < 224) {
#pragma unroll
      for (int k = 0; k < 2; ++k) st_sc1(rs, lane * 16, (int)O_WP + (wF * 16 + wks + k) * 1024, scl[k]);
    }
    if (g == 0) {
      LAS float *EP = (LAS float *)(L + B_WT + 1024 * w + 768) + i16;
      EP[0] = e1[0]; EP[16] = e2[0];
    }
    BAR();
    if (b < 224 && tid < 32) {
      LAS float *EP = (LAS float *)(L + B_WT + 768) + tid;
      st_sc1_32(rs, (tid & 15) * 4 + (tid >> 4) * (FF * 4), (int)O_E12 + ((b & 1) * 2 * FF + 16 * wF) * 4, f2u((EP[0] + EP[256]) + (EP[512] + EP[768])));
    }
    {
      const int d = d0 + ch;
      LAS f32x4 *R = (LAS f32x4 *)(L + B_RED + (ch * 2 + pp) * 4096) + lane;
      LAS unsigned char *U = L + B_U + ch * U_CH + (31 + 32 * pp + i32) * 80 + 8 * h;
      LAS unsigned char *XU = L + B_XU + ch * 5120 + (32 * pp + i32) * 80 + 8 * h;
      f32x4 o[4];
      u32x2 hv[4], xe[4];
#pragma unroll
      for (int rg = 0; rg < 4; ++rg) { o[rg] = R[rg * 64]; hv[rg] = *(LAS u32x2 *)(U + 16 * rg); xe[rg] = *(LAS u32x2 *)(XU + 16 * rg); }
      SB();
      unsigned o0[4], o1[4];
      const f32x2 dk2 = {dsk, dsk}, ga2 = {gaw, gaw};
#pragma unroll
      for (int rg = 0; rg < 4; ++rg) {
        const f32x2 ya = lo2(o[rg]) + f32x2{acc0[4 * rg], acc0[4 * rg + 1]}, yb = hi2(o[rg]) + f32x2{acc0[4 * rg + 2], acc0[4 * rg + 3]};
        o0[rg] = pack2(pk_fma(ga2, pk_fma(bf_2(hv[rg].x), dk2, ya), bf_2(xe[rg].x)));
        o1[rg] = pack2(pk_fma(ga2, pk_fma(bf_2(hv[rg].y), dk2, yb), bf_2(xe[rg].y)));
      }
      const bool sw = (d >> 3) & 1;
      const int vo = (i32 >> 1) * 65536 + (i32 & 1) * 64 + 16 * h;
#pragma unroll
      for (int k = 0; k < 2; ++k) {
        auto s0 = __builtin_amdgcn_permlane32_swap(o0[2 * k], o0[2 * k + 1], false, false);
        auto s1 = __builtin_amdgcn_permlane32_swap(o1[2 * k], o1[2 * k + 1], false, false);
        const unsigned l0 = s0[0], h0 = s0[1], l1 = s1[0], h1 = s1[1];
        const u32x4 v = sw ? u32x4{h0, h1, l0, l1} : u32x4{l0, l1, h0, h1};
        st_sc1(rs, vo + 32 * k, (int)O_X1T + 16 * pp * 65536 + d * 128, v);
      }
    }
    SB();
    WAIT_VM(0);
    BAR();
    if (tid < 8) __hip_atomic_fetch_add(ctl + C_E2 + (tid * 8 + (b & 7)) * 32, 1u, RLX_AGENT);
    SB();
    transit_read(L, lane, w, stash, 1);
    tr.begin(lane, w, rt, (w + 2) & 3);
#pragma unroll
    for (int i = 0; i < 16; ++i) { tr.step(i); if (R2_SLEEP) __builtin_amdgcn_s_sleep(R2_SLEEP); }
    if (w == 0) wave_poll<true>(ctl + C_E2 + ((b & 7) * 8 + (lane & 7)) * 32, lane < 8, 32u);
    WAIT_VM(0);
    BAR();
  }

  u32x4 xres[2];
  {
    FRESH_IDS();
    const int ph2[2] = {2 * w, 2 * w + 1};
    const int ph1[2] = {w < 3 ? 2 * w : 5, w < 3 ? 2 * w + 1 : 6};
    const int Fs[4] = {56 + 7 * j + ph2[0], 56 + 7 * j + (ph2[1] < 7 ? ph2[1] : 0), 7 * j + ph1[0], 7 * j + (w == 2 ? 0 : ph1[1])};
    int wo[4];
#pragma unroll
    for (int s = 0; s < 4; ++s) wo[s] = (int)O_WP + Fs[s] * 16 * 1024 + lane * 16;
    u32x4 wf[G1_WIN][4];
    unsigned ep[4], gbv[8];
    {
      u32x4 xs[16];
      const int xo = (int)O_X1T + rt * 65536 + tid * 16;
#pragma unroll
      for (int i = 0; i < 16; ++i) xs[i] = ld_sc1(rs, xo, i * 4096);
      SB();
      transit_read(L, lane, w, stash, 2);
      SB();
      const int r0 = tid >> 3, cc = tid & 7;
      LAS unsigned char *X = L + B_X + r0 * 128 + ((cc ^ (4 * ((r0 >> 1) & 1))) * 16);
#pragma unroll
      for (int i = 0; i < 16; ++i) {
        *(LAS u32x4 *)(X + i * 4096) = xs[i];
        wf[i >> 2][i & 3] = ld_sc1(rs, wo[i & 3], (i >> 2) * 1024);
        SB();
      }
      const int tf = tid < 224 ? tid : 0, f = tf < 112 ? 112 * j + tf : FH + 112 * j + (tf - 112);
#pragma unroll
      for (int q = 0; q < 4; ++q) ep[q] = ld_sc1_32(rs, f * 4, (int)O_E12 + q * FF * 4);
#pragma unroll
      for (int q = 0; q < 8; ++q) gbv[q] = ld_sc1_32(rs, (tid & 63) * 4, (int)O_MOD + (q * 3072 + 5 * 512 + 64 * j) * 4);
      SB();
    }
    BAR();
    f32x4 acc[4][4], ssum[2], ssq[2];
    if (w < 2) gemm1_loop<0>(rs, L, lane, wo, wf, acc, ssum, ssq);
    else if (w == 2) gemm1_loop<2>(rs, L, lane, wo, wf, acc, ssum, ssq);
    else gemm1_loop<3>(rs, L, lane, wo, wf, acc, ssum, ssq);

    if (tid < 224) {
      LAS float *E = (LAS float *)(L + B_E) + tid;
      E[0] = u2f(ep[0]) + u2f(ep[2]);
      E[224] = u2f(ep[1]) + u2f(ep[3]);
    }
    if (tid < 64)
      ((LAS float *)(L + B_GB))[tid] = rbf(((u2f(gbv[0]) + u2f(gbv[1])) + (u2f(gbv[2]) + u2f(gbv[3]))) + ((u2f(gbv[4]) + u2f(gbv[5])) + (u2f(gbv[6]) + u2f(gbv[7]))));
    LAS float *SM = (LAS float *)(L + B_ST), *SR = SM + 64;
    asm volatile("" : "+v"(SM));
    SR = SM + 64;
    if (w >= 2 && g == (i16 >> 2)) {
#pragma unroll
      for (int k = 0; k < 2; ++k) {
        const int tau = (w == 2 ? 0 : 2) + k;
        const float mean = pick4(ssum[k], i16 & 3) * (1.f / DM);
        const float var = pick4(ssq[k], i16 & 3) * (1.f / DM) - mean * mean;
        const int s = tile_row(tau, i16);
        const float r = ln_rstd(fmaxf(var, 0.f));
        SM[s] = -rbf(mean) * r;
        SR[s] = r;
      }
    }
    {
      const int q = i16 >> 2, pq = lane & 3;
      LAS unsigned char *xb = L + B_X + (8 * g + q) * 128 + (((4 * (w >> 1) + pq) ^ (4 * (q >> 1))) * 16) + (((w & 1) ^ (g & 1)) * 8) + 2 * j * 4096;
#pragma unroll
      for (int dl = 0; dl < 2; ++dl) xres[dl] = cat2(tr_read(xb + dl * 4096), tr_read(xb + dl * 4096 + 512));
    }
    tr.begin(lane, w, rt, (w + 3) & 3);
    BAR();

    const int nz2 = w == 3 ? 1 : 2, nz1 = w == 2 ? 1 : 2;
    u32x4 zf[2][2];
    f32x4 e1z[2], e2z[2];
    float m1[4], r1[4];
    {
      LAS float *E1 = (LAS float *)(L + B_E);
      asm volatile("" : "+v"(E1));
      f32x4 m4[4], r4[4];
      float e1[2], e2[2];
#pragma unroll
      for (int t = 0; t < 4; ++t) { m4[t] = *(LAS f32x4 *)(SM + 32 * (t >> 1) + 8 * g + 4 * (t & 1)); r4[t] = *(LAS f32x4 *)(SR + 32 * (t >> 1) + 8 * g + 4 * (t & 1)); }
#pragma unroll
      for (int s = 0; s < 2; ++s) {
        const int p2 = s < nz2 ? ph2[s] : 0, p1 = s < nz1 ? ph1[s] : 0;
        e1[s] = E1[112 + 16 * p2 + i16]; e2[s] = E1[224 + 112 + 16 * p2 + i16];
        e1z[s] = *(LAS f32x4 *)(E1 + 16 * p1 + 4 * g); e2z[s] = *(LAS f32x4 *)(E1 + 224 + 16 * p1 + 4 * g);
      }
#pragma unroll
      for (int t = 0; t < 4; ++t) { const int sr = tile_row(t, i16); m1[t] = SM[sr]; r1[t] = SR[sr]; }
      SB();
      LAS unsigned char *ZW = L + B_ZT + i16 * 128 + 16 * g;
#pragma unroll
      for (int s = 0; s < 2; ++s) {
        if (s < nz2) {
          const f32x2 e12 = {e1[s], e1[s]}, e22 = {e2[s], e2[s]};
#pragma unroll
          for (int t = 0; t < 4; ++t) {
            const f32x4 a = acc[s][t];
            zf[s][t >> 1][2 * (t & 1)] = pack2(gelu2(pk_fma(lo2(a), lo2(r4[t]), pk_fma(lo2(m4[t]), e12, e22))));
            zf[s][t >> 1][2 * (t & 1) + 1] = pack2(gelu2(pk_fma(hi2(a), hi2(r4[t]), pk_fma(hi2(m4[t]), e12, e22))));
            if (s == 0) { tr.step(2 * t); tr.step(2 * t + 1); SB(); }
          }
#pragma unroll
          for (int kp = 0; kp < 2; ++kp) *(LAS u32x4 *)(ZW + ph2[s] * 2048 + 64 * kp) = zf[s][kp];
        }
      }
    }
    if (tid < 128) *(LAS u32x4 *)(L + B_ZT + 112 * 128 + 16 * tid) = u32x4{0, 0, 0, 0};
    BAR();
    {
      const int q = i16 >> 2, pq = lane & 3;
      const u32x4 ones = opaque_ones();
      f32x4 su, sq;
      u32x4 za[4];
      LAS unsigned char *A = L + B_ZT + (8 * g + q) * 128 + (16 * w + 4 * pq) * 2;
#pragma unroll
      for (int st = 0; st < 4; ++st) { za[st] = cat2(tr_read(A + st * 4096), tr_read(A + st * 4096 + 512)); tr.step(8 + st); }
      mma16zn(su, za[0], ones);
      mma16z(sq, za[0], za[0]);
#pragma unroll
      for (int st = 1; st < 4; ++st) { mma16(su, za[st], ones); mma16(sq, za[st], za[st]); }
#pragma unroll
      for (int st = 12; st < 16; ++st) tr.step(st);
      MFMA_FENCE();
      if (g == q) {
        const u32x4 gr = {f2u(pick4(su, i16 & 3)), epoch, f2u(pick4(sq, i16 & 3)), epoch};
        st_sc1(rs, (16 * w + i16) * 16, (int)O_GRAN + (rt * 8 + j) * 1024, gr);
      }
    }
    {
      LAS unsigned char *Z1 = L + B_Z1 + lane * 8;
      asm volatile("" : "+v"(Z1));
#pragma unroll
      for (int s = 0; s < 2; ++s) {
        if (s < nz1) {
          u32x2 z[4];
#pragma unroll
          for (int t = 0; t < 4; ++t) {
            const f32x2 m2 = {m1[t], m1[t]}, r2 = {r1[t], r1[t]};
            const f32x4 a = acc[2 + s][t];
            z[t] = u32x2{pack2(gelu2(pk_fma(lo2(a), r2, pk_fma(m2, lo2(e1z[s]), lo2(e2z[s]))))),
                         pack2(gelu2(pk_fma(hi2(a), r2, pk_fma(m2, hi2(e1z[s]), hi2(e2z[s])))))};
          }
#pragma unroll
          for (int t = 0; t < 4; ++t) *(LAS u32x2 *)(Z1 + ph1[s] * 2048 + t * 512) = z[t];
        }
      }
    }
    {
      float su, sq;
      for (unsigned spins = 0; spins < SPIN_CAP; ++spins) {
        u32x4 gr[8];
#pragma unroll
        for (int q = 0; q < 8; ++q) gr[q] = ld_sc1(rs, lane * 16, (int)O_GRAN + (rt * 8 + q) * 1024);
        unsigned bad = 0u;
        su = 0.f; sq = 0.f;
#pragma unroll
        for (int q = 0; q < 8; ++q) { bad |= (gr[q][1] ^ epoch) | (gr[q][3] ^ epoch); su += u2f(gr[q][0]); sq += u2f(gr[q][2]); }
        if (__all(bad == 0u)) break;
        __builtin_amdgcn_s_sleep(POLL_SLEEP);
      }
      const float mean = su * (1.f / FH), var = sq * (1.f / FH) - mean * mean;
      const float r = ln_rstd(fmaxf(var, 0.f));
      LAS float *ZR = (LAS float *)(L + B_ZS + w * 512);
      asm volatile("" : "+v"(ZR));
      ZR[lane] = r;
      ZR[64 + lane] = -rbf(mean) * r;
      f32x4 r4[4], m4[4];
#pragma unroll
      for (int t = 0; t < 4; ++t) { r4[t] = *(LAS f32x4 *)(ZR + 32 * (t >> 1) + 8 * g + 4 * (t & 1)); m4[t] = *(LAS f32x4 *)(ZR + 64 + 32 * (t >> 1) + 8 * g + 4 * (t & 1)); }
      SB();
#pragma unroll
      for (int s = 0; s < 2; ++s) {
        if (s < nz2) {
#pragma unroll
          for (int kp = 0; kp < 2; ++kp) {
            u32x4 o;
#pragma unroll
            for (int hf = 0; hf < 2; ++hf) {
              o[2 * hf] = pack2(pk_fma(bf_2(zf[s][kp][2 * hf]), lo2(r4[2 * kp + hf]), lo2(m4[2 * kp + hf])));
              o[2 * hf + 1] = pack2(pk_fma(bf_2(zf[s][kp][2 * hf + 1]), hi2(r4[2 * kp + hf]), hi2(m4[2 * kp + hf])));
            }
            st_sc1(rs, lane * 16, (int)O_Z2F + ((7 * j + ph2[s]) * 64 + 2 * rt + kp) * 1024, o);
          }
        }
      }
    }
    SB();
    WAIT_VM(0);
    BAR();
    if (tid == 0) __hip_atomic_store(ctl + C_F3 + (rt * 8 + j) * 32, epoch, RLX_AGENT);
    SB();
    transit_read(L, lane, w, stash, 3);
    BAR();
  }

  u32x4 wof[4][7];
  {
    FRESH_IDS();
    const float bsp = bf2f(p.b_sp[64 * rt + tile_row(w, i16)]);
    wave_poll<false>(ctl + C_F3 + ((8 * w + (lane & 7)) * 8 + j) * 32, lane < 8, epoch);
    const int ao = (int)O_Z2F + (7 * j * 64 + 16 * w) * 1024, vo = lane * 16;
    u32x4 af[G2_WIN];
#pragma unroll
    for (int n = 0; n < G2_WIN; ++n) af[n] = ld_sc1(rs, vo, ao + ((n >> 4) * 64 + (n & 15)) * 1024);
    SB();
    f32x4 acc[2][4], keep[7];
    LAS f32x4 *R = (LAS f32x4 *)L + lane;
    LAS f32x4 *Rs[3];
#pragma unroll
    for (int s = 1; s < 4; ++s) Rs[s - 1] = R + (((s + w) & 3) * 3 + (s - 1)) * 7 * 64;
#pragma unroll
    for (int ph = 0; ph < 7; ++ph) {
#pragma unroll
      for (int k = 0; k < 16; ++k) {
        const int n = ph * 16 + k;
        const u32x4 cur = af[n % G2_WIN];
#pragma unroll
        for (int s = 0; s < 4; ++s) { if (k == 0) mma16az(acc[ph & 1][s], cur, stash[k][s]); else mma16a(acc[ph & 1][s], cur, stash[k][s]); }
        if (n + G2_WIN < 112) af[n % G2_WIN] = ld_sc1(rs, vo, ao + (((n + G2_WIN) >> 4) * 64 + ((n + G2_WIN) & 15)) * 1024);
        if (ph > 0 && k >= 3 && k < 6) Rs[k - 3][(ph - 1) * 64] = acc[(ph - 1) & 1][k - 2];
        if (ph > 0 && k == 6) keep[ph - 1] = acc[(ph - 1) & 1][0];
        if (n >= 84) {
          const int m = n - 84;
          wof[m / 7][m % 7] = ld_sc1(rs, vo, (int)O_WOF + ((4 * j + m / 7) * 28 + 7 * w + m % 7) * 1024);
        }
        SB();
      }
    }
    MFMA_FENCE();
#pragma unroll
    for (int s = 1; s < 4; ++s) Rs[s - 1][6 * 64] = acc[0][s];
    keep[6] = acc[0][0];
    SB();
    BAR();
    unsigned vv[7][2];
    {
      const f32x2 b2 = {bsp, bsp};
      LAS f32x4 *Rw = R + w * 3 * 7 * 64;
      LAS unsigned char *Z1 = L + B_Z1 + (w * 64 + lane) * 8;
      asm volatile("" : "+v"(Z1));
      f32x4 ra[7][3];
      u32x2 z1[7];
#pragma unroll
      for (int ph = 0; ph < 7; ++ph) {
#pragma unroll
        for (int q = 0; q < 3; ++q) ra[ph][q] = Rw[(q * 7 + ph) * 64];
        z1[ph] = *(LAS u32x2 *)(Z1 + ph * 2048);
      }
      SB();
#pragma unroll
      for (int ph = 0; ph < 7; ++ph) {
        const f32x4 s = (keep[ph] + ra[ph][0]) + (ra[ph][1] + ra[ph][2]);
        vv[ph][0] = pack2(bf_2(z1[ph].x) * (lo2(s) + b2));
        vv[ph][1] = pack2(bf_2(z1[ph].y) * (hi2(s) + b2));
      }
    }
    const int vb = (int)O_VF + (4 * rt + w) * 28 * 1024;
    if ((j & 1) == 0) {
      const int p0 = 7 * j / 2;
#pragma unroll
      for (int k = 0; k < 3; ++k) st_sc1(rs, vo, vb + (p0 + k) * 1024, u32x4{vv[2 * k][0], vv[2 * k][1], vv[2 * k + 1][0], vv[2 * k + 1][1]});
      st_sc1_64(rs, vo, vb + (p0 + 3) * 1024, u32x2{vv[6][0], vv[6][1]});
    } else {
      const int p0 = (7 * j - 1) / 2;
      st_sc1_64(rs, vo + 8, vb + p0 * 1024, u32x2{vv[0][0], vv[0][1]});
#pragma unroll
      for (int k = 0; k < 3; ++k)
        st_sc1(rs, vo, vb + (p0 + 1 + k) * 1024, u32x4{vv[2 * k + 1][0], vv[2 * k + 1][1], vv[2 * k + 2][0], vv[2 * k + 2][1]});
    }
    SB();
    WAIT_VM(0);
    BAR();
    if (tid == 0) __hip_atomic_store(ctl + C_F4 + (rt * 8 + j) * 32, epoch, RLX_AGENT);
  }

  {
    FRESH_IDS();
    wave_poll<false>(ctl + C_F4 + (rt * 8 + 2 * w + (lane & 1)) * 32, lane < 2, epoch);
    u32x4 vf[7][4];
#pragma unroll
    for (int k = 0; k < 7; ++k)
#pragma unroll
      for (int s = 0; s < 4; ++s) vf[k][s] = ld_sc1(rs, lane * 16, (int)O_VF + ((4 * rt + ((s + w) & 3)) * 28 + 7 * w + k) * 1024);
    SB();
    f32x4 acc[4][4];
#pragma unroll
    for (int k = 0; k < 7; ++k)
#pragma unroll
      for (int s = 0; s < 4; ++s) {
#pragma unroll
        for (int dd = 0; dd < 4; ++dd) { if (k == 0) mma16az(acc[dd][s], wof[dd][k], vf[k][s]); else mma16a(acc[dd][s], wof[dd][k], vf[k][s]); }
        SB();
      }
    MFMA_FENCE();
    LAS f32x4 *R = (LAS f32x4 *)L + lane;
#pragma unroll
    for (int s = 1; s < 4; ++s) {
      LAS f32x4 *Rs = R + (((s + w) & 3) * 3 + (s - 1)) * 4 * 64;
#pragma unroll
      for (int dd = 0; dd < 4; ++dd) Rs[dd * 64] = acc[dd][s];
    }
    BAR();
    const int t = 64 * rt + tile_row(w, i16);
    {
      LAS f32x4 *Rw = R + w * 3 * 4 * 64;
      LAS unsigned char *GB = L + B_GB + 32 * g;
      asm volatile("" : "+v"(GB));
      f32x4 ra[4][3], gb[4];
#pragma unroll
      for (int dd = 0; dd < 4; ++dd) {
#pragma unroll
        for (int q = 0; q < 3; ++q) ra[dd][q] = Rw[(q * 4 + dd) * 64];
        gb[dd] = *(LAS f32x4 *)(GB + 128 * (dd >> 1) + 16 * (dd & 1));
      }
      SB();
#pragma unroll
      for (int dl = 0; dl < 2; ++dl) {
        u32x4 o;
#pragma unroll
        for (int hf = 0; hf < 2; ++hf) {
          const int dd = 2 * dl + hf;
          const f32x4 s = (acc[dd][0] + ra[dd][0]) + (ra[dd][1] + ra[dd][2]);
          o[2 * hf] = pack2(pk_fma(lo2(gb[dd]), lo2(s), bf_2(xres[dl][2 * hf])));
          o[2 * hf + 1] = pack2(pk_fma(hi2(gb[dd]), hi2(s), bf_2(xres[dl][2 * hf + 1])));
        }
        *(GAS u32x4 *)(p.out + (size_t)t * DM + 64 * j + 32 * dl + 8 * g) = o;
      }
    }
    if (b == 0 && tid == 0) __hip_atomic_store(ctl + C_EPOCH, epoch, RLX_AGENT);
  }
}

extern "C" void kernel_launch(void *const *d_in, const int *in_sizes, int n_in, void *d_out, int out_size, void *d_ws,
                              size_t ws_size, hipStream_t stream) {
  Params p;
  p.x = (const u16 *)d_in[0];
  p.a_re = (const u16 *)d_in[1]; p.a_im = (const u16 *)d_in[2];
  p.b_re = (const u16 *)d_in[3]; p.b_im = (const u16 *)d_in[4];
  p.c_re = (const u16 *)d_in[5]; p.c_im = (const u16 *)d_in[6];
  p.log_dt = (const u16 *)d_in[7]; p.dskip = (const u16 *)d_in[8];
  p.w_in = (const u16 *)d_in[10]; p.w_sp = (const u16 *)d_in[11]; p.b_sp = (const u16 *)d_in[12];
  p.w_out = (const u16 *)d_in[13]; p.cvec = (const u16 *)d_in[14]; p.w_mod = (const u16 *)d_in[15];
  p.out = (u16 *)d_out;
  p.ws = (unsigned char *)d_ws;
  (void)hipFuncSetAttribute((const void *)kb_main, hipFuncAttributeMaxDynamicSharedMemorySize, B_LDS);
  kb_main<<<256, 256, B_LDS, stream>>>(p);
}
```

```cpp
#include <hip/hip_runtime.h>
#include <stdint.h>
#include <string.h>

#define GAS __attribute__((address_space(1)))
#define LAS __attribute__((address_space(3)))
#define CAS __attribute__((address_space(4)))
#define DEVI __device__ __forceinline__
typedef unsigned short u16;
typedef unsigned long long u64;
typedef float f32x2 __attribute__((ext_vector_type(2)));
typedef float f32x4 __attribute__((ext_vector_type(4)));
typedef float f32x16 __attribute__((ext_vector_type(16)));
typedef unsigned u32x2 __attribute__((ext_vector_type(2)));
typedef unsigned u32x4 __attribute__((ext_vector_type(4)));
typedef __bf16 bf16x2_t __attribute__((ext_vector_type(2)));
typedef GAS unsigned gu32;
typedef GAS u64 gu64;
typedef __amdgpu_buffer_rsrc_t rsrc_t;
#define RLX_AGENT __ATOMIC_RELAXED, __HIP_MEMORY_SCOPE_AGENT

constexpr int SEQ = 2048, DM = 512, NS = 128, FF = 1792, FH = 896;

constexpr size_t O_GRAN = 131072;
constexpr size_t O_E12 = O_GRAN + 32 * 8 * 64 * 16;
constexpr size_t O_MOD = 1 << 20;
constexpr size_t O_XT = 2 << 20;
constexpr size_t O_X1T = 9 << 20;
constexpr size_t O_WP = 11 << 20;
constexpr size_t O_WOF = 13 << 20;
constexpr size_t O_Z2F = 14 << 20;
constexpr size_t O_VF = 18 << 20;
constexpr size_t O_ONES = (1 << 20) + (128 << 10);
constexpr int WS_USED = 22 << 20;
constexpr int C_EPOCH = 0;
constexpr int C_E1 = 1024;
constexpr int C_E2 = 4096;
constexpr int C_F3 = C_E2 + 64 * 32;
constexpr int C_F4 = C_F3 + 256 * 32;
static_assert((C_F4 + 256 * 32) * 4 <= (int)O_GRAN, "control block");

struct Params {
  const u16 *x, *a_re, *a_im, *b_re, *b_im, *c_re, *c_im, *log_dt, *dskip;
  const u16 *w_in, *w_sp, *b_sp, *w_out, *cvec, *w_mod;
  u16 *out;
  unsigned char *ws;
};

#define STAMP_DECL
#define STAMP(kid, slot) do {} while (0)
#define STAMP_FLUSH(kid, n) do {} while (0)
DEVI float u2f(unsigned u) { return __builtin_bit_cast(float, u); }
DEVI unsigned f2u(float f) { return __builtin_bit_cast(unsigned, f); }
DEVI float bf_lo(unsigned u) { return u2f(u << 16); }
DEVI float bf_hi(unsigned u) { return u2f(u & 0xffff0000u); }
DEVI f32x2 bf_2(unsigned u) { return f32x2{bf_lo(u), bf_hi(u)}; }
DEVI float bf2f(u16 h) { return u2f((unsigned)h << 16); }
DEVI unsigned pack2(f32x2 v) { return __builtin_bit_cast(unsigned, __builtin_convertvector(v, bf16x2_t)); }
DEVI unsigned pack_bf(float lo, float hi) { return pack2(f32x2{lo, hi}); }
DEVI float rbf(float f) { return bf_lo(pack_bf(f, f)); }
DEVI f32x2 pk_fma(f32x2 a, f32x2 b, f32x2 c) { return __builtin_elementwise_fma(a, b, c); }
DEVI f32x2 lo2(f32x4 v) { return f32x2{v[0], v[1]}; }
DEVI f32x2 hi2(f32x4 v) { return f32x2{v[2], v[3]}; }
template <int CTRL> DEVI float dpp_f(float x) {
  return __builtin_bit_cast(float, __builtin_amdgcn_update_dpp(0, __builtin_bit_cast(int, x), CTRL, 0xf, 0xf, true));
}
DEVI float wave_sum(float x) {
  x += dpp_f<0xB1>(x);
  x += dpp_f<0x4E>(x);
  x += dpp_f<0x141>(x);
  x += dpp_f<0x140>(x);
  auto s = __builtin_amdgcn_permlane16_swap(f2u(x), f2u(x), false, false);
  x = u2f(s[0]) + u2f(s[1]);
  auto t = __builtin_amdgcn_permlane32_swap(f2u(x), f2u(x), false, false);
  return u2f(t[0]) + u2f(t[1]);
}
DEVI void wave_sum4(float (&x)[4]) {
#pragma unroll
  for (int i = 0; i < 4; ++i) x[i] += dpp_f<0xB1>(x[i]);
#pragma unroll
  for (int i = 0; i < 4; ++i) x[i] += dpp_f<0x4E>(x[i]);
#pragma unroll
  for (int i = 0; i < 4; ++i) x[i] += dpp_f<0x141>(x[i]);
#pragma unroll
  for (int i = 0; i < 4; ++i) x[i] += dpp_f<0x140>(x[i]);
#pragma unroll
  for (int i = 0; i < 4; ++i) { auto s = __builtin_amdgcn_permlane16_swap(f2u(x[i]), f2u(x[i]), false, false); x[i] = u2f(s[0]) + u2f(s[1]); }
#pragma unroll
  for (int i = 0; i < 4; ++i) { auto t = __builtin_amdgcn_permlane32_swap(f2u(x[i]), f2u(x[i]), false, false); x[i] = u2f(t[0]) + u2f(t[1]); }
}
DEVI u32x2 tr_read(LAS unsigned char *p) {
  typedef short v4i16_t __attribute__((ext_vector_type(4)));
  return __builtin_bit_cast(u32x2, __builtin_amdgcn_ds_read_tr16_b64_v4i16((LAS v4i16_t *)p));
}
DEVI u32x4 cat2(u32x2 a, u32x2 b) { return u32x4{a.x, a.y, b.x, b.y}; }
DEVI float ln_rstd(float var) {
  return __builtin_amdgcn_rcpf(rbf(__builtin_amdgcn_sqrtf(rbf(rbf(var) + 1e-5f))));
}
DEVI f32x2 gelu2(f32x2 x) {
  constexpr float K0 = -0.7978845608028654f * 2.0f * 1.4426950408889634f, K1 = K0 * 0.044715f;
  const f32x2 u = x * pk_fma(x * x, f32x2{K1, K1}, f32x2{K0, K0});
  const f32x2 d = f32x2{__builtin_amdgcn_exp2f(u[0]), __builtin_amdgcn_exp2f(u[1])} + f32x2{1.f, 1.f};
  return x * f32x2{__builtin_amdgcn_rcpf(d[0]), __builtin_amdgcn_rcpf(d[1])};
}
DEVI void block_rt_j(int b, int &rt, int &j) {
  const int x = b & 7, q = b >> 3;
  rt = 8 * (x >> 1) + (q & 7);
  j = 4 * (x & 1) + (q >> 3);
}
DEVI int tile_row(int tau, int i) { return 32 * (tau >> 1) + 8 * (i >> 2) + 4 * (tau & 1) + (i & 3); }
DEVI f32x2 cmul(f32x2 z, f32x2 v, f32x2 vr) { return pk_fma(f32x2{z[1], z[1]}, vr, f32x2{z[0], z[0]} * v); }
DEVI f32x2 crot(f32x2 v) { return f32x2{-v[1], v[0]}; }
DEVI f32x2 cexp_m(float lr2, float rev, float m) {
  const float er = __builtin_amdgcn_exp2f(lr2 * m);
  const float fr = __builtin_amdgcn_fractf(rev * m);
  return f32x2{er * __builtin_amdgcn_cosf(fr), er * __builtin_amdgcn_sinf(fr)};
}

#define SB() __builtin_amdgcn_sched_barrier(0)
#define WAIT_VM(n) asm volatile("s_waitcnt vmcnt(" #n ")" ::: "memory")
#define WAIT_LGKM0() asm volatile("s_waitcnt lgkmcnt(0)" ::: "memory")
#define BAR() asm volatile("s_waitcnt lgkmcnt(0)\n\ts_barrier" ::: "memory")
constexpr int AUX_SC1 = 16;
constexpr unsigned SPIN_CAP = 1u << 18;
#define POLL_SLEEP 2
#define R2_SLEEP 2

constexpr int B_RC = 4160;
constexpr int B_U = 8 * B_RC;
constexpr int U_CH = 95 * 80 + 16;
constexpr int B_RED = B_U + 2 * U_CH;
constexpr int KT_ROW = 208;
constexpr int B_KT = 4 * B_RC;
constexpr int B_KD = B_KT + 256 * KT_ROW;
constexpr int B_WT = B_KD + 4096;
constexpr int B_TR = B_WT + 4096;
constexpr int TR_WAVE = 16 * 1040;
constexpr int B_Z1 = B_TR + 4 * TR_WAVE;
constexpr int B_ST = B_Z1 + 14336;
constexpr int B_E = B_ST + 512;
constexpr int B_ZS = B_E + 2 * 224 * 4;
constexpr int B_GB = B_ZS + 2048;
constexpr int B_LDS = B_GB + 256;
constexpr int B_CS = B_Z1 + 10240;
constexpr int B_XU = B_Z1;
constexpr int B_LN = 0;
constexpr int B_WO = 16384;
constexpr int B_MW = 24576;
constexpr int B_MS = B_MW + 64 * 272;
constexpr int B_X = 0;
constexpr int B_ZT = 0;
static_assert(B_RED + 16384 <= B_WT && B_MS + 128 <= B_WT && 65536 <= B_WT && (B_KD & 15) == 0 && (B_WT & 15) == 0 &&
              4 * 3 * 7 * 1024 <= B_Z1 && B_LDS <= 163840, "lds map");
constexpr int G1_WIN = 4;
constexpr int G2_WIN = 16;

DEVI rsrc_t mk_rsrc(const void *p, int bytes) { return __builtin_amdgcn_make_buffer_rsrc((void *)p, 0, bytes, 0x00020000); }
DEVI u32x4 ld128(rsrc_t rs, int voff, int soff) { return __builtin_amdgcn_raw_buffer_load_b128(rs, voff, soff, 0); }
DEVI unsigned ld16(rsrc_t rs, int voff, int soff) { return __builtin_amdgcn_raw_buffer_load_b16(rs, voff, soff, 0); }
DEVI u32x4 ld_sc1(rsrc_t rs, int voff, int soff) { return __builtin_amdgcn_raw_buffer_load_b128(rs, voff, soff, AUX_SC1); }
DEVI u32x2 ld_sc1_64(rsrc_t rs, int voff, int soff) { return __builtin_amdgcn_raw_buffer_load_b64(rs, voff, soff, AUX_SC1); }
DEVI unsigned ld_sc1_32(rsrc_t rs, int voff, int soff) { return __builtin_amdgcn_raw_buffer_load_b32(rs, voff, soff, AUX_SC1); }
DEVI void st_sc1(rsrc_t rs, int voff, int soff, u32x4 v) { __builtin_amdgcn_raw_buffer_store_b128(v, rs, voff, soff, AUX_SC1); }
DEVI void st_sc1_64(rsrc_t rs, int voff, int soff, u32x2 v) { __builtin_amdgcn_raw_buffer_store_b64(v, rs, voff, soff, AUX_SC1); }
DEVI void st_sc1_32(rsrc_t rs, int voff, int soff, unsigned v) { __builtin_amdgcn_raw_buffer_store_b32(v, rs, voff, soff, AUX_SC1); }
DEVI float pick4(f32x4 v, int r) { return r == 0 ? v[0] : r == 1 ? v[1] : r == 2 ? v[2] : v[3]; }
template <bool GE> DEVI void wave_poll(gu32 *word, bool on, unsigned want) {
  for (unsigned spins = 0; spins < SPIN_CAP; ++spins) {
    const unsigned v = on ? __hip_atomic_load(word, RLX_AGENT) : want;
    if (__all(GE ? v >= want : v == want)) break;
    __builtin_amdgcn_s_sleep(POLL_SLEEP);
  }
  __builtin_amdgcn_fence(__ATOMIC_ACQUIRE, "wavefront");
}

DEVI void mma16(f32x4 &c, u32x4 a, u32x4 b) { asm volatile("v_mfma_f32_16x16x32_bf16 %0, %1, %2, %0" : "+v"(c) : "v"(a), "v"(b)); }
DEVI void mma16a(f32x4 &c, u32x4 a, u32x4 b) { asm volatile("v_mfma_f32_16x16x32_bf16 %0, %1, %2, %0" : "+v"(c) : "v"(a), "a"(b)); }
DEVI void mma32(f32x16 &c, u32x4 a, u32x4 b) { asm volatile("v_mfma_f32_32x32x16_bf16 %0, %1, %2, %0" : "+v"(c) : "v"(a), "v"(b)); }
DEVI void mma16z(f32x4 &c, u32x4 a, u32x4 b) { asm volatile("v_mfma_f32_16x16x32_bf16 %0, %1, %2, 0" : "=&v"(c) : "v"(a), "v"(b)); }
DEVI void mma16n(f32x4 &c, u32x4 a, u32x4 b) { asm volatile("s_nop 1\n\tv_mfma_f32_16x16x32_bf16 %0, %1, %2, %0" : "+v"(c) : "v"(a), "v"(b)); }
DEVI void mma16zn(f32x4 &c, u32x4 a, u32x4 b) { asm volatile("s_nop 1\n\tv_mfma_f32_16x16x32_bf16 %0, %1, %2, 0" : "=&v"(c) : "v"(a), "v"(b)); }
DEVI void mma16az(f32x4 &c, u32x4 a, u32x4 b) { asm volatile("v_mfma_f32_16x16x32_bf16 %0, %1, %2, 0" : "=&v"(c) : "v"(a), "a"(b)); }
DEVI void mma16A(f32x4 &c, u32x4 a, u32x4 b) { asm volatile("v_mfma_f32_16x16x32_bf16 %0, %1, %2, %0" : "+v"(c) : "a"(a), "v"(b)); }
DEVI void mma16Az(f32x4 &c, u32x4 a, u32x4 b) { asm volatile("v_mfma_f32_16x16x32_bf16 %0, %1, %2, 0" : "=&v"(c) : "a"(a), "v"(b)); }
DEVI void mma32z(f32x16 &c, u32x4 a, u32x4 b) { asm volatile("s_nop 1\n\tv_mfma_f32_32x32x16_bf16 %0, %1, %2, 0" : "=&v"(c) : "v"(a), "v"(b)); }
DEVI void mma32n(f32x16 &c, u32x4 a, u32x4 b) { asm volatile("s_nop 1\n\tv_mfma_f32_32x32x16_bf16 %0, %1, %2, %0" : "+v"(c) : "v"(a), "v"(b)); }
#define MFMA_FENCE() do { SB(); asm volatile("s_nop 7\n\ts_nop 7" ::: "memory"); SB(); } while (0)
#define FRESH_IDS()                                   \
  int tid = threadIdx.x;                              \
  asm volatile("" : "+v"(tid));                       \
  const int lane = tid & 63, i16 = lane & 15, g = lane >> 4; \
  (void)i16; (void)g
DEVI u32x4 opaque_ones() {
  u32x4 o = u32x4{0x3f803f80u, 0x3f803f80u, 0x3f803f80u, 0x3f803f80u};
  asm volatile("" : "+v"(o));
  return o;
}
struct Transit {
  rsrc_t rw;
  int v[4], so, la;
  DEVI void begin(int lane, int w, int rt, int tau) {
#pragma unroll
    for (int c = 0; c < 4; ++c) v[c] = lane * 16 + c * 4096;
    so = ((64 * rt + 32 * (tau >> 1) + 4 * (tau & 1)) * SEQ + 512 * w) * 2;
    la = B_TR + w * TR_WAVE;
  }
  DEVI void step(int i) {
    asm volatile("s_mov_b32 m0, %0\n\ts_add_u32 %0, %0, 1040\n\tbuffer_load_dwordx4 %2, %3, %1 offen lds"
                 : "+s"(la) : "s"(so), "v"(v[i & 3]), "s"(rw) : "memory", "scc");
    if ((i & 3) == 3) so += 32768;
  }
  DEVI void all() {
#pragma unroll
    for (int i = 0; i < 16; ++i) step(i);
  }
};
DEVI void transit_read(LAS unsigned char *L, int lane, int w, u32x4 (&stash)[16][4], int slot) {
  LAS unsigned char *T = L + B_TR + w * TR_WAVE + (lane & 15) * 1040 + (lane >> 4) * 16;
  asm volatile("" : "+v"(T));
#pragma unroll
  for (int k = 0; k < 16; ++k) {
    const u32x4 v = *(LAS u32x4 *)(T + 64 * k);
    if (slot == 0) stash[k][0] = v; else if (slot == 1) stash[k][1] = v; else if (slot == 2) stash[k][2] = v; else stash[k][3] = v;
  }
  WAIT_LGKM0();
}

DEVI void gemm1_loop(rsrc_t rs, LAS unsigned char *L, int lane, int w, const int (&wo)[4], u32x4 (&wf)[G1_WIN][4], f32x4 (&acc)[4][4],
                     f32x4 &ssq) {
  const int g = lane >> 4, q = (lane & 15) >> 2, pq = lane & 3;
  LAS unsigned char *xb[5];
#pragma unroll
  for (int tau = 0; tau < 5; ++tau) {
    const int t = tau < 4 ? tau : w;
    xb[tau] = L + B_X + (8 * g + q) * 128 + (((4 * (t >> 1) + pq) ^ (4 * (q >> 1))) * 16) + (((t & 1) ^ (g & 1)) * 8);
  }
  u32x2 xl[2][5], xh[2][5];
#pragma unroll
  for (int tau = 0; tau < 5; ++tau) { xl[0][tau] = tr_read(xb[tau]); xh[0][tau] = tr_read(xb[tau] + 512); }
  SB();
  asm volatile("s_nop 1");
#pragma unroll 1
  for (int trip = 0; trip < 4; ++trip) {
#pragma unroll
    for (int ks = 0; ks < 4; ++ks) {
      const int c = ks & 1, n = c ^ 1;
      int so = (4 * trip + ks + 4) * 1024;
      asm volatile("" : "+s"(so));
      u32x4 xf[4];
#pragma unroll
      for (int tau = 0; tau < 4; ++tau) xf[tau] = cat2(xl[c][tau], xh[c][tau]);
#pragma unroll
      for (int s = 0; s < 4; ++s) {
        const u32x4 cur = wf[ks][s];
#pragma unroll
        for (int tau = 0; tau < 4; ++tau) {
          if (s < 2) mma16(acc[s][tau], xf[tau], cur); else mma16(acc[s][tau], cur, xf[tau]);
          const int m = 4 * s + tau;
          if (m < 10) {
            if (m & 1) xh[n][m >> 1] = tr_read(xb[m >> 1] + (ks + 1) * 4096 + 512);
            else xl[n][m >> 1] = tr_read(xb[m >> 1] + (ks + 1) * 4096);
          }
          if (tau == 3) wf[ks][s] = ld_sc1(rs, wo[s], so);
          SB();
        }
      }
      const u32x4 xo = cat2(xl[c][4], xh[c][4]);
      mma16(ssq, xo, xo);
      SB();
    }
#pragma unroll
    for (int tau = 0; tau < 5; ++tau) xb[tau] += 4 * 4096;
  }
  MFMA_FENCE();
}

__global__ __launch_bounds__(256, 1) void kb_main(Params p) {
  extern __shared__ __attribute__((aligned(16))) unsigned char smem_raw[];
  LAS unsigned char *L = (LAS unsigned char *)smem_raw;
  const int b = blockIdx.x;
  const int w = __builtin_amdgcn_readfirstlane(threadIdx.x >> 6);
  const rsrc_t rs = mk_rsrc(p.ws, WS_USED);
  gu32 *ctl = (gu32 *)p.ws;
  int rt, j;
  block_rt_j(b, rt, j);
  const int d0 = 64 * (b & 7) + 2 * (b >> 3);
  const unsigned epoch_raw = __hip_atomic_load(ctl + C_EPOCH, RLX_AGENT);
  unsigned epoch;

  u32x4 stash[16][4];
  Transit tr;
  tr.rw = mk_rsrc(p.w_sp, SEQ * SEQ * 2);

  unsigned prm[2][6], ldt[2];
  f32x2 z[2][2], zm[2], zmr[2], y[2][2], ym[2], ymr[2];
  int kidx[12];
  {
    FRESH_IDS();
    const int t0 = 8 * b;
    const rsrc_t rx = mk_rsrc(p.x, SEQ * DM * 2);
    u32x4 xr[2];
#pragma unroll
    for (int r = 0; r < 2; ++r) xr[r] = ld128(rx, lane * 16, (t0 + 2 * w + r) * (DM * 2));
    u32x4 jm[4], jo[2];
    unsigned cv = 0;
    const int mcg = b % 24, mkb = b / 24;
    if (b < 192) {
      const rsrc_t rm = mk_rsrc(p.w_mod, DM * 3072 * 2);
#pragma unroll
      for (int q = 0; q < 4; ++q) jm[q] = ld128(rm, g * (3072 * 2) + i16 * 16, ((64 * mkb + 16 * w + 4 * q) * 3072 + 128 * mcg) * 2);
      cv = ((const GAS unsigned *)p.cvec)[32 * mkb + (lane & 31)];
    }
    if (b >= 144) {
      const rsrc_t ro = mk_rsrc(p.w_out, FH * DM * 2);
      const int bb = b - 144;
#pragma unroll
      for (int q = 0; q < 2; ++q) jo[q] = ld128(ro, (tid >> 4) * (DM * 2) + (tid & 15) * 16, ((32 * (bb >> 2) + 16 * q) * DM + 128 * (bb & 3)) * 2);
    }
    {
      const int po = d0 * NS + (w & 1) * 64 + lane;
      const u16 *pa[6] = {p.a_re, p.a_im, p.b_re, p.b_im, p.c_re, p.c_im};
#pragma unroll
      for (int a = 0; a < 6; ++a)
#pragma unroll
        for (int c = 0; c < 2; ++c) prm[c][a] = ((const GAS u16 *)pa[a])[po + c * NS];
#pragma unroll
      for (int c = 0; c < 2; ++c) ldt[c] = ((const GAS u16 *)p.log_dt)[d0 + c];
    }
    SB();
#pragma unroll
    for (int e = 0; e < 12; ++e) {
      const int l = 2047 - (8 * tid + e);
      const int lr = (int)rbf((float)(e < 8 ? l : max(l, 0)));
      kidx[e] = min(lr, 384 + (lr >> 2)) * 2;
      asm volatile("" : "+v"(kidx[e]));
    }
    SB();
    f32x2 xv[2][4], xn[2][4];
    {
      float st[4];
#pragma unroll
      for (int r = 0; r < 2; ++r) {
#pragma unroll
        for (int e = 0; e < 4; ++e) xv[r][e] = bf_2(xr[r][e]);
        const f32x2 s2 = (xv[r][0] + xv[r][1]) + (xv[r][2] + xv[r][3]);
        const f32x2 q2 = pk_fma(xv[r][0], xv[r][0], xv[r][1] * xv[r][1]) + pk_fma(xv[r][2], xv[r][2], xv[r][3] * xv[r][3]);
        st[2 * r] = s2[0] + s2[1];
        st[2 * r + 1] = q2[0] + q2[1];
      }
      wave_sum4(st);
#pragma unroll
      for (int r = 0; r < 2; ++r) {
        const float mean = st[2 * r] * (1.f / DM), var = __builtin_fmaf(st[2 * r + 1], 1.f / DM, -mean * mean);
        const float rsd = ln_rstd(fmaxf(var, 0.f)), mb = -rbf(mean) * rsd;
#pragma unroll
        for (int e = 0; e < 4; ++e) xn[r][e] = pk_fma(xv[r][e], f32x2{rsd, rsd}, f32x2{mb, mb});
      }
    }
    {
      u32x4 a0, a1, n0, n1;
#pragma unroll
      for (int e = 0; e < 4; ++e) {
        const unsigned lo = __builtin_amdgcn_perm(xr[1][e], xr[0][e], 0x05040100u), hi = __builtin_amdgcn_perm(xr[1][e], xr[0][e], 0x07060302u);
        const unsigned nl = pack_bf(xn[0][e][0], xn[1][e][0]), nh = pack_bf(xn[0][e][1], xn[1][e][1]);
        if (e < 2) { a0[2 * e] = lo; a0[2 * e + 1] = hi; n0[2 * e] = nl; n0[2 * e + 1] = nh; }
        else { a1[2 * e - 4] = lo; a1[2 * e - 3] = hi; n1[2 * e - 4] = nl; n1[2 * e - 3] = nh; }
      }
      LAS u32x4 *B0 = (LAS u32x4 *)(L + B_LN + w * 2048) + 2 * lane;
      B0[0] = a0; B0[1] = a1; B0[512] = n0; B0[513] = n1;
    }
    if (b < 192) {
      LAS unsigned char *MW = L + B_MW + (16 * w + g) * 272 + i16 * 16;
#pragma unroll
      for (int q = 0; q < 4; ++q) *(LAS u32x4 *)(MW + q * 4 * 272) = jm[q];
      if (w == 0 && lane < 32) {
        const f32x2 c2 = bf_2(cv);
        const f32x2 e2 = f32x2{__builtin_amdgcn_exp2f(c2[0] * -1.4426950408889634f), __builtin_amdgcn_exp2f(c2[1] * -1.4426950408889634f)} + f32x2{1.f, 1.f};
        ((LAS unsigned *)(L + B_MS))[lane] = pack2(c2 * f32x2{__builtin_amdgcn_rcpf(e2[0]), __builtin_amdgcn_rcpf(e2[1])});
      }
    }
    if (b >= 144) {
#pragma unroll
      for (int q = 0; q < 2; ++q) *(LAS u32x4 *)(L + B_WO + q * 4096 + tid * 16) = jo[q];
    }
    BAR();
    {
      LAS unsigned *S = (LAS unsigned *)(L + B_LN) + ((tid & 2) ? 0 : 2048) + 2 * (tid >> 2) + (tid & 1);
      u32x4 v[4];
#pragma unroll
      for (int a = 0; a < 4; ++a) { LAS unsigned *Sa = S + a * 128; v[a] = u32x4{Sa[0], Sa[512], Sa[1024], Sa[1536]}; }
#pragma unroll
      for (int a = 0; a < 4; ++a) st_sc1(rs, tid * 16 + a * 4096, (int)O_XT + b * 16384, v[a]);
    }
    if (b < 192) {
      u32x4 sa4[2], wb[2][2];
      LAS unsigned char *Bq = L + B_MW + (8 * g + (i16 >> 2)) * 272 + (lane & 3) * 8 + 64 * w;
#pragma unroll
      for (int ks = 0; ks < 2; ++ks) {
        sa4[ks] = *(LAS u32x4 *)(L + B_MS + ks * 64 + g * 16);
#pragma unroll
        for (int ct = 0; ct < 2; ++ct) wb[ks][ct] = cat2(tr_read(Bq + ks * 32 * 272 + ct * 32), tr_read(Bq + (ks * 32 + 4) * 272 + ct * 32));
      }
      f32x4 m0, m1;
      mma16zn(m0, sa4[0], wb[0][0]);
      mma16zn(m1, sa4[0], wb[0][1]);
      mma16n(m0, sa4[1], wb[1][0]);
      mma16n(m1, sa4[1], wb[1][1]);
      MFMA_FENCE();
      asm volatile("" :: "v"(m0), "v"(m1));
      if (lane < 32) st_sc1_32(rs, lane * 4, (int)O_MOD + (mkb * 3072 + 128 * mcg + 32 * w) * 4, f2u(g == 0 ? m0[0] : m1[0]));
    }
    if (b >= 144) {
      const int bb = b - 144;
#pragma unroll
      for (int k = 0; k < 2; ++k) {
        const int dl = 2 * w + k;
        LAS unsigned char *A = L + B_WO + (4 * g + (i16 >> 2)) * 256 + (32 * (dl >> 1) + 8 * (lane & 3) + 4 * (dl & 1)) * 2;
        st_sc1(rs, lane * 16, (int)O_WOF + ((8 * (bb & 3) + dl) * 28 + (bb >> 2)) * 1024, cat2(tr_read(A), tr_read(A + 16 * 256)));
      }
    }
    if (b == 255 && tid < 64) st_sc1_32(rs, tid * 128, C_E2 * 4, 0u);
    if (b == 0) {
#pragma unroll
      for (int q = 0; q < 5; ++q) st_sc1(rs, tid * 16 + q * 4096, (int)O_ONES, u32x4{0x3f803f80u, 0x3f803f80u, 0x3f803f80u, 0x3f803f80u});
    }
    SB();
    {
    const int part = w >> 1;
#pragma unroll
    for (int c = 0; c < 2; ++c) {
      const float dt = rbf(expf(bf_lo(ldt[c])));
      const float lr2 = dt * bf_lo(prm[c][0]) * 1.4426950408889634f, rev = dt * bf_lo(prm[c][1]) * 0.15915494309189535f;
      const f32x2 cc = {bf_lo(prm[c][4]), bf_lo(prm[c][5])};
      const f32x2 beta = cmul(f32x2{dt * bf_lo(prm[c][2]), dt * bf_lo(prm[c][3])}, cc, crot(cc));
      const f32x2 e0 = cexp_m(lr2, rev, part ? 512.f : 0.f), e1 = cexp_m(lr2, rev, part ? 64.f : 16.f), w1 = cexp_m(lr2, rev, part ? 4.f : 1.f);
      const f32x2 e1r = crot(e1);
      z[c][0] = cmul(beta, e0, crot(e0));
      z[c][1] = cmul(z[c][0], e1, e1r);
      zm[c] = cmul(e1, e1, e1r); zmr[c] = crot(zm[c]);
      y[c][0] = f32x2{1.f, 0.f};
      y[c][1] = w1;
      ym[c] = cmul(w1, w1, crot(w1)); ymr[c] = crot(ym[c]);
    }
#pragma unroll
    for (int c = 0; c < 2; ++c)
      asm volatile("" : "+v"(z[c][0]), "+v"(z[c][1]), "+v"(zm[c]), "+v"(zmr[c]), "+v"(y[c][1]), "+v"(ym[c]), "+v"(ymr[c]));
    }
    SB();
    epoch = __builtin_amdgcn_readfirstlane(epoch_raw) + 1u;
    WAIT_VM(0);
    BAR();
    if (tid < 8) __hip_atomic_store(ctl + C_E1 + tid * 256 + b, epoch, RLX_AGENT);
    SB();
  }

  {
    FRESH_IDS();
    {
      const int part = w >> 1, n = (w & 1) * 64 + lane;
      const int s0o = (64 * rt + tile_row(w, i16)) * (SEQ * 2) + (512 * w + 8 * g) * 2;
      LAS unsigned char *T0 = L + B_KT + n * (2 * KT_ROW) + part * 64, *T1 = L + B_TR + n * (2 * KT_ROW) + part * 64;
      asm volatile("" : "+v"(T0), "+v"(T1));
#pragma unroll
      for (int grp = 0; grp < 6; ++grp) {
        u32x4 re[2], im[2];
#pragma unroll
        for (int q = 0; q < 4; ++q) {
#pragma unroll
          for (int c = 0; c < 2; ++c) {
            if (grp < 4) {
              re[c][q] = pack_bf(z[c][0][0], z[c][1][0]); im[c][q] = pack_bf(z[c][0][1], z[c][1][1]);
              z[c][0] = cmul(z[c][0], zm[c], zmr[c]); z[c][1] = cmul(z[c][1], zm[c], zmr[c]);
            } else {
              re[c][q] = pack_bf(y[c][0][0], y[c][1][0]); im[c][q] = pack_bf(-y[c][0][1], -y[c][1][1]);
              y[c][0] = cmul(y[c][0], ym[c], ymr[c]); y[c][1] = cmul(y[c][1], ym[c], ymr[c]);
            }
          }
        }
#pragma unroll
        for (int c = 0; c < 2; ++c) {
          LAS unsigned char *T = (c ? T1 : T0) + (grp < 4 ? 16 * grp : 128 - part * 32 + 16 * (grp - 4));
          *(LAS u32x4 *)T = re[c];
          *(LAS u32x4 *)(T + KT_ROW) = im[c];
        }
        stash[2 * grp][0] = ld128(tr.rw, s0o, (2 * grp) * 64);
        stash[2 * grp + 1][0] = ld128(tr.rw, s0o, (2 * grp + 1) * 64);
        SB();
      }
      BAR();
      f32x4 acc[2];
      u32x4 a[2][8], bq[2][8];
#pragma unroll
      for (int c = 0; c < 2; ++c) {
        LAS unsigned char *TA = L + (c ? B_TR : B_KT) + (8 * g + (i16 >> 2)) * KT_ROW + 8 * (lane & 3) + 32 * w;
        LAS unsigned char *TB = L + (c ? B_TR : B_KT) + (8 * g + (i16 >> 2)) * KT_ROW + 8 * (lane & 3) + 128 + 32 * (w >> 1);
#pragma unroll
        for (int ks = 0; ks < 8; ++ks) {
          a[c][ks] = cat2(tr_read(TA + ks * 32 * KT_ROW), tr_read(TA + (ks * 32 + 4) * KT_ROW));
          bq[c][ks] = cat2(tr_read(TB + ks * 32 * KT_ROW), tr_read(TB + (ks * 32 + 4) * KT_ROW));
        }
      }
      SB();
#pragma unroll
      for (int q = 12; q < 14; ++q) stash[q][0] = ld128(tr.rw, s0o, q * 64);
      mma16zn(acc[0], a[0][0], bq[0][0]);
      mma16zn(acc[1], a[1][0], bq[1][0]);
#pragma unroll
      for (int ks = 1; ks < 8; ++ks) { mma16(acc[0], a[0][ks], bq[0][ks]); mma16(acc[1], a[1][ks], bq[1][ks]); }
#pragma unroll
      for (int q = 14; q < 16; ++q) stash[q][0] = ld128(tr.rw, s0o, q * 64);
      MFMA_FENCE();
#pragma unroll
      for (int c = 0; c < 2; ++c) {
        LAS u16 *KD = (LAS u16 *)(L + B_KD + c * 2048) + (16 * w + 4 * g) * 16 + i16;
        const unsigned k01 = pack_bf(acc[c][0], acc[c][1]), k23 = pack_bf(acc[c][2], acc[c][3]);
        KD[0] = (u16)k01; KD[16] = (u16)(k01 >> 16); KD[32] = (u16)k23; KD[48] = (u16)(k23 >> 16);
      }
      if (w == 0) {
        for (unsigned spins = 0; spins < SPIN_CAP; ++spins) {
          const u32x4 f = ld_sc1(rs, lane * 16, (C_E1 + (b & 7) * 256) * 4);
          if (__all(((f[0] ^ epoch) | (f[1] ^ epoch) | (f[2] ^ epoch) | (f[3] ^ epoch)) == 0u)) break;
          __builtin_amdgcn_s_sleep(POLL_SLEEP);
        }
        __builtin_amdgcn_fence(__ATOMIC_ACQUIRE, "wavefront");
      }
      BAR();
    }
    const int ch = w >> 1, pp = w & 1;
    const int i32 = lane & 31, h = lane >> 5;
    const int wF = b < 224 ? b >> 1 : 0, wks = 8 * (b & 1) + 2 * w;
    u32x4 xn[4];
#pragma unroll
    for (int i = 0; i < 4; ++i) xn[i] = ld_sc1(rs, (lane >> 2) * 16384 + (lane & 3) * 16, (int)O_XT + (64 * w + 16 * i) * 16384 + d0 * 32);
    float mraw = u2f(ld_sc1_32(rs, (lane & 7) * (3072 * 4) + ((lane >> 4) % 3) * 2048 + ((lane >> 3) & 1) * 4, (int)O_MOD + d0 * 4));
    float cbs[2][8];
#pragma unroll
    for (int q = 0; q < 8; ++q) {
      cbs[0][q] = u2f(ld_sc1_32(rs, lane * 4, (int)O_MOD + (q * 3072 + 4 * 512 + 32 * wks) * 4));
      cbs[1][q] = u2f(ld_sc1_32(rs, lane * 4, (int)O_MOD + (q * 3072 + 3 * 512 + 32 * wks) * 4));
    }
    const rsrc_t rin = mk_rsrc(p.w_in, DM * FF * 2);
    u32x4 wr[2];
#pragma unroll
    for (int k = 0; k < 2; ++k) wr[k] = ld128(rin, (lane >> 1) * (FF * 2) + (lane & 1) * 16, (32 * (wks + k) * FF + 16 * wF) * 2);
    SB();
    {
      unsigned v[2][12];
#pragma unroll
      for (int c = 0; c < 2; ++c)
#pragma unroll
        for (int e = 0; e < 12; ++e) v[c][e] = *(LAS u16 *)(L + B_KD + c * 2048 + kidx[e]);
      SB();
#pragma unroll
      for (int c = 0; c < 2; ++c) {
#pragma unroll
        for (int e = 8; e < 12; ++e) v[c][e] = tid == 255 ? 0u : v[c][e];
        unsigned D[6];
#pragma unroll
        for (int e = 0; e < 6; ++e) D[e] = v[c][2 * e] | (v[c][2 * e + 1] << 16);
        unsigned A[5];
#pragma unroll
        for (int e = 0; e < 5; ++e) A[e] = __builtin_amdgcn_alignbit(D[e + 1], D[e], 16);
        LAS unsigned char *R = L + c * 4 * B_RC + 16 * tid;
        *(LAS u32x4 *)(R + 0 * B_RC) = u32x4{D[0], D[1], D[2], D[3]};
        *(LAS u32x4 *)(R + 1 * B_RC) = u32x4{A[0], A[1], A[2], A[3]};
        *(LAS u32x4 *)(R + 2 * B_RC) = u32x4{D[1], D[2], D[3], D[4]};
        *(LAS u32x4 *)(R + 3 * B_RC) = u32x4{A[1], A[2], A[3], A[4]};
      }
      if (tid < 32) *(LAS u32x4 *)(L + (tid >> 2) * B_RC + 4096 + 16 * (tid & 3)) = u32x4{0, 0, 0, 0};
    }
    SB();
    const float dsk = bf2f(((const CAS u16 *)p.dskip)[d0 + ch]);
    if (tid < 155) {
      *(LAS u32x4 *)(L + B_U + 16 * tid) = u32x4{0, 0, 0, 0};
      *(LAS u32x4 *)(L + B_U + U_CH + 16 * tid) = u32x4{0, 0, 0, 0};
    }
    mraw += dpp_f<0xB1>(mraw); mraw += dpp_f<0x4E>(mraw); mraw += dpp_f<0x141>(mraw);
    const unsigned mval = f2u(rbf(mraw));
    const float gaw = u2f(__builtin_amdgcn_readlane(mval, 32 + 8 * ch));
    {
      const int c = lane & 1;
      const bool isx = lane & 2;
      const float sa0 = u2f(__builtin_amdgcn_readlane(mval, 0)), sa1 = u2f(__builtin_amdgcn_readlane(mval, 8));
      const float ca0 = u2f(__builtin_amdgcn_readlane(mval, 16)), ca1 = u2f(__builtin_amdgcn_readlane(mval, 24));
      const float sa = c ? sa1 : sa0, c1 = rbf(1.f + (c ? ca1 : ca0));
      LAS unsigned char *PU = L + (isx ? B_XU + c * 5120 : B_U + c * U_CH + 31 * 80) + (16 * w + g) * 80 + 16 * ((lane >> 2) & 3);
#pragma unroll
      for (int i = 0; i < 4; ++i) {
        u32x4 hv;
#pragma unroll
        for (int e = 0; e < 4; ++e) { const unsigned m = pack2(pk_fma(bf_2(xn[i][e]), f32x2{c1, c1}, f32x2{sa, sa})); hv[e] = isx ? xn[i][e] : m; }
        *(LAS u32x4 *)(PU + i * 320) = hv;
      }
    }
    LAS unsigned char *WT = L + B_WT + w * 1024;
    *(LAS u32x4 *)(WT + lane * 16) = wr[0];
    u32x4 sbv[2];
    f32x2 c2[2][4];
    {
      float t[2];
#pragma unroll
      for (int a = 0; a < 2; ++a) t[a] = ((cbs[a][0] + cbs[a][1]) + (cbs[a][2] + cbs[a][3])) + ((cbs[a][4] + cbs[a][5]) + (cbs[a][6] + cbs[a][7]));
      LAS u16 *CS = (LAS u16 *)(L + B_CS + w * 256);
      const unsigned pk = pack_bf(1.f + rbf(t[0]), t[1]);
      CS[lane] = (u16)pk; CS[64 + lane] = (u16)(pk >> 16);
#pragma unroll
      for (int k = 0; k < 2; ++k) {
        const u32x4 cq = *(LAS u32x4 *)(L + B_CS + w * 256 + 64 * k + 16 * g);
        sbv[k] = *(LAS u32x4 *)(L + B_CS + w * 256 + 128 + 64 * k + 16 * g);
#pragma unroll
        for (int e = 0; e < 4; ++e) c2[k][e] = bf_2(cq[e]);
      }
    }
    u32x4 ones = opaque_ones();
    asm volatile("" : "+v"(sbv[0]), "+v"(sbv[1]));
    tr.begin(lane, w, rt, (w + 1) & 3);
    BAR();

    f32x16 acc0, acc1;
    u32x4 raw[2], scl[2];
    f32x4 e1, e2;
    {
      LAS unsigned char *Al = L + ch * 4 * B_RC + (3 - (i32 & 3)) * B_RC + (2044 - 4 * (i32 >> 2) + 8 * h) * 2 - 64 * pp - 128 * 15 - 2048;
      LAS unsigned char *Ah = Al + 8;
      asm volatile("" : "+v"(Ah));
      LAS unsigned char *Bb = L + B_U + ch * U_CH + 80 * i32 + 16 * h - 80 * pp;
      LAS unsigned char *WA = WT + (8 * g + (i16 >> 2)) * 32 + 8 * (lane & 3);
      u32x2 a0l[3], a0h[3], a1l[3], a1h[3];
      u32x4 b0[3], b1[3];
#define CONV_LOAD_A(n, sl)                                                                          \
  do {                                                                                              \
    const int o_ = 128 * (15 - ((n) >> 1)) + 32 * ((n) & 1);                                        \
    a0l[sl] = *(LAS u32x2 *)(Al + o_ + 2048); a0h[sl] = *(LAS u32x2 *)(Ah + o_ + 2048);             \
  } while (0)
#define CONV_LOAD_A1(n, sl)                                                                         \
  do {                                                                                              \
    const int o_ = 128 * (15 - ((n) >> 1)) + 32 * ((n) & 1);                                        \
    a1l[sl] = *(LAS u32x2 *)(Al + o_); a1h[sl] = *(LAS u32x2 *)(Ah + o_);                           \
  } while (0)
#define CONV_LOAD_B(n, sl)                                                                          \
  do {                                                                                              \
    b0[sl] = *(LAS u32x4 *)(Bb + (31 - 2 * ((n) >> 1)) * 80 + 32 * ((n) & 1));                      \
    b1[sl] = *(LAS u32x4 *)(Bb + (63 - 2 * ((n) >> 1)) * 80 + 32 * ((n) & 1));                      \
  } while (0)
#pragma unroll
      for (int n = 0; n < 2; ++n) { CONV_LOAD_A(n, n); CONV_LOAD_B(n, n); CONV_LOAD_A1(n, n); }
      SB();
#pragma unroll
      for (int n = 0; n < 32; ++n) {
        const int c = n % 3, nx = (n + 2) % 3;
        const u32x4 A0 = cat2(a0l[c], a0h[c]), A1 = cat2(a1l[c], a1h[c]);
        if (n == 0) mma32z(acc0, A0, b0[c]); else if (n == 1) mma32n(acc0, A0, b0[c]); else mma32(acc0, A0, b0[c]);
        if (n + 2 < 32) CONV_LOAD_A(n + 2, nx);
        SB();
        if (n == 0) mma32z(acc1, A0, b1[c]); else mma32(acc1, A0, b1[c]);
        if (n + 2 < 32) CONV_LOAD_B(n + 2, nx);
        SB();
        if (n < 2) mma32n(acc1, A1, b0[c]); else mma32(acc1, A1, b0[c]);
        if (n + 2 < 32) CONV_LOAD_A1(n + 2, nx);
        if (n < 16) tr.step(n);
        if (n == 2 || n == 8) raw[n == 8] = cat2(tr_read(WA), tr_read(WA + 128));
        if (n == 5) *(LAS u32x4 *)(WT + lane * 16) = wr[1];
        if (n == 4 || n == 5 || n == 10 || n == 11) {
          const int k = n >= 10, e0 = 2 * (n & 1);
          scl[k][e0] = pack2(bf_2(raw[k][e0]) * c2[k][e0]);
          scl[k][e0 + 1] = pack2(bf_2(raw[k][e0 + 1]) * c2[k][e0 + 1]);
        }
        if (n == 6) { mma16zn(e1, ones, scl[0]); mma16z(e2, sbv[0], raw[0]); }
        if (n == 12) { mma16n(e1, ones, scl[1]); mma16(e2, sbv[1], raw[1]); }
        SB();
      }
      MFMA_FENCE();
    }
    if (pp) { const f32x16 t = acc0; acc0 = acc1; acc1 = t; }
    {
      LAS f32x4 *R = (LAS f32x4 *)(L + B_RED + (ch * 2 + (1 - pp)) * 4096) + lane;
#pragma unroll
      for (int rg = 0; rg < 4; ++rg) R[rg * 64] = f32x4{acc1[4 * rg], acc1[4 * rg + 1], acc1[4 * rg + 2], acc1[4 * rg + 3]};
    }
    asm volatile("" :: "v"(e1), "v"(e2));
    if (b < 224) {
#pragma unroll
      for (int k = 0; k < 2; ++k) st_sc1(rs, lane * 16, (int)O_WP + (wF * 16 + wks + k) * 1024, scl[k]);
    }
    if (g == 0) {
      LAS float *EP = (LAS float *)(L + B_WT + 1024 * w + 768) + i16;
      EP[0] = e1[0]; EP[16] = e2[0];
    }
    BAR();
    if (b < 224 && tid < 32) {
      LAS float *EP = (LAS float *)(L + B_WT + 768) + tid;
      st_sc1_32(rs, (tid & 15) * 4 + (tid >> 4) * (FF * 4), (int)O_E12 + ((b & 1) * 2 * FF + 16 * wF) * 4, f2u((EP[0] + EP[256]) + (EP[512] + EP[768])));
    }
    {
      const int d = d0 + ch;
      LAS f32x4 *R = (LAS f32x4 *)(L + B_RED + (ch * 2 + pp) * 4096) + lane;
      LAS unsigned char *U = L + B_U + ch * U_CH + (31 + 32 * pp + i32) * 80 + 8 * h;
      LAS unsigned char *XU = L + B_XU + ch * 5120 + (32 * pp + i32) * 80 + 8 * h;
      f32x4 o[4];
      u32x2 hv[4], xe[4];
#pragma unroll
      for (int rg = 0; rg < 4; ++rg) { o[rg] = R[rg * 64]; hv[rg] = *(LAS u32x2 *)(U + 16 * rg); xe[rg] = *(LAS u32x2 *)(XU + 16 * rg); }
      SB();
      unsigned o0[4], o1[4];
      const f32x2 dk2 = {dsk, dsk}, ga2 = {gaw, gaw};
#pragma unroll
      for (int rg = 0; rg < 4; ++rg) {
        const f32x2 ya = lo2(o[rg]) + f32x2{acc0[4 * rg], acc0[4 * rg + 1]}, yb = hi2(o[rg]) + f32x2{acc0[4 * rg + 2], acc0[4 * rg + 3]};
        o0[rg] = pack2(pk_fma(ga2, pk_fma(bf_2(hv[rg].x), dk2, ya), bf_2(xe[rg].x)));
        o1[rg] = pack2(pk_fma(ga2, pk_fma(bf_2(hv[rg].y), dk2, yb), bf_2(xe[rg].y)));
      }
      const bool sw = (d >> 3) & 1;
      const int vo = (i32 >> 1) * 65536 + (i32 & 1) * 64 + 16 * h;
#pragma unroll
      for (int k = 0; k < 2; ++k) {
        auto s0 = __builtin_amdgcn_permlane32_swap(o0[2 * k], o0[2 * k + 1], false, false);
        auto s1 = __builtin_amdgcn_permlane32_swap(o1[2 * k], o1[2 * k + 1], false, false);
        const unsigned l0 = s0[0], h0 = s0[1], l1 = s1[0], h1 = s1[1];
        const u32x4 v = sw ? u32x4{h0, h1, l0, l1} : u32x4{l0, l1, h0, h1};
        st_sc1(rs, vo + 32 * k, (int)O_X1T + 16 * pp * 65536 + d * 128, v);
      }
    }
    SB();
    WAIT_VM(0);
    BAR();
    if (tid < 8) __hip_atomic_fetch_add(ctl + C_E2 + (tid * 8 + (b & 7)) * 32, 1u, RLX_AGENT);
    SB();
    transit_read(L, lane, w, stash, 1);
    tr.begin(lane, w, rt, (w + 2) & 3);
#pragma unroll
    for (int i = 0; i < 16; ++i) { tr.step(i); if (R2_SLEEP) __builtin_amdgcn_s_sleep(R2_SLEEP); }
    if (w == 0) wave_poll<true>(ctl + C_E2 + ((b & 7) * 8 + (lane & 7)) * 32, lane < 8, 32u);
    WAIT_VM(0);
    BAR();
  }

  u32x4 xres[2];
  {
    FRESH_IDS();
    const int ph2[2] = {2 * w, 2 * w + 1};
    const int ph1[2] = {w < 3 ? 2 * w : 5, w < 3 ? 2 * w + 1 : 6};
    const int Fs[4] = {56 + 7 * j + ph2[0], 56 + 7 * j + (ph2[1] < 7 ? ph2[1] : 0), 7 * j + ph1[0], 7 * j + (w == 2 ? 0 : ph1[1])};
    int wo[4];
#pragma unroll
    for (int s = 0; s < 4; ++s) wo[s] = (int)O_WP + Fs[s] * 16 * 1024 + lane * 16;
    if (w == 3) wo[1] = (int)O_ONES + lane * 16;
    u32x4 wf[G1_WIN][4];
    unsigned ep[4], gbv[8];
    f32x4 acc[4][4], ssq;
    {
      u32x4 xs[16];
      const int xo = (int)O_X1T + rt * 65536 + tid * 16;
#pragma unroll
      for (int i = 0; i < 16; ++i) xs[i] = ld_sc1(rs, xo, i * 4096);
      SB();
      transit_read(L, lane, w, stash, 2);
      SB();
      const int r0 = tid >> 3, cc = tid & 7;
      LAS unsigned char *X = L + B_X + r0 * 128 + ((cc ^ (4 * ((r0 >> 1) & 1))) * 16);
#pragma unroll
      for (int i = 0; i < 16; ++i) {
        *(LAS u32x4 *)(X + i * 4096) = xs[i];
        wf[i >> 2][i & 3] = ld_sc1(rs, wo[i & 3], (i >> 2) * 1024);
        acc[i >> 2][i & 3] = f32x4{0.f, 0.f, 0.f, 0.f};
        asm volatile("" : "+v"(acc[i >> 2][i & 3]));
        if (i == 15) { ssq = f32x4{0.f, 0.f, 0.f, 0.f}; asm volatile("" : "+v"(ssq)); }
        SB();
      }
      const int tf = tid < 224 ? tid : 0, f = tf < 112 ? 112 * j + tf : FH + 112 * j + (tf - 112);
#pragma unroll
      for (int q = 0; q < 4; ++q) ep[q] = ld_sc1_32(rs, f * 4, (int)O_E12 + q * FF * 4);
#pragma unroll
      for (int q = 0; q < 8; ++q) gbv[q] = ld_sc1_32(rs, (tid & 63) * 4, (int)O_MOD + (q * 3072 + 5 * 512 + 64 * j) * 4);
      SB();
    }
    BAR();
    gemm1_loop(rs, L, lane, w, wo, wf, acc, ssq);

    if (tid < 224) {
      LAS float *E = (LAS float *)(L + B_E) + tid;
      E[0] = u2f(ep[0]) + u2f(ep[2]);
      E[224] = u2f(ep[1]) + u2f(ep[3]);
    }
    if (tid < 64)
      ((LAS float *)(L + B_GB))[tid] = rbf(((u2f(gbv[0]) + u2f(gbv[1])) + (u2f(gbv[2]) + u2f(gbv[3]))) + ((u2f(gbv[4]) + u2f(gbv[5])) + (u2f(gbv[6]) + u2f(gbv[7]))));
    {
      LAS float *RS = (LAS float *)(L + B_ST);
      if (w == 3 && i16 == 0) {
#pragma unroll
        for (int t = 0; t < 4; ++t) *(LAS f32x4 *)(RS + 16 * t + 4 * g) = acc[1][t];
      }
      if (g == (i16 >> 2)) RS[64 + 16 * w + i16] = pick4(ssq, i16 & 3);
    }
    {
      const int q = i16 >> 2, pq = lane & 3;
      LAS unsigned char *xb = L + B_X + (8 * g + q) * 128 + (((4 * (w >> 1) + pq) ^ (4 * (q >> 1))) * 16) + (((w & 1) ^ (g & 1)) * 8) + 2 * j * 4096;
#pragma unroll
      for (int dl = 0; dl < 2; ++dl) xres[dl] = cat2(tr_read(xb + dl * 4096), tr_read(xb + dl * 4096 + 512));
    }
    tr.begin(lane, w, rt, (w + 3) & 3);
    BAR();
    LAS float *SM = (LAS float *)(L + B_ZS + w * 512), *SR = SM + 64;
    asm volatile("" : "+v"(SM));
    SR = SM + 64;
    {
      LAS float *RS = (LAS float *)(L + B_ST);
      const float mean = RS[lane] * (1.f / DM), var = __builtin_fmaf(RS[64 + lane], 1.f / DM, -mean * mean);
      const float r = ln_rstd(fmaxf(var, 0.f));
      const int s = tile_row(g, i16);
      SM[s] = -rbf(mean) * r;
      SR[s] = r;
    }

    const int nz2 = w == 3 ? 1 : 2, nz1 = w == 2 ? 1 : 2;
    u32x4 zf[2][2];
    f32x4 e1z[2], e2z[2];
    float m1[4], r1[4];
    {
      LAS float *E1 = (LAS float *)(L + B_E);
      asm volatile("" : "+v"(E1));
      f32x4 m4[4], r4[4];
      float e1[2], e2[2];
#pragma unroll
      for (int t = 0; t < 4; ++t) { m4[t] = *(LAS f32x4 *)(SM + 32 * (t >> 1) + 8 * g + 4 * (t & 1)); r4[t] = *(LAS f32x4 *)(SR + 32 * (t >> 1) + 8 * g + 4 * (t & 1)); }
#pragma unroll
      for (int s = 0; s < 2; ++s) {
        const int p2 = s < nz2 ? ph2[s] : 0, p1 = s < nz1 ? ph1[s] : 0;
        e1[s] = E1[112 + 16 * p2 + i16]; e2[s] = E1[224 + 112 + 16 * p2 + i16];
        e1z[s] = *(LAS f32x4 *)(E1 + 16 * p1 + 4 * g); e2z[s] = *(LAS f32x4 *)(E1 + 224 + 16 * p1 + 4 * g);
      }
#pragma unroll
      for (int t = 0; t < 4; ++t) { const int sr = tile_row(t, i16); m1[t] = SM[sr]; r1[t] = SR[sr]; }
      SB();
      LAS unsigned char *ZW = L + B_ZT + i16 * 128 + 16 * g;
#pragma unroll
      for (int s = 0; s < 2; ++s) {
        if (s < nz2) {
          const f32x2 e12 = {e1[s], e1[s]}, e22 = {e2[s], e2[s]};
#pragma unroll
          for (int t = 0; t < 4; ++t) {
            const f32x4 a = acc[s][t];
            zf[s][t >> 1][2 * (t & 1)] = pack2(gelu2(pk_fma(lo2(a), lo2(r4[t]), pk_fma(lo2(m4[t]), e12, e22))));
            zf[s][t >> 1][2 * (t & 1) + 1] = pack2(gelu2(pk_fma(hi2(a), hi2(r4[t]), pk_fma(hi2(m4[t]), e12, e22))));
            if (s == 0) { tr.step(2 * t); tr.step(2 * t + 1); SB(); }
          }
#pragma unroll
          for (int kp = 0; kp < 2; ++kp) *(LAS u32x4 *)(ZW + ph2[s] * 2048 + 64 * kp) = zf[s][kp];
        }
      }
    }
    if (tid < 128) *(LAS u32x4 *)(L + B_ZT + 112 * 128 + 16 * tid) = u32x4{0, 0, 0, 0};
    BAR();
    {
      const int q = i16 >> 2, pq = lane & 3;
      const u32x4 ones = opaque_ones();
      f32x4 su, sq;
      u32x4 za[4];
      LAS unsigned char *A = L + B_ZT + (8 * g + q) * 128 + (16 * w + 4 * pq) * 2;
#pragma unroll
      for (int st = 0; st < 4; ++st) { za[st] = cat2(tr_read(A + st * 4096), tr_read(A + st * 4096 + 512)); tr.step(8 + st); }
      mma16zn(su, za[0], ones);
      mma16z(sq, za[0], za[0]);
#pragma unroll
      for (int st = 1; st < 4; ++st) { mma16(su, za[st], ones); mma16(sq, za[st], za[st]); }
#pragma unroll
      for (int st = 12; st < 16; ++st) tr.step(st);
      MFMA_FENCE();
      if (g == q) {
        const u32x4 gr = {f2u(pick4(su, i16 & 3)), epoch, f2u(pick4(sq, i16 & 3)), epoch};
        st_sc1(rs, (16 * w + i16) * 16, (int)O_GRAN + (rt * 8 + j) * 1024, gr);
      }
    }
    {
      LAS unsigned char *Z1 = L + B_Z1 + lane * 8;
      asm volatile("" : "+v"(Z1));
#pragma unroll
      for (int s = 0; s < 2; ++s) {
        if (s < nz1) {
          u32x2 z[4];
#pragma unroll
          for (int t = 0; t < 4; ++t) {
            const f32x2 m2 = {m1[t], m1[t]}, r2 = {r1[t], r1[t]};
            const f32x4 a = acc[2 + s][t];
            z[t] = u32x2{pack2(gelu2(pk_fma(lo2(a), r2, pk_fma(m2, lo2(e1z[s]), lo2(e2z[s]))))),
                         pack2(gelu2(pk_fma(hi2(a), r2, pk_fma(m2, hi2(e1z[s]), hi2(e2z[s])))))};
          }
#pragma unroll
          for (int t = 0; t < 4; ++t) *(LAS u32x2 *)(Z1 + ph1[s] * 2048 + t * 512) = z[t];
        }
      }
    }
    {
      float su, sq;
      for (unsigned spins = 0; spins < SPIN_CAP; ++spins) {
        u32x4 gr[8];
#pragma unroll
        for (int q = 0; q < 8; ++q) gr[q] = ld_sc1(rs, lane * 16, (int)O_GRAN + (rt * 8 + q) * 1024);
        unsigned bad = 0u;
        su = 0.f; sq = 0.f;
#pragma unroll
        for (int q = 0; q < 8; ++q) { bad |= (gr[q][1] ^ epoch) | (gr[q][3] ^ epoch); su += u2f(gr[q][0]); sq += u2f(gr[q][2]); }
        if (__all(bad == 0u)) break;
        __builtin_amdgcn_s_sleep(POLL_SLEEP);
      }
      const float mean = su * (1.f / FH), var = sq * (1.f / FH) - mean * mean;
      const float r = ln_rstd(fmaxf(var, 0.f));
      LAS float *ZR = (LAS float *)(L + B_ZS + w * 512);
      asm volatile("" : "+v"(ZR));
      ZR[lane] = r;
      ZR[64 + lane] = -rbf(mean) * r;
      f32x4 r4[4], m4[4];
#pragma unroll
      for (int t = 0; t < 4; ++t) { r4[t] = *(LAS f32x4 *)(ZR + 32 * (t >> 1) + 8 * g + 4 * (t & 1)); m4[t] = *(LAS f32x4 *)(ZR + 64 + 32 * (t >> 1) + 8 * g + 4 * (t & 1)); }
      SB();
#pragma unroll
      for (int s = 0; s < 2; ++s) {
        if (s < nz2) {
#pragma unroll
          for (int kp = 0; kp < 2; ++kp) {
            u32x4 o;
#pragma unroll
            for (int hf = 0; hf < 2; ++hf) {
              o[2 * hf] = pack2(pk_fma(bf_2(zf[s][kp][2 * hf]), lo2(r4[2 * kp + hf]), lo2(m4[2 * kp + hf])));
              o[2 * hf + 1] = pack2(pk_fma(bf_2(zf[s][kp][2 * hf + 1]), hi2(r4[2 * kp + hf]), hi2(m4[2 * kp + hf])));
            }
            st_sc1(rs, lane * 16, (int)O_Z2F + ((7 * j + ph2[s]) * 64 + 2 * rt + kp) * 1024, o);
          }
        }
      }
    }
    SB();
    WAIT_VM(0);
    BAR();
    if (tid == 0) __hip_atomic_store(ctl + C_F3 + (rt * 8 + j) * 32, epoch, RLX_AGENT);
    SB();
    transit_read(L, lane, w, stash, 3);
    BAR();
  }

  u32x4 wof[4][7];
  {
    FRESH_IDS();
    const float bsp = bf2f(p.b_sp[64 * rt + tile_row(w, i16)]);
    wave_poll<false>(ctl + C_F3 + ((8 * w + (lane & 7)) * 8 + j) * 32, lane < 8, epoch);
    const int ao = (int)O_Z2F + (7 * j * 64 + 16 * w) * 1024, vo = lane * 16;
    u32x4 af[G2_WIN];
#pragma unroll
    for (int n = 0; n < G2_WIN; ++n) af[n] = ld_sc1(rs, vo, ao + ((n >> 4) * 64 + (n & 15)) * 1024);
    SB();
    f32x4 acc[2][4], keep[7];
    LAS f32x4 *R = (LAS f32x4 *)L + lane;
    LAS f32x4 *Rs[3];
#pragma unroll
    for (int s = 1; s < 4; ++s) Rs[s - 1] = R + (((s + w) & 3) * 3 + (s - 1)) * 7 * 64;
#pragma unroll
    for (int ph = 0; ph < 7; ++ph) {
#pragma unroll
      for (int k = 0; k < 16; ++k) {
        const int n = ph * 16 + k;
        const u32x4 cur = af[n % G2_WIN];
#pragma unroll
        for (int s = 0; s < 4; ++s) { if (k == 0) mma16az(acc[ph & 1][s], cur, stash[k][s]); else mma16a(acc[ph & 1][s], cur, stash[k][s]); }
        if (n + G2_WIN < 112) af[n % G2_WIN] = ld_sc1(rs, vo, ao + (((n + G2_WIN) >> 4) * 64 + ((n + G2_WIN) & 15)) * 1024);
        if (ph > 0 && k >= 3 && k < 6) Rs[k - 3][(ph - 1) * 64] = acc[(ph - 1) & 1][k - 2];
        if (ph > 0 && k == 6) keep[ph - 1] = acc[(ph - 1) & 1][0];
        if (n >= 84) {
          const int m = n - 84;
          wof[m / 7][m % 7] = ld_sc1(rs, vo, (int)O_WOF + ((4 * j + m / 7) * 28 + 7 * w + m % 7) * 1024);
        }
        SB();
      }
    }
    MFMA_FENCE();
#pragma unroll
    for (int s = 1; s < 4; ++s) Rs[s - 1][6 * 64] = acc[0][s];
    keep[6] = acc[0][0];
    SB();
    BAR();
    unsigned vv[7][2];
    {
      const f32x2 b2 = {bsp, bsp};
      LAS f32x4 *Rw = R + w * 3 * 7 * 64;
      LAS unsigned char *Z1 = L + B_Z1 + (w * 64 + lane) * 8;
      asm volatile("" : "+v"(Z1));
      f32x4 ra[7][3];
      u32x2 z1[7];
#pragma unroll
      for (int ph = 0; ph < 7; ++ph) {
#pragma unroll
        for (int q = 0; q < 3; ++q) ra[ph][q] = Rw[(q * 7 + ph) * 64];
        z1[ph] = *(LAS u32x2 *)(Z1 + ph * 2048);
      }
      SB();
#pragma unroll
      for (int ph = 0; ph < 7; ++ph) {
        const f32x4 s = (keep[ph] + ra[ph][0]) + (ra[ph][1] + ra[ph][2]);
        vv[ph][0] = pack2(bf_2(z1[ph].x) * (lo2(s) + b2));
        vv[ph][1] = pack2(bf_2(z1[ph].y) * (hi2(s) + b2));
      }
    }
    const int vb = (int)O_VF + (4 * rt + w) * 28 * 1024;
    if ((j & 1) == 0) {
      const int p0 = 7 * j / 2;
#pragma unroll
      for (int k = 0; k < 3; ++k) st_sc1(rs, vo, vb + (p0 + k) * 1024, u32x4{vv[2 * k][0], vv[2 * k][1], vv[2 * k + 1][0], vv[2 * k + 1][1]});
      st_sc1_64(rs, vo, vb + (p0 + 3) * 1024, u32x2{vv[6][0], vv[6][1]});
    } else {
      const int p0 = (7 * j - 1) / 2;
      st_sc1_64(rs, vo + 8, vb + p0 * 1024, u32x2{vv[0][0], vv[0][1]});
#pragma unroll
      for (int k = 0; k < 3; ++k)
        st_sc1(rs, vo, vb + (p0 + 1 + k) * 1024, u32x4{vv[2 * k + 1][0], vv[2 * k + 1][1], vv[2 * k + 2][0], vv[2 * k + 2][1]});
    }
    SB();
    WAIT_VM(0);
    BAR();
    if (tid == 0) __hip_atomic_store(ctl + C_F4 + (rt * 8 + j) * 32, epoch, RLX_AGENT);
  }

  {
    FRESH_IDS();
    wave_poll<false>(ctl + C_F4 + (rt * 8 + 2 * w + (lane & 1)) * 32, lane < 2, epoch);
    u32x4 vf[7][4];
#pragma unroll
    for (int k = 0; k < 7; ++k)
#pragma unroll
      for (int s = 0; s < 4; ++s) vf[k][s] = ld_sc1(rs, lane * 16, (int)O_VF + ((4 * rt + ((s + w) & 3)) * 28 + 7 * w + k) * 1024);
    SB();
    f32x4 acc[4][4];
#pragma unroll
    for (int k = 0; k < 7; ++k)
#pragma unroll
      for (int s = 0; s < 4; ++s) {
#pragma unroll
        for (int dd = 0; dd < 4; ++dd) { if (k == 0) mma16az(acc[dd][s], wof[dd][k], vf[k][s]); else mma16a(acc[dd][s], wof[dd][k], vf[k][s]); }
        SB();
      }
    MFMA_FENCE();
    LAS f32x4 *R = (LAS f32x4 *)L + lane;
#pragma unroll
    for (int s = 1; s < 4; ++s) {
      LAS f32x4 *Rs = R + (((s + w) & 3) * 3 + (s - 1)) * 4 * 64;
#pragma unroll
      for (int dd = 0; dd < 4; ++dd) Rs[dd * 64] = acc[dd][s];
    }
    BAR();
    const int t = 64 * rt + tile_row(w, i16);
    {
      LAS f32x4 *Rw = R + w * 3 * 4 * 64;
      LAS unsigned char *GB = L + B_GB + 32 * g;
      asm volatile("" : "+v"(GB));
      f32x4 ra[4][3], gb[4];
#pragma unroll
      for (int dd = 0; dd < 4; ++dd) {
#pragma unroll
        for (int q = 0; q < 3; ++q) ra[dd][q] = Rw[(q * 4 + dd) * 64];
        gb[dd] = *(LAS f32x4 *)(GB + 128 * (dd >> 1) + 16 * (dd & 1));
      }
      SB();
#pragma unroll
      for (int dl = 0; dl < 2; ++dl) {
        u32x4 o;
#pragma unroll
        for (int hf = 0; hf < 2; ++hf) {
          const int dd = 2 * dl + hf;
          const f32x4 s = (acc[dd][0] + ra[dd][0]) + (ra[dd][1] + ra[dd][2]);
          o[2 * hf] = pack2(pk_fma(lo2(gb[dd]), lo2(s), bf_2(xres[dl][2 * hf])));
          o[2 * hf + 1] = pack2(pk_fma(hi2(gb[dd]), hi2(s), bf_2(xres[dl][2 * hf + 1])));
        }
        *(GAS u32x4 *)(p.out + (size_t)t * DM + 64 * j + 32 * dl + 8 * g) = o;
      }
    }
    if (b == 0 && tid == 0) __hip_atomic_store(ctl + C_EPOCH, epoch, RLX_AGENT);
  }
}

extern "C" void kernel_launch(void *const *d_in, const int *in_sizes, int n_in, void *d_out, int out_size, void *d_ws,
                              size_t ws_size, hipStream_t stream) {
  Params p;
  p.x = (const u16 *)d_in[0];
  p.a_re = (const u16 *)d_in[1]; p.a_im = (const u16 *)d_in[2];
  p.b_re = (const u16 *)d_in[3]; p.b_im = (const u16 *)d_in[4];
  p.c_re = (const u16 *)d_in[5]; p.c_im = (const u16 *)d_in[6];
  p.log_dt = (const u16 *)d_in[7]; p.dskip = (const u16 *)d_in[8];
  p.w_in = (const u16 *)d_in[10]; p.w_sp = (const u16 *)d_in[11]; p.b_sp = (const u16 *)d_in[12];
  p.w_out = (const u16 *)d_in[13]; p.cvec = (const u16 *)d_in[14]; p.w_mod = (const u16 *)d_in[15];
  p.out = (u16 *)d_out;
  p.ws = (unsigned char *)d_ws;
  (void)hipFuncSetAttribute((const void *)kb_main, hipFuncAttributeMaxDynamicSharedMemorySize, B_LDS);
  kb_main<<<256, 256, B_LDS, stream>>>(p);
}
```

```cpp
#include <hip/hip_runtime.h>
#include <stdint.h>
#include <string.h>

#define GAS __attribute__((address_space(1)))
#define LAS __attribute__((address_space(3)))
#define CAS __attribute__((address_space(4)))
#define DEVI __device__ __forceinline__
typedef unsigned short u16;
typedef unsigned long long u64;
typedef float f32x2 __attribute__((ext_vector_type(2)));
typedef float f32x4 __attribute__((ext_vector_type(4)));
typedef float f32x16 __attribute__((ext_vector_type(16)));
typedef unsigned u32x2 __attribute__((ext_vector_type(2)));
typedef unsigned u32x4 __attribute__((ext_vector_type(4)));
typedef __bf16 bf16x2_t __attribute__((ext_vector_type(2)));
typedef GAS unsigned gu32;
typedef GAS u64 gu64;
typedef __amdgpu_buffer_rsrc_t rsrc_t;
#define RLX_AGENT __ATOMIC_RELAXED, __HIP_MEMORY_SCOPE_AGENT

constexpr int SEQ = 2048, DM = 512, NS = 128, FF = 1792, FH = 896;

constexpr size_t O_GRAN = 131072;
constexpr size_t O_E12 = O_GRAN + 32 * 8 * 64 * 16;
constexpr size_t O_MOD = 1 << 20;
constexpr size_t O_XT = 2 << 20;
constexpr size_t O_X1T = 9 << 20;
constexpr size_t O_WP = 11 << 20;
constexpr size_t O_WOF = 13 << 20;
constexpr size_t O_Z2F = 14 << 20;
constexpr size_t O_VF = 18 << 20;
constexpr int WS_USED = 22 << 20;
constexpr int C_EPOCH = 0;
constexpr int C_E1 = 1024;
constexpr int C_E2 = 4096;
constexpr int C_F3 = C_E2 + 64 * 32;
constexpr int C_F4 = C_F3 + 256 * 32;
static_assert((C_F4 + 256 * 32) * 4 <= (int)O_GRAN, "control block");

struct Params {
  const u16 *x, *a_re, *a_im, *b_re, *b_im, *c_re, *c_im, *log_dt, *dskip;
  const u16 *w_in, *w_sp, *b_sp, *w_out, *cvec, *w_mod;
  u16 *out;
  unsigned char *ws;
};

#define STAMP_DECL
#define STAMP(kid, slot) do {} while (0)
#define STAMP_FLUSH(kid, n) do {} while (0)
DEVI float u2f(unsigned u) { return __builtin_bit_cast(float, u); }
DEVI unsigned f2u(float f) { return __builtin_bit_cast(unsigned, f); }
DEVI float bf_lo(unsigned u) { return u2f(u << 16); }
DEVI float bf_hi(unsigned u) { return u2f(u & 0xffff0000u); }
DEVI f32x2 bf_2(unsigned u) { return f32x2{bf_lo(u), bf_hi(u)}; }
DEVI float bf2f(u16 h) { return u2f((unsigned)h << 16); }
DEVI unsigned pack2(f32x2 v) { return __builtin_bit_cast(unsigned, __builtin_convertvector(v, bf16x2_t)); }
DEVI unsigned pack_bf(float lo, float hi) { return pack2(f32x2{lo, hi}); }
DEVI float rbf(float f) { return bf_lo(pack_bf(f, f)); }
DEVI f32x2 pk_fma(f32x2 a, f32x2 b, f32x2 c) { return __builtin_elementwise_fma(a, b, c); }
DEVI f32x2 lo2(f32x4 v) { return f32x2{v[0], v[1]}; }
DEVI f32x2 hi2(f32x4 v) { return f32x2{v[2], v[3]}; }
template <int CTRL> DEVI float dpp_f(float x) {
  return __builtin_bit_cast(float, __builtin_amdgcn_update_dpp(0, __builtin_bit_cast(int, x), CTRL, 0xf, 0xf, true));
}
DEVI float wave_sum(float x) {
  x += dpp_f<0xB1>(x);
  x += dpp_f<0x4E>(x);
  x += dpp_f<0x141>(x);
  x += dpp_f<0x140>(x);
  auto s = __builtin_amdgcn_permlane16_swap(f2u(x), f2u(x), false, false);
  x = u2f(s[0]) + u2f(s[1]);
  auto t = __builtin_amdgcn_permlane32_swap(f2u(x), f2u(x), false, false);
  return u2f(t[0]) + u2f(t[1]);
}
DEVI void wave_sum4(float (&x)[4]) {
#pragma unroll
  for (int i = 0; i < 4; ++i) x[i] += dpp_f<0xB1>(x[i]);
#pragma unroll
  for (int i = 0; i < 4; ++i) x[i] += dpp_f<0x4E>(x[i]);
#pragma unroll
  for (int i = 0; i < 4; ++i) x[i] += dpp_f<0x141>(x[i]);
#pragma unroll
  for (int i = 0; i < 4; ++i) x[i] += dpp_f<0x140>(x[i]);
#pragma unroll
  for (int i = 0; i < 4; ++i) { auto s = __builtin_amdgcn_permlane16_swap(f2u(x[i]), f2u(x[i]), false, false); x[i] = u2f(s[0]) + u2f(s[1]); }
#pragma unroll
  for (int i = 0; i < 4; ++i) { auto t = __builtin_amdgcn_permlane32_swap(f2u(x[i]), f2u(x[i]), false, false); x[i] = u2f(t[0]) + u2f(t[1]); }
}
DEVI u32x2 tr_read(LAS unsigned char *p) {
  typedef short v4i16_t __attribute__((ext_vector_type(4)));
  return __builtin_bit_cast(u32x2, __builtin_amdgcn_ds_read_tr16_b64_v4i16((LAS v4i16_t *)p));
}
DEVI u32x4 cat2(u32x2 a, u32x2 b) { return u32x4{a.x, a.y, b.x, b.y}; }
DEVI float ln_rstd(float var) {
  return __builtin_amdgcn_rcpf(rbf(__builtin_amdgcn_sqrtf(rbf(rbf(var) + 1e-5f))));
}
DEVI f32x2 gelu2(f32x2 x) {
  constexpr float K0 = -0.7978845608028654f * 2.0f * 1.4426950408889634f, K1 = K0 * 0.044715f;
  const f32x2 u = x * pk_fma(x * x, f32x2{K1, K1}, f32x2{K0, K0});
  const f32x2 d = f32x2{__builtin_amdgcn_exp2f(u[0]), __builtin_amdgcn_exp2f(u[1])} + f32x2{1.f, 1.f};
  return x * f32x2{__builtin_amdgcn_rcpf(d[0]), __builtin_amdgcn_rcpf(d[1])};
}
DEVI void block_rt_j(int b, int &rt, int &j) {
  const int x = b & 7, q = b >> 3;
  rt = 8 * (x >> 1) + (q & 7);
  j = 4 * (x & 1) + (q >> 3);
}
DEVI int tile_row(int tau, int i) { return 32 * (tau >> 1) + 8 * (i >> 2) + 4 * (tau & 1) + (i & 3); }
DEVI f32x2 cmul(f32x2 z, f32x2 v, f32x2 vr) { return pk_fma(f32x2{z[1], z[1]}, vr, f32x2{z[0], z[0]} * v); }
DEVI f32x2 crot(f32x2 v) { return f32x2{-v[1], v[0]}; }
DEVI f32x2 cexp_m(float lr2, float rev, float m) {
  const float er = __builtin_amdgcn_exp2f(lr2 * m);
  const float fr = __builtin_amdgcn_fractf(rev * m);
  return f32x2{er * __builtin_amdgcn_cosf(fr), er * __builtin_amdgcn_sinf(fr)};
}

#define SB() __builtin_amdgcn_sched_barrier(0)
#define WAIT_VM(n) asm volatile("s_waitcnt vmcnt(" #n ")" ::: "memory")
#define WAIT_LGKM0() asm volatile("s_waitcnt lgkmcnt(0)" ::: "memory")
#define BAR() asm volatile("s_waitcnt lgkmcnt(0)\n\ts_barrier" ::: "memory")
constexpr int AUX_SC1 = 16;
constexpr unsigned SPIN_CAP = 1u << 18;
#define POLL_SLEEP 2
#define R2_SLEEP 2

constexpr int B_RC = 4160;
constexpr int B_U = 8 * B_RC;
constexpr int U_CH = 95 * 80 + 16;
constexpr int B_RED = B_U + 2 * U_CH;
constexpr int KT_ROW = 208;
constexpr int B_KT = 4 * B_RC;
constexpr int B_KD = B_KT + 256 * KT_ROW;
constexpr int B_WT = B_KD + 4096;
constexpr int B_TR = B_WT + 4096;
constexpr int TR_WAVE = 16 * 1040;
constexpr int B_Z1 = B_TR + 4 * TR_WAVE;
constexpr int B_ST = B_Z1 + 14336;
constexpr int B_E = B_ST + 512;
constexpr int B_ZS = B_E + 2 * 224 * 4;
constexpr int B_GB = B_ZS + 2048;
constexpr int B_LDS = B_GB + 256;
constexpr int B_CS = B_Z1 + 10240;
constexpr int B_XU = B_Z1;
constexpr int B_LN = 0;
constexpr int B_WO = 16384;
constexpr int B_MW = 24576;
constexpr int B_MS = B_MW + 64 * 272;
constexpr int B_X = 0;
constexpr int B_ZT = 0;
static_assert(B_RED + 16384 <= B_WT && B_MS + 128 <= B_WT && 65536 <= B_WT && (B_KD & 15) == 0 && (B_WT & 15) == 0 &&
              4 * 3 * 7 * 1024 <= B_Z1 && B_LDS <= 163840, "lds map");
constexpr int G1_WIN = 4;
constexpr int G2_WIN = 24;

DEVI rsrc_t mk_rsrc(const void *p, int bytes) { return __builtin_amdgcn_make_buffer_rsrc((void *)p, 0, bytes, 0x00020000); }
DEVI u32x4 ld128(rsrc_t rs, int voff, int soff) { return __builtin_amdgcn_raw_buffer_load_b128(rs, voff, soff, 0); }
DEVI unsigned ld16(rsrc_t rs, int voff, int soff) { return __builtin_amdgcn_raw_buffer_load_b16(rs, voff, soff, 0); }
DEVI u32x4 ld_sc1(rsrc_t rs, int voff, int soff) { return __builtin_amdgcn_raw_buffer_load_b128(rs, voff, soff, AUX_SC1); }
DEVI u32x2 ld_sc1_64(rsrc_t rs, int voff, int soff) { return __builtin_amdgcn_raw_buffer_load_b64(rs, voff, soff, AUX_SC1); }
DEVI unsigned ld_sc1_32(rsrc_t rs, int voff, int soff) { return __builtin_amdgcn_raw_buffer_load_b32(rs, voff, soff, AUX_SC1); }
DEVI void st_sc1(rsrc_t rs, int voff, int soff, u32x4 v) { __builtin_amdgcn_raw_buffer_store_b128(v, rs, voff, soff, AUX_SC1); }
DEVI void st_sc1_64(rsrc_t rs, int voff, int soff, u32x2 v) { __builtin_amdgcn_raw_buffer_store_b64(v, rs, voff, soff, AUX_SC1); }
DEVI void st_sc1_32(rsrc_t rs, int voff, int soff, unsigned v) { __builtin_amdgcn_raw_buffer_store_b32(v, rs, voff, soff, AUX_SC1); }
DEVI float pick4(f32x4 v, int r) { return r == 0 ? v[0] : r == 1 ? v[1] : r == 2 ? v[2] : v[3]; }
template <bool GE> DEVI void wave_poll(gu32 *word, bool on, unsigned want) {
  for (unsigned spins = 0; spins < SPIN_CAP; ++spins) {
    const unsigned v = on ? __hip_atomic_load(word, RLX_AGENT) : want;
    if (__all(GE ? v >= want : v == want)) break;
    __builtin_amdgcn_s_sleep(POLL_SLEEP);
  }
  __builtin_amdgcn_fence(__ATOMIC_ACQUIRE, "wavefront");
}

DEVI void mma16(f32x4 &c, u32x4 a, u32x4 b) { asm volatile("v_mfma_f32_16x16x32_bf16 %0, %1, %2, %0" : "+v"(c) : "v"(a), "v"(b)); }
DEVI void mma16a(f32x4 &c, u32x4 a, u32x4 b) { asm volatile("v_mfma_f32_16x16x32_bf16 %0, %1, %2, %0" : "+v"(c) : "v"(a), "a"(b)); }
DEVI void mma32(f32x16 &c, u32x4 a, u32x4 b) { asm volatile("v_mfma_f32_32x32x16_bf16 %0, %1, %2, %0" : "+v"(c) : "v"(a), "v"(b)); }
DEVI void mma16z(f32x4 &c, u32x4 a, u32x4 b) { asm volatile("v_mfma_f32_16x16x32_bf16 %0, %1, %2, 0" : "=&v"(c) : "v"(a), "v"(b)); }
DEVI void mma16n(f32x4 &c, u32x4 a, u32x4 b) { asm volatile("s_nop 1\n\tv_mfma_f32_16x16x32_bf16 %0, %1, %2, %0" : "+v"(c) : "v"(a), "v"(b)); }
DEVI void mma16zn(f32x4 &c, u32x4 a, u32x4 b) { asm volatile("s_nop 1\n\tv_mfma_f32_16x16x32_bf16 %0, %1, %2, 0" : "=&v"(c) : "v"(a), "v"(b)); }
DEVI void mma16az(f32x4 &c, u32x4 a, u32x4 b) { asm volatile("v_mfma_f32_16x16x32_bf16 %0, %1, %2, 0" : "=&v"(c) : "v"(a), "a"(b)); }
DEVI void mma16A(f32x4 &c, u32x4 a, u32x4 b) { asm volatile("v_mfma_f32_16x16x32_bf16 %0, %1, %2, %0" : "+v"(c) : "a"(a), "v"(b)); }
DEVI void mma16Az(f32x4 &c, u32x4 a, u32x4 b) { asm volatile("v_mfma_f32_16x16x32_bf16 %0, %1, %2, 0" : "=&v"(c) : "a"(a), "v"(b)); }
DEVI void mma32z(f32x16 &c, u32x4 a, u32x4 b) { asm volatile("s_nop 1\n\tv_mfma_f32_32x32x16_bf16 %0, %1, %2, 0" : "=&v"(c) : "v"(a), "v"(b)); }
DEVI void mma32n(f32x16 &c, u32x4 a, u32x4 b) { asm volatile("s_nop 1\n\tv_mfma_f32_32x32x16_bf16 %0, %1, %2, %0" : "+v"(c) : "v"(a), "v"(b)); }
#define MFMA_FENCE() do { SB(); asm volatile("s_nop 7\n\ts_nop 7" ::: "memory"); SB(); } while (0)
#define FRESH_IDS()                                   \
  int tid = threadIdx.x;                              \
  asm volatile("" : "+v"(tid));                       \
  const int lane = tid & 63, i16 = lane & 15, g = lane >> 4; \
  (void)i16; (void)g
DEVI u32x4 opaque_ones() {
  u32x4 o = u32x4{0x3f803f80u, 0x3f803f80u, 0x3f803f80u, 0x3f803f80u};
  asm volatile("" : "+v"(o));
  return o;
}
struct Transit {
  rsrc_t rw;
  int v[4], so, la;
  DEVI void begin(int lane, int w, int rt, int tau) {
#pragma unroll
    for (int c = 0; c < 4; ++c) v[c] = lane * 16 + c * 4096;
    so = ((64 * rt + 32 * (tau >> 1) + 4 * (tau & 1)) * SEQ + 512 * w) * 2;
    la = B_TR + w * TR_WAVE;
  }
  DEVI void step(int i) {
    asm volatile("s_mov_b32 m0, %0\n\ts_add_u32 %0, %0, 1040\n\tbuffer_load_dwordx4 %2, %3, %1 offen lds"
                 : "+s"(la) : "s"(so), "v"(v[i & 3]), "s"(rw) : "memory", "scc");
    if ((i & 3) == 3) so += 32768;
  }
  DEVI void all() {
#pragma unroll
    for (int i = 0; i < 16; ++i) step(i);
  }
};
DEVI void transit_read(LAS unsigned char *L, int lane, int w, u32x4 (&stash)[16][4], int slot) {
  LAS unsigned char *T = L + B_TR + w * TR_WAVE + (lane & 15) * 1040 + (lane >> 4) * 16;
  asm volatile("" : "+v"(T));
#pragma unroll
  for (int k = 0; k < 16; ++k) {
    const u32x4 v = *(LAS u32x4 *)(T + 64 * k);
    if (slot == 0) stash[k][0] = v; else if (slot == 1) stash[k][1] = v; else if (slot == 2) stash[k][2] = v; else stash[k][3] = v;
  }
  WAIT_LGKM0();
}

template <int VAR>
DEVI void gemm1_loop(rsrc_t rs, LAS unsigned char *L, int lane, const int (&wo)[4], u32x4 (&wf)[G1_WIN][4], f32x4 (&acc)[4][4],
                     f32x4 (&ssum)[2], f32x4 (&ssq)[2]) {
  const int g = lane >> 4, q = (lane & 15) >> 2, pq = lane & 3;
  LAS unsigned char *xb[4];
#pragma unroll
  for (int tau = 0; tau < 4; ++tau)
    xb[tau] = L + B_X + (8 * g + q) * 128 + (((4 * (tau >> 1) + pq) ^ (4 * (q >> 1))) * 16) + (((tau & 1) ^ (g & 1)) * 8);
  constexpr bool act[4] = {true, VAR != 3, true, VAR != 2};
  const u32x4 ones = opaque_ones();
  u32x2 xl[2][4], xh[2][4];
#pragma unroll
  for (int tau = 0; tau < 4; ++tau) { xl[0][tau] = tr_read(xb[tau]); xh[0][tau] = tr_read(xb[tau] + 512); }
  SB();
#pragma unroll
  for (int ks = 0; ks < 16; ++ks) {
    const int c = ks & 1, n = c ^ 1;
    u32x4 cur[4];
#pragma unroll
    for (int s = 0; s < 4; ++s) cur[s] = wf[ks % G1_WIN][s];
#pragma unroll
    for (int tau = 0; tau < 4; ++tau) {
      const u32x4 xf = cat2(xl[c][tau], xh[c][tau]);
      if (ks == 0) mma16z(acc[0][tau], xf, cur[0]); else mma16(acc[0][tau], xf, cur[0]);
      if (ks + 1 < 16) xl[n][tau] = tr_read(xb[tau] + (ks + 1) * 4096);
      SB();
      if (act[1]) { if (ks == 0) mma16z(acc[1][tau], xf, cur[1]); else mma16(acc[1][tau], xf, cur[1]); }
      else if (tau >= 2) { if (ks == 0) mma16z(ssum[tau - 2], xf, ones); else mma16(ssum[tau - 2], xf, ones); }
      if (ks + 1 < 16) xh[n][tau] = tr_read(xb[tau] + (ks + 1) * 4096 + 512);
      SB();
      if (ks == 0) mma16z(acc[2][tau], cur[2], xf); else mma16(acc[2][tau], cur[2], xf);
      SB();
      if (act[3]) { if (ks == 0) mma16z(acc[3][tau], cur[3], xf); else mma16(acc[3][tau], cur[3], xf); }
      else if (tau < 2) { if (ks == 0) mma16z(ssum[tau], xf, ones); else mma16(ssum[tau], xf, ones); }
      if (VAR == 2 && tau < 2) { if (ks == 0) mma16z(ssq[tau], xf, xf); else mma16(ssq[tau], xf, xf); }
      if (VAR == 3 && tau >= 2) { if (ks == 0) mma16z(ssq[tau - 2], xf, xf); else mma16(ssq[tau - 2], xf, xf); }
      if (ks + G1_WIN < 16 && act[tau]) wf[ks % G1_WIN][tau] = ld_sc1(rs, wo[tau], (ks + G1_WIN) * 1024);
      SB();
    }
  }
  MFMA_FENCE();
}

__global__ __launch_bounds__(256, 1) void kb_main(Params p) {
  extern __shared__ __attribute__((aligned(16))) unsigned char smem_raw[];
  LAS unsigned char *L = (LAS unsigned char *)smem_raw;
  const int b = blockIdx.x;
  const int w = __builtin_amdgcn_readfirstlane(threadIdx.x >> 6);
  const rsrc_t rs = mk_rsrc(p.ws, WS_USED);
  gu32 *ctl = (gu32 *)p.ws;
  int rt, j;
  block_rt_j(b, rt, j);
  const int d0 = 64 * (b & 7) + 2 * (b >> 3);
  const unsigned epoch_raw = __hip_atomic_load(ctl + C_EPOCH, RLX_AGENT);
  unsigned epoch;

  u32x4 stash[16][4];
  Transit tr;
  tr.rw = mk_rsrc(p.w_sp, SEQ * SEQ * 2);

  unsigned prm[2][6], ldt[2];
  f32x2 z[2][2], zm[2], zmr[2], y[2][2], ym[2], ymr[2];
  int kidx[12];
  {
    FRESH_IDS();
    const int t0 = 8 * b;
    const rsrc_t rx = mk_rsrc(p.x, SEQ * DM * 2);
    u32x4 xr[2];
#pragma unroll
    for (int r = 0; r < 2; ++r) xr[r] = ld128(rx, lane * 16, (t0 + 2 * w + r) * (DM * 2));
    u32x4 jm[4], jo[2];
    unsigned cv = 0;
    const int mcg = b % 24, mkb = b / 24;
    if (b < 192) {
      const rsrc_t rm = mk_rsrc(p.w_mod, DM * 3072 * 2);
#pragma unroll
      for (int q = 0; q < 4; ++q) jm[q] = ld128(rm, g * (3072 * 2) + i16 * 16, ((64 * mkb + 16 * w + 4 * q) * 3072 + 128 * mcg) * 2);
      cv = ((const GAS unsigned *)p.cvec)[32 * mkb + (lane & 31)];
    }
    if (b >= 144) {
      const rsrc_t ro = mk_rsrc(p.w_out, FH * DM * 2);
      const int bb = b - 144;
#pragma unroll
      for (int q = 0; q < 2; ++q) jo[q] = ld128(ro, (tid >> 4) * (DM * 2) + (tid & 15) * 16, ((32 * (bb >> 2) + 16 * q) * DM + 128 * (bb & 3)) * 2);
    }
    {
      const int po = d0 * NS + (w & 1) * 64 + lane;
      const u16 *pa[6] = {p.a_re, p.a_im, p.b_re, p.b_im, p.c_re, p.c_im};
#pragma unroll
      for (int a = 0; a < 6; ++a)
#pragma unroll
        for (int c = 0; c < 2; ++c) prm[c][a] = ((const GAS u16 *)pa[a])[po + c * NS];
#pragma unroll
      for (int c = 0; c < 2; ++c) ldt[c] = ((const GAS u16 *)p.log_dt)[d0 + c];
    }
    SB();
#pragma unroll
    for (int e = 0; e < 12; ++e) {
      const int l = 2047 - (8 * tid + e);
      const int lr = (int)rbf((float)(e < 8 ? l : max(l, 0)));
      kidx[e] = min(lr, 384 + (lr >> 2)) * 2;
      asm volatile("" : "+v"(kidx[e]));
    }
    SB();
    f32x2 xv[2][4], xn[2][4];
    {
      float st[4];
#pragma unroll
      for (int r = 0; r < 2; ++r) {
#pragma unroll
        for (int e = 0; e < 4; ++e) xv[r][e] = bf_2(xr[r][e]);
        const f32x2 s2 = (xv[r][0] + xv[r][1]) + (xv[r][2] + xv[r][3]);
        const f32x2 q2 = pk_fma(xv[r][0], xv[r][0], xv[r][1] * xv[r][1]) + pk_fma(xv[r][2], xv[r][2], xv[r][3] * xv[r][3]);
        st[2 * r] = s2[0] + s2[1];
        st[2 * r + 1] = q2[0] + q2[1];
      }
      wave_sum4(st);
#pragma unroll
      for (int r = 0; r < 2; ++r) {
        const float mean = st[2 * r] * (1.f / DM), var = __builtin_fmaf(st[2 * r + 1], 1.f / DM, -mean * mean);
        const float rsd = ln_rstd(fmaxf(var, 0.f)), mb = -rbf(mean) * rsd;
#pragma unroll
        for (int e = 0; e < 4; ++e) xn[r][e] = pk_fma(xv[r][e], f32x2{rsd, rsd}, f32x2{mb, mb});
      }
    }
    {
      u32x4 a0, a1, n0, n1;
#pragma unroll
      for (int e = 0; e < 4; ++e) {
        const unsigned lo = __builtin_amdgcn_perm(xr[1][e], xr[0][e], 0x05040100u), hi = __builtin_amdgcn_perm(xr[1][e], xr[0][e], 0x07060302u);
        const unsigned nl = pack_bf(xn[0][e][0], xn[1][e][0]), nh = pack_bf(xn[0][e][1], xn[1][e][1]);
        if (e < 2) { a0[2 * e] = lo; a0[2 * e + 1] = hi; n0[2 * e] = nl; n0[2 * e + 1] = nh; }
        else { a1[2 * e - 4] = lo; a1[2 * e - 3] = hi; n1[2 * e - 4] = nl; n1[2 * e - 3] = nh; }
      }
      LAS u32x4 *B0 = (LAS u32x4 *)(L + B_LN + w * 2048) + 2 * lane;
      B0[0] = a0; B0[1] = a1; B0[512] = n0; B0[513] = n1;
    }
    if (b < 192) {
      LAS unsigned char *MW = L + B_MW + (16 * w + g) * 272 + i16 * 16;
#pragma unroll
      for (int q = 0; q < 4; ++q) *(LAS u32x4 *)(MW + q * 4 * 272) = jm[q];
      if (w == 0 && lane < 32) {
        const f32x2 c2 = bf_2(cv);
        const f32x2 e2 = f32x2{__builtin_amdgcn_exp2f(c2[0] * -1.4426950408889634f), __builtin_amdgcn_exp2f(c2[1] * -1.4426950408889634f)} + f32x2{1.f, 1.f};
        ((LAS unsigned *)(L + B_MS))[lane] = pack2(c2 * f32x2{__builtin_amdgcn_rcpf(e2[0]), __builtin_amdgcn_rcpf(e2[1])});
      }
    }
    if (b >= 144) {
#pragma unroll
      for (int q = 0; q < 2; ++q) *(LAS u32x4 *)(L + B_WO + q * 4096 + tid * 16) = jo[q];
    }
    BAR();
    {
      LAS unsigned *S = (LAS unsigned *)(L + B_LN) + ((tid & 2) ? 0 : 2048) + 2 * (tid >> 2) + (tid & 1);
      u32x4 v[4];
#pragma unroll
      for (int a = 0; a < 4; ++a) { LAS unsigned *Sa = S + a * 128; v[a] = u32x4{Sa[0], Sa[512], Sa[1024], Sa[1536]}; }
#pragma unroll
      for (int a = 0; a < 4; ++a) st_sc1(rs, tid * 16 + a * 4096, (int)O_XT + b * 16384, v[a]);
    }
    if (b < 192) {
      u32x4 sa4[2], wb[2][2];
      LAS unsigned char *Bq = L + B_MW + (8 * g + (i16 >> 2)) * 272 + (lane & 3) * 8 + 64 * w;
#pragma unroll
      for (int ks = 0; ks < 2; ++ks) {
        sa4[ks] = *(LAS u32x4 *)(L + B_MS + ks * 64 + g * 16);
#pragma unroll
        for (int ct = 0; ct < 2; ++ct) wb[ks][ct] = cat2(tr_read(Bq + ks * 32 * 272 + ct * 32), tr_read(Bq + (ks * 32 + 4) * 272 + ct * 32));
      }
      f32x4 m0, m1;
      mma16zn(m0, sa4[0], wb[0][0]);
      mma16zn(m1, sa4[0], wb[0][1]);
      mma16n(m0, sa4[1], wb[1][0]);
      mma16n(m1, sa4[1], wb[1][1]);
      MFMA_FENCE();
      asm volatile("" :: "v"(m0), "v"(m1));
      if (lane < 32) st_sc1_32(rs, lane * 4, (int)O_MOD + (mkb * 3072 + 128 * mcg + 32 * w) * 4, f2u(g == 0 ? m0[0] : m1[0]));
    }
    if (b >= 144) {
      const int bb = b - 144;
#pragma unroll
      for (int k = 0; k < 2; ++k) {
        const int dl = 2 * w + k;
        LAS unsigned char *A = L + B_WO + (4 * g + (i16 >> 2)) * 256 + (32 * (dl >> 1) + 8 * (lane & 3) + 4 * (dl & 1)) * 2;
        st_sc1(rs, lane * 16, (int)O_WOF + ((8 * (bb & 3) + dl) * 28 + (bb >> 2)) * 1024, cat2(tr_read(A), tr_read(A + 16 * 256)));
      }
    }
    if (b == 255 && tid < 64) st_sc1_32(rs, tid * 128, C_E2 * 4, 0u);
    SB();
    {
    const int part = w >> 1;
#pragma unroll
    for (int c = 0; c < 2; ++c) {
      const float dt = rbf(expf(bf_lo(ldt[c])));
      const float lr2 = dt * bf_lo(prm[c][0]) * 1.4426950408889634f, rev = dt * bf_lo(prm[c][1]) * 0.15915494309189535f;
      const f32x2 cc = {bf_lo(prm[c][4]), bf_lo(prm[c][5])};
      const f32x2 beta = cmul(f32x2{dt * bf_lo(prm[c][2]), dt * bf_lo(prm[c][3])}, cc, crot(cc));
      const f32x2 e0 = cexp_m(lr2, rev, part ? 512.f : 0.f), e1 = cexp_m(lr2, rev, part ? 64.f : 16.f), w1 = cexp_m(lr2, rev, part ? 4.f : 1.f);
      const f32x2 e1r = crot(e1);
      z[c][0] = cmul(beta, e0, crot(e0));
      z[c][1] = cmul(z[c][0], e1, e1r);
      zm[c] = cmul(e1, e1, e1r); zmr[c] = crot(zm[c]);
      y[c][0] = f32x2{1.f, 0.f};
      y[c][1] = w1;
      ym[c] = cmul(w1, w1, crot(w1)); ymr[c] = crot(ym[c]);
    }
#pragma unroll
    for (int c = 0; c < 2; ++c)
      asm volatile("" : "+v"(z[c][0]), "+v"(z[c][1]), "+v"(zm[c]), "+v"(zmr[c]), "+v"(y[c][1]), "+v"(ym[c]), "+v"(ymr[c]));
    }
    SB();
    epoch = __builtin_amdgcn_readfirstlane(epoch_raw) + 1u;
    WAIT_VM(0);
    BAR();
    if (tid < 8) __hip_atomic_store(ctl + C_E1 + tid * 256 + b, epoch, RLX_AGENT);
    SB();
  }

  {
    FRESH_IDS();
    {
      const int part = w >> 1, n = (w & 1) * 64 + lane;
      const int s0o = (64 * rt + tile_row(w, i16)) * (SEQ * 2) + (512 * w + 8 * g) * 2;
      LAS unsigned char *T0 = L + B_KT + n * (2 * KT_ROW) + part * 64, *T1 = L + B_TR + n * (2 * KT_ROW) + part * 64;
      asm volatile("" : "+v"(T0), "+v"(T1));
#pragma unroll
      for (int grp = 0; grp < 6; ++grp) {
        u32x4 re[2], im[2];
#pragma unroll
        for (int q = 0; q < 4; ++q) {
#pragma unroll
          for (int c = 0; c < 2; ++c) {
            if (grp < 4) {
              re[c][q] = pack_bf(z[c][0][0], z[c][1][0]); im[c][q] = pack_bf(z[c][0][1], z[c][1][1]);
              z[c][0] = cmul(z[c][0], zm[c], zmr[c]); z[c][1] = cmul(z[c][1], zm[c], zmr[c]);
            } else {
              re[c][q] = pack_bf(y[c][0][0], y[c][1][0]); im[c][q] = pack_bf(-y[c][0][1], -y[c][1][1]);
              y[c][0] = cmul(y[c][0], ym[c], ymr[c]); y[c][1] = cmul(y[c][1], ym[c], ymr[c]);
            }
          }
        }
#pragma unroll
        for (int c = 0; c < 2; ++c) {
          LAS unsigned char *T = (c ? T1 : T0) + (grp < 4 ? 16 * grp : 128 - part * 32 + 16 * (grp - 4));
          *(LAS u32x4 *)T = re[c];
          *(LAS u32x4 *)(T + KT_ROW) = im[c];
        }
        stash[2 * grp][0] = ld128(tr.rw, s0o, (2 * grp) * 64);
        stash[2 * grp + 1][0] = ld128(tr.rw, s0o, (2 * grp + 1) * 64);
        SB();
      }
      BAR();
      f32x4 acc[2];
      u32x4 a[2][8], bq[2][8];
#pragma unroll
      for (int c = 0; c < 2; ++c) {
        LAS unsigned char *TA = L + (c ? B_TR : B_KT) + (8 * g + (i16 >> 2)) * KT_ROW + 8 * (lane & 3) + 32 * w;
        LAS unsigned char *TB = L + (c ? B_TR : B_KT) + (8 * g + (i16 >> 2)) * KT_ROW + 8 * (lane & 3) + 128 + 32 * (w >> 1);
#pragma unroll
        for (int ks = 0; ks < 8; ++ks) {
          a[c][ks] = cat2(tr_read(TA + ks * 32 * KT_ROW), tr_read(TA + (ks * 32 + 4) * KT_ROW));
          bq[c][ks] = cat2(tr_read(TB + ks * 32 * KT_ROW), tr_read(TB + (ks * 32 + 4) * KT_ROW));
        }
      }
      SB();
#pragma unroll
      for (int q = 12; q < 14; ++q) stash[q][0] = ld128(tr.rw, s0o, q * 64);
      mma16zn(acc[0], a[0][0], bq[0][0]);
      mma16zn(acc[1], a[1][0], bq[1][0]);
#pragma unroll
      for (int ks = 1; ks < 8; ++ks) { mma16(acc[0], a[0][ks], bq[0][ks]); mma16(acc[1], a[1][ks], bq[1][ks]); }
#pragma unroll
      for (int q = 14; q < 16; ++q) stash[q][0] = ld128(tr.rw, s0o, q * 64);
      MFMA_FENCE();
#pragma unroll
      for (int c = 0; c < 2; ++c) {
        LAS u16 *KD = (LAS u16 *)(L + B_KD + c * 2048) + (16 * w + 4 * g) * 16 + i16;
        const unsigned k01 = pack_bf(acc[c][0], acc[c][1]), k23 = pack_bf(acc[c][2], acc[c][3]);
        KD[0] = (u16)k01; KD[16] = (u16)(k01 >> 16); KD[32] = (u16)k23; KD[48] = (u16)(k23 >> 16);
      }
      if (w == 0) {
        for (unsigned spins = 0; spins < SPIN_CAP; ++spins) {
          const u32x4 f = ld_sc1(rs, lane * 16, (C_E1 + (b & 7) * 256) * 4);
          if (__all(((f[0] ^ epoch) | (f[1] ^ epoch) | (f[2] ^ epoch) | (f[3] ^ epoch)) == 0u)) break;
          __builtin_amdgcn_s_sleep(POLL_SLEEP);
        }
        __builtin_amdgcn_fence(__ATOMIC_ACQUIRE, "wavefront");
      }
      BAR();
    }
    const int ch = w >> 1, pp = w & 1;
    const int i32 = lane & 31, h = lane >> 5;
    const int wF = b < 224 ? b >> 1 : 0, wks = 8 * (b & 1) + 2 * w;
    u32x4 xn[4];
#pragma unroll
    for (int i = 0; i < 4; ++i) xn[i] = ld_sc1(rs, (lane >> 2) * 16384 + (lane & 3) * 16, (int)O_XT + (64 * w + 16 * i) * 16384 + d0 * 32);
    float mraw = u2f(ld_sc1_32(rs, (lane & 7) * (3072 * 4) + ((lane >> 4) % 3) * 2048 + ((lane >> 3) & 1) * 4, (int)O_MOD + d0 * 4));
    float cbs[2][8];
#pragma unroll
    for (int q = 0; q < 8; ++q) {
      cbs[0][q] = u2f(ld_sc1_32(rs, lane * 4, (int)O_MOD + (q * 3072 + 4 * 512 + 32 * wks) * 4));
      cbs[1][q] = u2f(ld_sc1_32(rs, lane * 4, (int)O_MOD + (q * 3072 + 3 * 512 + 32 * wks) * 4));
    }
    const rsrc_t rin = mk_rsrc(p.w_in, DM * FF * 2);
    u32x4 wr[2];
#pragma unroll
    for (int k = 0; k < 2; ++k) wr[k] = ld128(rin, (lane >> 1) * (FF * 2) + (lane & 1) * 16, (32 * (wks + k) * FF + 16 * wF) * 2);
    SB();
    {
      unsigned v[2][12];
#pragma unroll
      for (int c = 0; c < 2; ++c)
#pragma unroll
        for (int e = 0; e < 12; ++e) v[c][e] = *(LAS u16 *)(L + B_KD + c * 2048 + kidx[e]);
      SB();
#pragma unroll
      for (int c = 0; c < 2; ++c) {
#pragma unroll
        for (int e = 8; e < 12; ++e) v[c][e] = tid == 255 ? 0u : v[c][e];
        unsigned D[6];
#pragma unroll
        for (int e = 0; e < 6; ++e) D[e] = v[c][2 * e] | (v[c][2 * e + 1] << 16);
        unsigned A[5];
#pragma unroll
        for (int e = 0; e < 5; ++e) A[e] = __builtin_amdgcn_alignbit(D[e + 1], D[e], 16);
        LAS unsigned char *R = L + c * 4 * B_RC + 16 * tid;
        *(LAS u32x4 *)(R + 0 * B_RC) = u32x4{D[0], D[1], D[2], D[3]};
        *(LAS u32x4 *)(R + 1 * B_RC) = u32x4{A[0], A[1], A[2], A[3]};
        *(LAS u32x4 *)(R + 2 * B_RC) = u32x4{D[1], D[2], D[3], D[4]};
        *(LAS u32x4 *)(R + 3 * B_RC) = u32x4{A[1], A[2], A[3], A[4]};
      }
      if (tid < 32) *(LAS u32x4 *)(L + (tid >> 2) * B_RC + 4096 + 16 * (tid & 3)) = u32x4{0, 0, 0, 0};
    }
    SB();
    const float dsk = bf2f(((const CAS u16 *)p.dskip)[d0 + ch]);
    if (tid < 155) {
      *(LAS u32x4 *)(L + B_U + 16 * tid) = u32x4{0, 0, 0, 0};
      *(LAS u32x4 *)(L + B_U + U_CH + 16 * tid) = u32x4{0, 0, 0, 0};
    }
    mraw += dpp_f<0xB1>(mraw); mraw += dpp_f<0x4E>(mraw); mraw += dpp_f<0x141>(mraw);
    const unsigned mval = f2u(rbf(mraw));
    const float gaw = u2f(__builtin_amdgcn_readlane(mval, 32 + 8 * ch));
    {
      const int c = lane & 1;
      const bool isx = lane & 2;
      const float sa0 = u2f(__builtin_amdgcn_readlane(mval, 0)), sa1 = u2f(__builtin_amdgcn_readlane(mval, 8));
      const float ca0 = u2f(__builtin_amdgcn_readlane(mval, 16)), ca1 = u2f(__builtin_amdgcn_readlane(mval, 24));
      const float sa = c ? sa1 : sa0, c1 = rbf(1.f + (c ? ca1 : ca0));
      LAS unsigned char *PU = L + (isx ? B_XU + c * 5120 : B_U + c * U_CH + 31 * 80) + (16 * w + g) * 80 + 16 * ((lane >> 2) & 3);
#pragma unroll
      for (int i = 0; i < 4; ++i) {
        u32x4 hv;
#pragma unroll
        for (int e = 0; e < 4; ++e) { const unsigned m = pack2(pk_fma(bf_2(xn[i][e]), f32x2{c1, c1}, f32x2{sa, sa})); hv[e] = isx ? xn[i][e] : m; }
        *(LAS u32x4 *)(PU + i * 320) = hv;
      }
    }
    LAS unsigned char *WT = L + B_WT + w * 1024;
    *(LAS u32x4 *)(WT + lane * 16) = wr[0];
    u32x4 sbv[2];
    f32x2 c2[2][4];
    {
      float t[2];
#pragma unroll
      for (int a = 0; a < 2; ++a) t[a] = ((cbs[a][0] + cbs[a][1]) + (cbs[a][2] + cbs[a][3])) + ((cbs[a][4] + cbs[a][5]) + (cbs[a][6] + cbs[a][7]));
      LAS u16 *CS = (LAS u16 *)(L + B_CS + w * 256);
      const unsigned pk = pack_bf(1.f + rbf(t[0]), t[1]);
      CS[lane] = (u16)pk; CS[64 + lane] = (u16)(pk >> 16);
#pragma unroll
      for (int k = 0; k < 2; ++k) {
        const u32x4 cq = *(LAS u32x4 *)(L + B_CS + w * 256 + 64 * k + 16 * g);
        sbv[k] = *(LAS u32x4 *)(L + B_CS + w * 256 + 128 + 64 * k + 16 * g);
#pragma unroll
        for (int e = 0; e < 4; ++e) c2[k][e] = bf_2(cq[e]);
      }
    }
    u32x4 ones = opaque_ones();
    asm volatile("" : "+v"(sbv[0]), "+v"(sbv[1]));
    tr.begin(lane, w, rt, (w + 1) & 3);
    BAR();

    f32x16 acc0, acc1;
    u32x4 raw[2], scl[2];
    f32x4 e1, e2;
    {
      LAS unsigned char *Al = L + ch * 4 * B_RC + (3 - (i32 & 3)) * B_RC + (2044 - 4 * (i32 >> 2) + 8 * h) * 2 - 64 * pp - 128 * 15 - 2048;
      LAS unsigned char *Ah = Al + 8;
      asm volatile("" : "+v"(Ah));
      LAS unsigned char *Bb = L + B_U + ch * U_CH + 80 * i32 + 16 * h - 80 * pp;
      LAS unsigned char *WA = WT + (8 * g + (i16 >> 2)) * 32 + 8 * (lane & 3);
      u32x2 a0l[3], a0h[3], a1l[3], a1h[3];
      u32x4 b0[3], b1[3];
#define CONV_LOAD_A(n, sl)                                                                          \
  do {                                                                                              \
    const int o_ = 128 * (15 - ((n) >> 1)) + 32 * ((n) & 1);                                        \
    a0l[sl] = *(LAS u32x2 *)(Al + o_ + 2048); a0h[sl] = *(LAS u32x2 *)(Ah + o_ + 2048);             \
  } while (0)
#define CONV_LOAD_A1(n, sl)                                                                         \
  do {                                                                                              \
    const int o_ = 128 * (15 - ((n) >> 1)) + 32 * ((n) & 1);                                        \
    a1l[sl] = *(LAS u32x2 *)(Al + o_); a1h[sl] = *(LAS u32x2 *)(Ah + o_);                           \
  } while (0)
#define CONV_LOAD_B(n, sl)                                                                          \
  do {                                                                                              \
    b0[sl] = *(LAS u32x4 *)(Bb + (31 - 2 * ((n) >> 1)) * 80 + 32 * ((n) & 1));                      \
    b1[sl] = *(LAS u32x4 *)(Bb + (63 - 2 * ((n) >> 1)) * 80 + 32 * ((n) & 1));                      \
  } while (0)
#pragma unroll
      for (int n = 0; n < 2; ++n) { CONV_LOAD_A(n, n); CONV_LOAD_B(n, n); CONV_LOAD_A1(n, n); }
      SB();
#pragma unroll
      for (int n = 0; n < 32; ++n) {
        const int c = n % 3, nx = (n + 2) % 3;
        const u32x4 A0 = cat2(a0l[c], a0h[c]), A1 = cat2(a1l[c], a1h[c]);
        if (n == 0) mma32z(acc0, A0, b0[c]); else if (n == 1) mma32n(acc0, A0, b0[c]); else mma32(acc0, A0, b0[c]);
        if (n + 2 < 32) CONV_LOAD_A(n + 2, nx);
        SB();
        if (n == 0) mma32z(acc1, A0, b1[c]); else mma32(acc1, A0, b1[c]);
        if (n + 2 < 32) CONV_LOAD_B(n + 2, nx);
        SB();
        if (n < 2) mma32n(acc1, A1, b0[c]); else mma32(acc1, A1, b0[c]);
        if (n + 2 < 32) CONV_LOAD_A1(n + 2, nx);
        if (n < 16) tr.step(n);
        if (n == 2 || n == 8) raw[n == 8] = cat2(tr_read(WA), tr_read(WA + 128));
        if (n == 5) *(LAS u32x4 *)(WT + lane * 16) = wr[1];
        if (n == 4 || n == 5 || n == 10 || n == 11) {
          const int k = n >= 10, e0 = 2 * (n & 1);
          scl[k][e0] = pack2(bf_2(raw[k][e0]) * c2[k][e0]);
          scl[k][e0 + 1] = pack2(bf_2(raw[k][e0 + 1]) * c2[k][e0 + 1]);
        }
        if (n == 6) { mma16zn(e1, ones, scl[0]); mma16z(e2, sbv[0], raw[0]); }
        if (n == 12) { mma16n(e1, ones, scl[1]); mma16(e2, sbv[1], raw[1]); }
        SB();
      }
      MFMA_FENCE();
    }
    if (pp) { const f32x16 t = acc0; acc0 = acc1; acc1 = t; }
    {
      LAS f32x4 *R = (LAS f32x4 *)(L + B_RED + (ch * 2 + (1 - pp)) * 4096) + lane;
#pragma unroll
      for (int rg = 0; rg < 4; ++rg) R[rg * 64] = f32x4{acc1[4 * rg], acc1[4 * rg + 1], acc1[4 * rg + 2], acc1[4 * rg + 3]};
    }
    asm volatile("" :: "v"(e1), "v"(e2));
    if (b < 224) {
#pragma unroll
      for (int k = 0; k < 2; ++k) st_sc1(rs, lane * 16, (int)O_WP + (wF * 16 + wks + k) * 1024, scl[k]);
    }
    if (g == 0) {
      LAS float *EP = (LAS float *)(L + B_WT + 1024 * w + 768) + i16;
      EP[0] = e1[0]; EP[16] = e2[0];
    }
    BAR();
    if (b < 224 && tid < 32) {
      LAS float *EP = (LAS float *)(L + B_WT + 768) + tid;
      st_sc1_32(rs, (tid & 15) * 4 + (tid >> 4) * (FF * 4), (int)O_E12 + ((b & 1) * 2 * FF + 16 * wF) * 4, f2u((EP[0] + EP[256]) + (EP[512] + EP[768])));
    }
    {
      const int d = d0 + ch;
      LAS f32x4 *R = (LAS f32x4 *)(L + B_RED + (ch * 2 + pp) * 4096) + lane;
      LAS unsigned char *U = L + B_U + ch * U_CH + (31 + 32 * pp + i32) * 80 + 8 * h;
      LAS unsigned char *XU = L + B_XU + ch * 5120 + (32 * pp + i32) * 80 + 8 * h;
      f32x4 o[4];
      u32x2 hv[4], xe[4];
#pragma unroll
      for (int rg = 0; rg < 4; ++rg) { o[rg] = R[rg * 64]; hv[rg] = *(LAS u32x2 *)(U + 16 * rg); xe[rg] = *(LAS u32x2 *)(XU + 16 * rg); }
      SB();
      unsigned o0[4], o1[4];
      const f32x2 dk2 = {dsk, dsk}, ga2 = {gaw, gaw};
#pragma unroll
      for (int rg = 0; rg < 4; ++rg) {
        const f32x2 ya = lo2(o[rg]) + f32x2{acc0[4 * rg], acc0[4 * rg + 1]}, yb = hi2(o[rg]) + f32x2{acc0[4 * rg + 2], acc0[4 * rg + 3]};
        o0[rg] = pack2(pk_fma(ga2, pk_fma(bf_2(hv[rg].x), dk2, ya), bf_2(xe[rg].x)));
        o1[rg] = pack2(pk_fma(ga2, pk_fma(bf_2(hv[rg].y), dk2, yb), bf_2(xe[rg].y)));
      }
      const bool sw = (d >> 3) & 1;
      const int vo = (i32 >> 1) * 65536 + (i32 & 1) * 64 + 16 * h;
#pragma unroll
      for (int k = 0; k < 2; ++k) {
        auto s0 = __builtin_amdgcn_permlane32_swap(o0[2 * k], o0[2 * k + 1], false, false);
        auto s1 = __builtin_amdgcn_permlane32_swap(o1[2 * k], o1[2 * k + 1], false, false);
        const unsigned l0 = s0[0], h0 = s0[1], l1 = s1[0], h1 = s1[1];
        const u32x4 v = sw ? u32x4{h0, h1, l0, l1} : u32x4{l0, l1, h0, h1};
        st_sc1(rs, vo + 32 * k, (int)O_X1T + 16 * pp * 65536 + d * 128, v);
      }
    }
    SB();
    WAIT_VM(0);
    BAR();
    if (tid < 8) __hip_atomic_fetch_add(ctl + C_E2 + (tid * 8 + (b & 7)) * 32, 1u, RLX_AGENT);
    SB();
    transit_read(L, lane, w, stash, 1);
    tr.begin(lane, w, rt, (w + 2) & 3);
#pragma unroll
    for (int i = 0; i < 16; ++i) { tr.step(i); if (R2_SLEEP) __builtin_amdgcn_s_sleep(R2_SLEEP); }
    if (w == 0) wave_poll<true>(ctl + C_E2 + ((b & 7) * 8 + (lane & 7)) * 32, lane < 8, 32u);
    WAIT_VM(0);
    BAR();
  }

  u32x4 xres[2];
  {
    FRESH_IDS();
    const int ph2[2] = {2 * w, 2 * w + 1};
    const int ph1[2] = {w < 3 ? 2 * w : 5, w < 3 ? 2 * w + 1 : 6};
    const int Fs[4] = {56 + 7 * j + ph2[0], 56 + 7 * j + (ph2[1] < 7 ? ph2[1] : 0), 7 * j + ph1[0], 7 * j + (w == 2 ? 0 : ph1[1])};
    int wo[4];
#pragma unroll
    for (int s = 0; s < 4; ++s) wo[s] = (int)O_WP + Fs[s] * 16 * 1024 + lane * 16;
    u32x4 wf[G1_WIN][4];
    unsigned ep[4], gbv[8];
    {
      u32x4 xs[16];
      const int xo = (int)O_X1T + rt * 65536 + tid * 16;
#pragma unroll
      for (int i = 0; i < 16; ++i) xs[i] = ld_sc1(rs, xo, i * 4096);
      SB();
      transit_read(L, lane, w, stash, 2);
      SB();
      const int r0 = tid >> 3, cc = tid & 7;
      LAS unsigned char *X = L + B_X + r0 * 128 + ((cc ^ (4 * ((r0 >> 1) & 1))) * 16);
#pragma unroll
      for (int i = 0; i < 16; ++i) {
        *(LAS u32x4 *)(X + i * 4096) = xs[i];
        wf[i >> 2][i & 3] = ld_sc1(rs, wo[i & 3], (i >> 2) * 1024);
        SB();
      }
      const int tf = tid < 224 ? tid : 0, f = tf < 112 ? 112 * j + tf : FH + 112 * j + (tf - 112);
#pragma unroll
      for (int q = 0; q < 4; ++q) ep[q] = ld_sc1_32(rs, f * 4, (int)O_E12 + q * FF * 4);
#pragma unroll
      for (int q = 0; q < 8; ++q) gbv[q] = ld_sc1_32(rs, (tid & 63) * 4, (int)O_MOD + (q * 3072 + 5 * 512 + 64 * j) * 4);
      SB();
    }
    BAR();
    f32x4 acc[4][4], ssum[2], ssq[2];
    if (w < 2) gemm1_loop<0>(rs, L, lane, wo, wf, acc, ssum, ssq);
    else if (w == 2) gemm1_loop<2>(rs, L, lane, wo, wf, acc, ssum, ssq);
    else gemm1_loop<3>(rs, L, lane, wo, wf, acc, ssum, ssq);

    if (tid < 224) {
      LAS float *E = (LAS float *)(L + B_E) + tid;
      E[0] = u2f(ep[0]) + u2f(ep[2]);
      E[224] = u2f(ep[1]) + u2f(ep[3]);
    }
    if (tid < 64)
      ((LAS float *)(L + B_GB))[tid] = rbf(((u2f(gbv[0]) + u2f(gbv[1])) + (u2f(gbv[2]) + u2f(gbv[3]))) + ((u2f(gbv[4]) + u2f(gbv[5])) + (u2f(gbv[6]) + u2f(gbv[7]))));
    LAS float *SM = (LAS float *)(L + B_ST), *SR = SM + 64;
    asm volatile("" : "+v"(SM));
    SR = SM + 64;
    if (w >= 2 && g == (i16 >> 2)) {
#pragma unroll
      for (int k = 0; k < 2; ++k) {
        const int tau = (w == 2 ? 0 : 2) + k;
        const float mean = pick4(ssum[k], i16 & 3) * (1.f / DM);
        const float var = pick4(ssq[k], i16 & 3) * (1.f / DM) - mean * mean;
        const int s = tile_row(tau, i16);
        const float r = ln_rstd(fmaxf(var, 0.f));
        SM[s] = -rbf(mean) * r;
        SR[s] = r;
      }
    }
    {
      const int q = i16 >> 2, pq = lane & 3;
      LAS unsigned char *xb = L + B_X + (8 * g + q) * 128 + (((4 * (w >> 1) + pq) ^ (4 * (q >> 1))) * 16) + (((w & 1) ^ (g & 1)) * 8) + 2 * j * 4096;
#pragma unroll
      for (int dl = 0; dl < 2; ++dl) xres[dl] = cat2(tr_read(xb + dl * 4096), tr_read(xb + dl * 4096 + 512));
    }
    tr.begin(lane, w, rt, (w + 3) & 3);
    BAR();

    const int nz2 = w == 3 ? 1 : 2, nz1 = w == 2 ? 1 : 2;
    u32x4 zf[2][2];
    f32x4 e1z[2], e2z[2];
    float m1[4], r1[4];
    {
      LAS float *E1 = (LAS float *)(L + B_E);
      asm volatile("" : "+v"(E1));
      f32x4 m4[4], r4[4];
      float e1[2], e2[2];
#pragma unroll
      for (int t = 0; t < 4; ++t) { m4[t] = *(LAS f32x4 *)(SM + 32 * (t >> 1) + 8 * g + 4 * (t & 1)); r4[t] = *(LAS f32x4 *)(SR + 32 * (t >> 1) + 8 * g + 4 * (t & 1)); }
#pragma unroll
      for (int s = 0; s < 2; ++s) {
        const int p2 = s < nz2 ? ph2[s] : 0, p1 = s < nz1 ? ph1[s] : 0;
        e1[s] = E1[112 + 16 * p2 + i16]; e2[s] = E1[224 + 112 + 16 * p2 + i16];
        e1z[s] = *(LAS f32x4 *)(E1 + 16 * p1 + 4 * g); e2z[s] = *(LAS f32x4 *)(E1 + 224 + 16 * p1 + 4 * g);
      }
#pragma unroll
      for (int t = 0; t < 4; ++t) { const int sr = tile_row(t, i16); m1[t] = SM[sr]; r1[t] = SR[sr]; }
      SB();
      LAS unsigned char *ZW = L + B_ZT + i16 * 128 + 16 * g;
#pragma unroll
      for (int s = 0; s < 2; ++s) {
        if (s < nz2) {
          const f32x2 e12 = {e1[s], e1[s]}, e22 = {e2[s], e2[s]};
#pragma unroll
          for (int t = 0; t < 4; ++t) {
            const f32x4 a = acc[s][t];
            zf[s][t >> 1][2 * (t & 1)] = pack2(gelu2(pk_fma(lo2(a), lo2(r4[t]), pk_fma(lo2(m4[t]), e12, e22))));
            zf[s][t >> 1][2 * (t & 1) + 1] = pack2(gelu2(pk_fma(hi2(a), hi2(r4[t]), pk_fma(hi2(m4[t]), e12, e22))));
            if (s == 0) { tr.step(2 * t); tr.step(2 * t + 1); SB(); }
          }
#pragma unroll
          for (int kp = 0; kp < 2; ++kp) *(LAS u32x4 *)(ZW + ph2[s] * 2048 + 64 * kp) = zf[s][kp];
        }
      }
    }
    if (tid < 128) *(LAS u32x4 *)(L + B_ZT + 112 * 128 + 16 * tid) = u32x4{0, 0, 0, 0};
    BAR();
    {
      const int q = i16 >> 2, pq = lane & 3;
      const u32x4 ones = opaque_ones();
      f32x4 su, sq;
      u32x4 za[4];
      LAS unsigned char *A = L + B_ZT + (8 * g + q) * 128 + (16 * w + 4 * pq) * 2;
#pragma unroll
      for (int st = 0; st < 4; ++st) { za[st] = cat2(tr_read(A + st * 4096), tr_read(A + st * 4096 + 512)); tr.step(8 + st); }
      mma16zn(su, za[0], ones);
      mma16z(sq, za[0], za[0]);
#pragma unroll
      for (int st = 1; st < 4; ++st) { mma16(su, za[st], ones); mma16(sq, za[st], za[st]); }
#pragma unroll
      for (int st = 12; st < 16; ++st) tr.step(st);
      MFMA_FENCE();
      if (g == q) {
        const u32x4 gr = {f2u(pick4(su, i16 & 3)), epoch, f2u(pick4(sq, i16 & 3)), epoch};
        st_sc1(rs, (16 * w + i16) * 16, (int)O_GRAN + (rt * 8 + j) * 1024, gr);
      }
    }
    {
      LAS unsigned char *Z1 = L + B_Z1 + lane * 8;
      asm volatile("" : "+v"(Z1));
#pragma unroll
      for (int s = 0; s < 2; ++s) {
        if (s < nz1) {
          u32x2 z[4];
#pragma unroll
          for (int t = 0; t < 4; ++t) {
            const f32x2 m2 = {m1[t], m1[t]}, r2 = {r1[t], r1[t]};
            const f32x4 a = acc[2 + s][t];
            z[t] = u32x2{pack2(gelu2(pk_fma(lo2(a), r2, pk_fma(m2, lo2(e1z[s]), lo2(e2z[s]))))),
                         pack2(gelu2(pk_fma(hi2(a), r2, pk_fma(m2, hi2(e1z[s]), hi2(e2z[s])))))};
          }
#pragma unroll
          for (int t = 0; t < 4; ++t) *(LAS u32x2 *)(Z1 + ph1[s] * 2048 + t * 512) = z[t];
        }
      }
    }
    {
      float su, sq;
      for (unsigned spins = 0; spins < SPIN_CAP; ++spins) {
        u32x4 gr[8];
#pragma unroll
        for (int q = 0; q < 8; ++q) gr[q] = ld_sc1(rs, lane * 16, (int)O_GRAN + (rt * 8 + q) * 1024);
        unsigned bad = 0u;
        su = 0.f; sq = 0.f;
#pragma unroll
        for (int q = 0; q < 8; ++q) { bad |= (gr[q][1] ^ epoch) | (gr[q][3] ^ epoch); su += u2f(gr[q][0]); sq += u2f(gr[q][2]); }
        if (__all(bad == 0u)) break;
        __builtin_amdgcn_s_sleep(POLL_SLEEP);
      }
      const float mean = su * (1.f / FH), var = sq * (1.f / FH) - mean * mean;
      const float r = ln_rstd(fmaxf(var, 0.f));
      LAS float *ZR = (LAS float *)(L + B_ZS + w * 512);
      asm volatile("" : "+v"(ZR));
      ZR[lane] = r;
      ZR[64 + lane] = -rbf(mean) * r;
      f32x4 r4[4], m4[4];
#pragma unroll
      for (int t = 0; t < 4; ++t) { r4[t] = *(LAS f32x4 *)(ZR + 32 * (t >> 1) + 8 * g + 4 * (t & 1)); m4[t] = *(LAS f32x4 *)(ZR + 64 + 32 * (t >> 1) + 8 * g + 4 * (t & 1)); }
      SB();
#pragma unroll
      for (int s = 0; s < 2; ++s) {
        if (s < nz2) {
#pragma unroll
          for (int kp = 0; kp < 2; ++kp) {
            u32x4 o;
#pragma unroll
            for (int hf = 0; hf < 2; ++hf) {
              o[2 * hf] = pack2(pk_fma(bf_2(zf[s][kp][2 * hf]), lo2(r4[2 * kp + hf]), lo2(m4[2 * kp + hf])));
              o[2 * hf + 1] = pack2(pk_fma(bf_2(zf[s][kp][2 * hf + 1]), hi2(r4[2 * kp + hf]), hi2(m4[2 * kp + hf])));
            }
            st_sc1(rs, lane * 16, (int)O_Z2F + ((7 * j + ph2[s]) * 64 + 2 * rt + kp) * 1024, o);
          }
        }
      }
    }
    SB();
    WAIT_VM(0);
    BAR();
    if (tid == 0) __hip_atomic_store(ctl + C_F3 + (rt * 8 + j) * 32, epoch, RLX_AGENT);
    SB();
    transit_read(L, lane, w, stash, 3);
    BAR();
  }

  u32x4 wof[4][7];
  {
    FRESH_IDS();
    const float bsp = bf2f(p.b_sp[64 * rt + tile_row(w, i16)]);
    wave_poll<false>(ctl + C_F3 + ((8 * w + (lane & 7)) * 8 + j) * 32, lane < 8, epoch);
    const int ao = (int)O_Z2F + (7 * j * 64 + 16 * w) * 1024, vo = lane * 16;
    u32x4 af[G2_WIN];
#pragma unroll
    for (int n = 0; n < G2_WIN; ++n) af[n] = ld_sc1(rs, vo, ao + ((n >> 4) * 64 + (n & 15)) * 1024);
    SB();
    f32x4 acc[2][4], keep[7];
    LAS f32x4 *R = (LAS f32x4 *)L + lane;
    LAS f32x4 *Rs[3];
#pragma unroll
    for (int s = 1; s < 4; ++s) Rs[s - 1] = R + (((s + w) & 3) * 3 + (s - 1)) * 7 * 64;
#pragma unroll
    for (int ph = 0; ph < 7; ++ph) {
#pragma unroll
      for (int k = 0; k < 16; ++k) {
        const int n = ph * 16 + k;
        const u32x4 cur = af[n % G2_WIN];
#pragma unroll
        for (int s = 0; s < 4; ++s) { if (k == 0) mma16az(acc[ph & 1][s], cur, stash[k][s]); else mma16a(acc[ph & 1][s], cur, stash[k][s]); }
        if (n + G2_WIN < 112) af[n % G2_WIN] = ld_sc1(rs, vo, ao + (((n + G2_WIN) >> 4) * 64 + ((n + G2_WIN) & 15)) * 1024);
        if (ph > 0 && k >= 3 && k < 6) Rs[k - 3][(ph - 1) * 64] = acc[(ph - 1) & 1][k - 2];
        if (ph > 0 && k == 6) keep[ph - 1] = acc[(ph - 1) & 1][0];
        if (n >= 84) {
          const int m = n - 84;
          wof[m / 7][m % 7] = ld_sc1(rs, vo, (int)O_WOF + ((4 * j + m / 7) * 28 + 7 * w + m % 7) * 1024);
        }
        SB();
      }
    }
    MFMA_FENCE();
#pragma unroll
    for (int s = 1; s < 4; ++s) Rs[s - 1][6 * 64] = acc[0][s];
    keep[6] = acc[0][0];
    SB();
    BAR();
    unsigned vv[7][2];
    {
      const f32x2 b2 = {bsp, bsp};
      LAS f32x4 *Rw = R + w * 3 * 7 * 64;
      LAS unsigned char *Z1 = L + B_Z1 + (w * 64 + lane) * 8;
      asm volatile("" : "+v"(Z1));
      f32x4 ra[7][3];
      u32x2 z1[7];
#pragma unroll
      for (int ph = 0; ph < 7; ++ph) {
#pragma unroll
        for (int q = 0; q < 3; ++q) ra[ph][q] = Rw[(q * 7 + ph) * 64];
        z1[ph] = *(LAS u32x2 *)(Z1 + ph * 2048);
      }
      SB();
#pragma unroll
      for (int ph = 0; ph < 7; ++ph) {
        const f32x4 s = (keep[ph] + ra[ph][0]) + (ra[ph][1] + ra[ph][2]);
        vv[ph][0] = pack2(bf_2(z1[ph].x) * (lo2(s) + b2));
        vv[ph][1] = pack2(bf_2(z1[ph].y) * (hi2(s) + b2));
      }
    }
    const int vb = (int)O_VF + (4 * rt + w) * 28 * 1024;
    if ((j & 1) == 0) {
      const int p0 = 7 * j / 2;
#pragma unroll
      for (int k = 0; k < 3; ++k) st_sc1(rs, vo, vb + (p0 + k) * 1024, u32x4{vv[2 * k][0], vv[2 * k][1], vv[2 * k + 1][0], vv[2 * k + 1][1]});
      st_sc1_64(rs, vo, vb + (p0 + 3) * 1024, u32x2{vv[6][0], vv[6][1]});
    } else {
      const int p0 = (7 * j - 1) / 2;
      st_sc1_64(rs, vo + 8, vb + p0 * 1024, u32x2{vv[0][0], vv[0][1]});
#pragma unroll
      for (int k = 0; k < 3; ++k)
        st_sc1(rs, vo, vb + (p0 + 1 + k) * 1024, u32x4{vv[2 * k + 1][0], vv[2 * k + 1][1], vv[2 * k + 2][0], vv[2 * k + 2][1]});
    }
    SB();
    WAIT_VM(0);
    BAR();
    if (tid == 0) __hip_atomic_store(ctl + C_F4 + (rt * 8 + j) * 32, epoch, RLX_AGENT);
  }

  {
    FRESH_IDS();
    wave_poll<false>(ctl + C_F4 + (rt * 8 + 2 * w + (lane & 1)) * 32, lane < 2, epoch);
    u32x4 vf[7][4];
#pragma unroll
    for (int k = 0; k < 7; ++k)
#pragma unroll
      for (int s = 0; s < 4; ++s) vf[k][s] = ld_sc1(rs, lane * 16, (int)O_VF + ((4 * rt + ((s + w) & 3)) * 28 + 7 * w + k) * 1024);
    SB();
    f32x4 acc[4][4];
#pragma unroll
    for (int k = 0; k < 7; ++k)
#pragma unroll
      for (int s = 0; s < 4; ++s) {
#pragma unroll
        for (int dd = 0; dd < 4; ++dd) { if (k == 0) mma16az(acc[dd][s], wof[dd][k], vf[k][s]); else mma16a(acc[dd][s], wof[dd][k], vf[k][s]); }
        SB();
      }
    MFMA_FENCE();
    LAS f32x4 *R = (LAS f32x4 *)L + lane;
#pragma unroll
    for (int s = 1; s < 4; ++s) {
      LAS f32x4 *Rs = R + (((s + w) & 3) * 3 + (s - 1)) * 4 * 64;
#pragma unroll
      for (int dd = 0; dd < 4; ++dd) Rs[dd * 64] = acc[dd][s];
    }
    BAR();
    const int t = 64 * rt + tile_row(w, i16);
    {
      LAS f32x4 *Rw = R + w * 3 * 4 * 64;
      LAS unsigned char *GB = L + B_GB + 32 * g;
      asm volatile("" : "+v"(GB));
      f32x4 ra[4][3], gb[4];
#pragma unroll
      for (int dd = 0; dd < 4; ++dd) {
#pragma unroll
        for (int q = 0; q < 3; ++q) ra[dd][q] = Rw[(q * 4 + dd) * 64];
        gb[dd] = *(LAS f32x4 *)(GB + 128 * (dd >> 1) + 16 * (dd & 1));
      }
      SB();
#pragma unroll
      for (int dl = 0; dl < 2; ++dl) {
        u32x4 o;
#pragma unroll
        for (int hf = 0; hf < 2; ++hf) {
          const int dd = 2 * dl + hf;
          const f32x4 s = (acc[dd][0] + ra[dd][0]) + (ra[dd][1] + ra[dd][2]);
          o[2 * hf] = pack2(pk_fma(lo2(gb[dd]), lo2(s), bf_2(xres[dl][2 * hf])));
          o[2 * hf + 1] = pack2(pk_fma(hi2(gb[dd]), hi2(s), bf_2(xres[dl][2 * hf + 1])));
        }
        *(GAS u32x4 *)(p.out + (size_t)t * DM + 64 * j + 32 * dl + 8 * g) = o;
      }
    }
    if (b == 0 && tid == 0) __hip_atomic_store(ctl + C_EPOCH, epoch, RLX_AGENT);
  }
}

extern "C" void kernel_launch(void *const *d_in, const int *in_sizes, int n_in, void *d_out, int out_size, void *d_ws,
                              size_t ws_size, hipStream_t stream) {
  Params p;
  p.x = (const u16 *)d_in[0];
  p.a_re = (const u16 *)d_in[1]; p.a_im = (const u16 *)d_in[2];
  p.b_re = (const u16 *)d_in[3]; p.b_im = (const u16 *)d_in[4];
  p.c_re = (const u16 *)d_in[5]; p.c_im = (const u16 *)d_in[6];
  p.log_dt = (const u16 *)d_in[7]; p.dskip = (const u16 *)d_in[8];
  p.w_in = (const u16 *)d_in[10]; p.w_sp = (const u16 *)d_in[11]; p.b_sp = (const u16 *)d_in[12];
  p.w_out = (const u16 *)d_in[13]; p.cvec = (const u16 *)d_in[14]; p.w_mod = (const u16 *)d_in[15];
  p.out = (u16 *)d_out;
  p.ws = (unsigned char *)d_ws;
  (void)hipFuncSetAttribute((const void *)kb_main, hipFuncAttributeMaxDynamicSharedMemorySize, B_LDS);
  kb_main<<<256, 256, B_LDS, stream>>>(p);
}
```

```cpp
#include <hip/hip_runtime.h>
#include <stdint.h>
#include <string.h>

#define GAS __attribute__((address_space(1)))
#define LAS __attribute__((address_space(3)))
#define CAS __attribute__((address_space(4)))
#define DEVI __device__ __forceinline__
typedef unsigned short u16;
typedef unsigned long long u64;
typedef float f32x2 __attribute__((ext_vector_type(2)));
typedef float f32x4 __attribute__((ext_vector_type(4)));
typedef float f32x16 __attribute__((ext_vector_type(16)));
typedef unsigned u32x2 __attribute__((ext_vector_type(2)));
typedef unsigned u32x4 __attribute__((ext_vector_type(4)));
typedef __bf16 bf16x2_t __attribute__((ext_vector_type(2)));
typedef GAS unsigned gu32;
typedef GAS u64 gu64;
typedef __amdgpu_buffer_rsrc_t rsrc_t;
#define RLX_AGENT __ATOMIC_RELAXED, __HIP_MEMORY_SCOPE_AGENT

constexpr int SEQ = 2048, DM = 512, NS = 128, FF = 1792, FH = 896;

constexpr size_t O_GRAN = 131072;
constexpr size_t O_E12 = O_GRAN + 32 * 8 * 64 * 16;
constexpr size_t O_MOD = 1 << 20;
constexpr size_t O_XT = 2 << 20;
constexpr size_t O_X1T = 9 << 20;
constexpr size_t O_WP = 11 << 20;
constexpr size_t O_WOF = 13 << 20;
constexpr size_t O_Z2F = 14 << 20;
constexpr size_t O_VF = 18 << 20;
constexpr int WS_USED = 22 << 20;
constexpr int C_EPOCH = 0;
constexpr int C_E1 = 1024;
constexpr int C_E2 = 4096;
constexpr int C_F3 = C_E2 + 64 * 32;
constexpr int C_F4 = C_F3 + 256 * 32;
static_assert((C_F4 + 256 * 32) * 4 <= (int)O_GRAN, "control block");

struct Params {
  const u16 *x, *a_re, *a_im, *b_re, *b_im, *c_re, *c_im, *log_dt, *dskip;
  const u16 *w_in, *w_sp, *b_sp, *w_out, *cvec, *w_mod;
  u16 *out;
  unsigned char *ws;
};

#define STAMP_DECL
#define STAMP(kid, slot) do {} while (0)
#define STAMP_FLUSH(kid, n) do {} while (0)
DEVI float u2f(unsigned u) { return __builtin_bit_cast(float, u); }
DEVI unsigned f2u(float f) { return __builtin_bit_cast(unsigned, f); }
DEVI float bf_lo(unsigned u) { return u2f(u << 16); }
DEVI float bf_hi(unsigned u) { return u2f(u & 0xffff0000u); }
DEVI f32x2 bf_2(unsigned u) { return f32x2{bf_lo(u), bf_hi(u)}; }
DEVI float bf2f(u16 h) { return u2f((unsigned)h << 16); }
DEVI unsigned pack2(f32x2 v) { return __builtin_bit_cast(unsigned, __builtin_convertvector(v, bf16x2_t)); }
DEVI unsigned pack_bf(float lo, float hi) { return pack2(f32x2{lo, hi}); }
DEVI float rbf(float f) { return bf_lo(pack_bf(f, f)); }
DEVI f32x2 pk_fma(f32x2 a, f32x2 b, f32x2 c) { return __builtin_elementwise_fma(a, b, c); }
DEVI f32x2 lo2(f32x4 v) { return f32x2{v[0], v[1]}; }
DEVI f32x2 hi2(f32x4 v) { return f32x2{v[2], v[3]}; }
template <int CTRL> DEVI float dpp_f(float x) {
  return __builtin_bit_cast(float, __builtin_amdgcn_update_dpp(0, __builtin_bit_cast(int, x), CTRL, 0xf, 0xf, true));
}
DEVI float wave_sum(float x) {
  x += dpp_f<0xB1>(x);
  x += dpp_f<0x4E>(x);
  x += dpp_f<0x141>(x);
  x += dpp_f<0x140>(x);
  auto s = __builtin_amdgcn_permlane16_swap(f2u(x), f2u(x), false, false);
  x = u2f(s[0]) + u2f(s[1]);
  auto t = __builtin_amdgcn_permlane32_swap(f2u(x), f2u(x), false, false);
  return u2f(t[0]) + u2f(t[1]);
}
DEVI void wave_sum4(float (&x)[4]) {
#pragma unroll
  for (int i = 0; i < 4; ++i) x[i] += dpp_f<0xB1>(x[i]);
#pragma unroll
  for (int i = 0; i < 4; ++i) x[i] += dpp_f<0x4E>(x[i]);
#pragma unroll
  for (int i = 0; i < 4; ++i) x[i] += dpp_f<0x141>(x[i]);
#pragma unroll
  for (int i = 0; i < 4; ++i) x[i] += dpp_f<0x140>(x[i]);
#pragma unroll
  for (int i = 0; i < 4; ++i) { auto s = __builtin_amdgcn_permlane16_swap(f2u(x[i]), f2u(x[i]), false, false); x[i] = u2f(s[0]) + u2f(s[1]); }
#pragma unroll
  for (int i = 0; i < 4; ++i) { auto t = __builtin_amdgcn_permlane32_swap(f2u(x[i]), f2u(x[i]), false, false); x[i] = u2f(t[0]) + u2f(t[1]); }
}
DEVI u32x2 tr_read(LAS unsigned char *p) {
  typedef short v4i16_t __attribute__((ext_vector_type(4)));
  return __builtin_bit_cast(u32x2, __builtin_amdgcn_ds_read_tr16_b64_v4i16((LAS v4i16_t *)p));
}
DEVI u32x4 cat2(u32x2 a, u32x2 b) { return u32x4{a.x, a.y, b.x, b.y}; }
DEVI float ln_rstd(float var) {
  return __builtin_amdgcn_rcpf(rbf(__builtin_amdgcn_sqrtf(rbf(rbf(var) + 1e-5f))));
}
DEVI f32x2 gelu2(f32x2 x) {
  constexpr float K0 = -0.7978845608028654f * 2.0f * 1.4426950408889634f, K1 = K0 * 0.044715f;
  const f32x2 u = x * pk_fma(x * x, f32x2{K1, K1}, f32x2{K0, K0});
  const f32x2 d = f32x2{__builtin_amdgcn_exp2f(u[0]), __builtin_amdgcn_exp2f(u[1])} + f32x2{1.f, 1.f};
  return x * f32x2{__builtin_amdgcn_rcpf(d[0]), __builtin_amdgcn_rcpf(d[1])};
}
DEVI void block_rt_j(int b, int &rt, int &j) {
  const int x = b & 7, q = b >> 3;
  rt = 8 * (x >> 1) + (q & 7);
  j = 4 * (x & 1) + (q >> 3);
}
DEVI int tile_row(int tau, int i) { return 32 * (tau >> 1) + 8 * (i >> 2) + 4 * (tau & 1) + (i & 3); }
DEVI f32x2 cmul(f32x2 z, f32x2 v, f32x2 vr) { return pk_fma(f32x2{z[1], z[1]}, vr, f32x2{z[0], z[0]} * v); }
DEVI f32x2 crot(f32x2 v) { return f32x2{-v[1], v[0]}; }
DEVI f32x2 cexp_m(float lr2, float rev, float m) {
  const float er = __builtin_amdgcn_exp2f(lr2 * m);
  const float fr = __builtin_amdgcn_fractf(rev * m);
  return f32x2{er * __builtin_amdgcn_cosf(fr), er * __builtin_amdgcn_sinf(fr)};
}

#define SB() __builtin_amdgcn_sched_barrier(0)
#define WAIT_VM(n) asm volatile("s_waitcnt vmcnt(" #n ")" ::: "memory")
#define WAIT_LGKM0() asm volatile("s_waitcnt lgkmcnt(0)" ::: "memory")
#define BAR() asm volatile("s_waitcnt lgkmcnt(0)\n\ts_barrier" ::: "memory")
constexpr int AUX_SC1 = 16;
constexpr unsigned SPIN_CAP = 1u << 18;
#define POLL_SLEEP 2
#define R2_SLEEP 2

constexpr int B_RC = 4160;
constexpr int B_U = 8 * B_RC;
constexpr int U_CH = 95 * 80 + 16;
constexpr int B_RED = B_U + 2 * U_CH;
constexpr int KT_N = 400;
constexpr int KT_IM = 192;
constexpr int B_KT = 4 * B_RC;
constexpr int B_KD = B_KT + 53248;
constexpr int B_WT = B_KD + 4096;
constexpr int B_TR = B_WT + 4096;
constexpr int TR_WAVE = 16 * 1040;
constexpr int B_Z1 = B_TR + 4 * TR_WAVE;
constexpr int B_ST = B_Z1 + 14336;
constexpr int B_E = B_ST + 512;
constexpr int B_ZS = B_E + 2 * 224 * 4;
constexpr int B_GB = B_ZS + 2048;
constexpr int B_LDS = B_GB + 256;
constexpr int B_CS = B_Z1 + 10240;
constexpr int B_XU = B_Z1;
constexpr int B_LN = 0;
constexpr int B_WO = 16384;
constexpr int B_MW = 24576;
constexpr int B_MS = B_MW + 64 * 272;
constexpr int B_X = 0;
constexpr int B_ZT = 0;
static_assert(B_RED + 16384 <= B_WT && B_MS + 128 <= B_WT && 65536 <= B_WT && (B_KD & 15) == 0 && (B_WT & 15) == 0 &&
              4 * 3 * 7 * 1024 <= B_Z1 && B_LDS <= 163840, "lds map");
constexpr int G1_WIN = 4;
constexpr int G2_WIN = 24;

DEVI rsrc_t mk_rsrc(const void *p, int bytes) { return __builtin_amdgcn_make_buffer_rsrc((void *)p, 0, bytes, 0x00020000); }
DEVI u32x4 ld128(rsrc_t rs, int voff, int soff) { return __builtin_amdgcn_raw_buffer_load_b128(rs, voff, soff, 0); }
DEVI unsigned ld16(rsrc_t rs, int voff, int soff) { return __builtin_amdgcn_raw_buffer_load_b16(rs, voff, soff, 0); }
DEVI u32x4 ld_sc1(rsrc_t rs, int voff, int soff) { return __builtin_amdgcn_raw_buffer_load_b128(rs, voff, soff, AUX_SC1); }
DEVI u32x2 ld_sc1_64(rsrc_t rs, int voff, int soff) { return __builtin_amdgcn_raw_buffer_load_b64(rs, voff, soff, AUX_SC1); }
DEVI unsigned ld_sc1_32(rsrc_t rs, int voff, int soff) { return __builtin_amdgcn_raw_buffer_load_b32(rs, voff, soff, AUX_SC1); }
DEVI void st_sc1(rsrc_t rs, int voff, int soff, u32x4 v) { __builtin_amdgcn_raw_buffer_store_b128(v, rs, voff, soff, AUX_SC1); }
DEVI void st_sc1_64(rsrc_t rs, int voff, int soff, u32x2 v) { __builtin_amdgcn_raw_buffer_store_b64(v, rs, voff, soff, AUX_SC1); }
DEVI void st_sc1_32(rsrc_t rs, int voff, int soff, unsigned v) { __builtin_amdgcn_raw_buffer_store_b32(v, rs, voff, soff, AUX_SC1); }
DEVI float pick4(f32x4 v, int r) { return r == 0 ? v[0] : r == 1 ? v[1] : r == 2 ? v[2] : v[3]; }
template <bool GE> DEVI void wave_poll(gu32 *word, bool on, unsigned want) {
  for (unsigned spins = 0; spins < SPIN_CAP; ++spins) {
    const unsigned v = on ? __hip_atomic_load(word, RLX_AGENT) : want;
    if (__all(GE ? v >= want : v == want)) break;
    __builtin_amdgcn_s_sleep(POLL_SLEEP);
  }
  __builtin_amdgcn_fence(__ATOMIC_ACQUIRE, "wavefront");
}

DEVI void mma16(f32x4 &c, u32x4 a, u32x4 b) { asm volatile("v_mfma_f32_16x16x32_bf16 %0, %1, %2, %0" : "+v"(c) : "v"(a), "v"(b)); }
DEVI void mma16a(f32x4 &c, u32x4 a, u32x4 b) { asm volatile("v_mfma_f32_16x16x32_bf16 %0, %1, %2, %0" : "+v"(c) : "v"(a), "a"(b)); }
DEVI void mma32(f32x16 &c, u32x4 a, u32x4 b) { asm volatile("v_mfma_f32_32x32x16_bf16 %0, %1, %2, %0" : "+v"(c) : "v"(a), "v"(b)); }
DEVI void mma16z(f32x4 &c, u32x4 a, u32x4 b) { asm volatile("v_mfma_f32_16x16x32_bf16 %0, %1, %2, 0" : "=&v"(c) : "v"(a), "v"(b)); }
DEVI void mma16n(f32x4 &c, u32x4 a, u32x4 b) { asm volatile("s_nop 1\n\tv_mfma_f32_16x16x32_bf16 %0, %1, %2, %0" : "+v"(c) : "v"(a), "v"(b)); }
DEVI void mma16zn(f32x4 &c, u32x4 a, u32x4 b) { asm volatile("s_nop 1\n\tv_mfma_f32_16x16x32_bf16 %0, %1, %2, 0" : "=&v"(c) : "v"(a), "v"(b)); }
DEVI void mma16az(f32x4 &c, u32x4 a, u32x4 b) { asm volatile("v_mfma_f32_16x16x32_bf16 %0, %1, %2, 0" : "=&v"(c) : "v"(a), "a"(b)); }
DEVI void mma16A(f32x4 &c, u32x4 a, u32x4 b) { asm volatile("v_mfma_f32_16x16x32_bf16 %0, %1, %2, %0" : "+v"(c) : "a"(a), "v"(b)); }
DEVI void mma16Az(f32x4 &c, u32x4 a, u32x4 b) { asm volatile("v_mfma_f32_16x16x32_bf16 %0, %1, %2, 0" : "=&v"(c) : "a"(a), "v"(b)); }
DEVI void mma32z(f32x16 &c, u32x4 a, u32x4 b) { asm volatile("s_nop 1\n\tv_mfma_f32_32x32x16_bf16 %0, %1, %2, 0" : "=&v"(c) : "v"(a), "v"(b)); }
DEVI void mma32n(f32x16 &c, u32x4 a, u32x4 b) { asm volatile("s_nop 1\n\tv_mfma_f32_32x32x16_bf16 %0, %1, %2, %0" : "+v"(c) : "v"(a), "v"(b)); }
#define MFMA_FENCE() do { SB(); asm volatile("s_nop 7\n\ts_nop 7" ::: "memory"); SB(); } while (0)
#define FRESH_IDS()                                   \
  int tid = threadIdx.x;                              \
  asm volatile("" : "+v"(tid));                       \
  const int lane = tid & 63, i16 = lane & 15, g = lane >> 4; \
  (void)i16; (void)g
DEVI u32x4 opaque_ones() {
  u32x4 o = u32x4{0x3f803f80u, 0x3f803f80u, 0x3f803f80u, 0x3f803f80u};
  asm volatile("" : "+v"(o));
  return o;
}
struct Transit {
  rsrc_t rw;
  int v[4], so, la;
  DEVI void begin(int lane, int w, int rt, int tau) {
#pragma unroll
    for (int c = 0; c < 4; ++c) v[c] = lane * 16 + c * 4096;
    so = ((64 * rt + 32 * (tau >> 1) + 4 * (tau & 1)) * SEQ + 512 * w) * 2;
    la = B_TR + w * TR_WAVE;
  }
  DEVI void step(int i) {
    asm volatile("s_mov_b32 m0, %0\n\ts_add_u32 %0, %0, 1040\n\tbuffer_load_dwordx4 %2, %3, %1 offen lds"
                 : "+s"(la) : "s"(so), "v"(v[i & 3]), "s"(rw) : "memory", "scc");
    if ((i & 3) == 3) so += 32768;
  }
  DEVI void all() {
#pragma unroll
    for (int i = 0; i < 16; ++i) step(i);
  }
};
DEVI void transit_read(LAS unsigned char *L, int lane, int w, u32x4 (&stash)[16][4], int slot) {
  LAS unsigned char *T = L + B_TR + w * TR_WAVE + (lane & 15) * 1040 + (lane >> 4) * 16;
  asm volatile("" : "+v"(T));
#pragma unroll
  for (int k = 0; k < 16; ++k) {
    const u32x4 v = *(LAS u32x4 *)(T + 64 * k);
    if (slot == 0) stash[k][0] = v; else if (slot == 1) stash[k][1] = v; else if (slot == 2) stash[k][2] = v; else stash[k][3] = v;
  }
  WAIT_LGKM0();
}

template <int VAR>
DEVI void gemm1_loop(rsrc_t rs, LAS unsigned char *L, int lane, const int (&wo)[4], u32x4 (&wf)[G1_WIN][4], f32x4 (&acc)[4][4],
                     f32x4 (&ssum)[2], f32x4 (&ssq)[2]) {
  const int g = lane >> 4, q = (lane & 15) >> 2, pq = lane & 3;
  LAS unsigned char *xb[4];
#pragma unroll
  for (int tau = 0; tau < 4; ++tau)
    xb[tau] = L + B_X + (8 * g + q) * 128 + (((4 * (tau >> 1) + pq) ^ (4 * (q >> 1))) * 16) + (((tau & 1) ^ (g & 1)) * 8);
  constexpr bool act[4] = {true, VAR != 3, true, VAR != 2};
  const u32x4 ones = opaque_ones();
  u32x2 xl[2][4], xh[2][4];
#pragma unroll
  for (int tau = 0; tau < 4; ++tau) { xl[0][tau] = tr_read(xb[tau]); xh[0][tau] = tr_read(xb[tau] + 512); }
  SB();
#pragma unroll
  for (int ks = 0; ks < 16; ++ks) {
    const int c = ks & 1, n = c ^ 1;
    u32x4 cur[4];
#pragma unroll
    for (int s = 0; s < 4; ++s) cur[s] = wf[ks % G1_WIN][s];
#pragma unroll
    for (int tau = 0; tau < 4; ++tau) {
      const u32x4 xf = cat2(xl[c][tau], xh[c][tau]);
      if (ks == 0) mma16z(acc[0][tau], xf, cur[0]); else mma16(acc[0][tau], xf, cur[0]);
      if (ks + 1 < 16) xl[n][tau] = tr_read(xb[tau] + (ks + 1) * 4096);
      SB();
      if (act[1]) { if (ks == 0) mma16z(acc[1][tau], xf, cur[1]); else mma16(acc[1][tau], xf, cur[1]); }
      else if (tau >= 2) { if (ks == 0) mma16z(ssum[tau - 2], xf, ones); else mma16(ssum[tau - 2], xf, ones); }
      if (ks + 1 < 16) xh[n][tau] = tr_read(xb[tau] + (ks + 1) * 4096 + 512);
      SB();
      if (ks == 0) mma16z(acc[2][tau], cur[2], xf); else mma16(acc[2][tau], cur[2], xf);
      SB();
      if (act[3]) { if (ks == 0) mma16z(acc[3][tau], cur[3], xf); else mma16(acc[3][tau], cur[3], xf); }
      else if (tau < 2) { if (ks == 0) mma16z(ssum[tau], xf, ones); else mma16(ssum[tau], xf, ones); }
      if (VAR == 2 && tau < 2) { if (ks == 0) mma16z(ssq[tau], xf, xf); else mma16(ssq[tau], xf, xf); }
      if (VAR == 3 && tau >= 2) { if (ks == 0) mma16z(ssq[tau - 2], xf, xf); else mma16(ssq[tau - 2], xf, xf); }
      if (ks + G1_WIN < 16 && act[tau]) wf[ks % G1_WIN][tau] = ld_sc1(rs, wo[tau], (ks + G1_WIN) * 1024);
      SB();
    }
  }
  MFMA_FENCE();
}

__global__ __launch_bounds__(256, 1) void kb_main(Params p) {
  extern __shared__ __attribute__((aligned(16))) unsigned char smem_raw[];
  LAS unsigned char *L = (LAS unsigned char *)smem_raw;
  const int b = blockIdx.x;
  const int w = __builtin_amdgcn_readfirstlane(threadIdx.x >> 6);
  const rsrc_t rs = mk_rsrc(p.ws, WS_USED);
  gu32 *ctl = (gu32 *)p.ws;
  int rt, j;
  block_rt_j(b, rt, j);
  const int d0 = 64 * (b & 7) + 2 * (b >> 3);
  const unsigned epoch_raw = __hip_atomic_load(ctl + C_EPOCH, RLX_AGENT);
  unsigned epoch;

  u32x4 stash[16][4];
  Transit tr;
  tr.rw = mk_rsrc(p.w_sp, SEQ * SEQ * 2);

  unsigned prm[2][6], ldt[2];
  f32x2 z[2][2], zm[2], zmr[2], y[2][2], ym[2], ymr[2];
  int kidx[12];
  {
    FRESH_IDS();
    const int t0 = 8 * b;
    const rsrc_t rx = mk_rsrc(p.x, SEQ * DM * 2);
    u32x4 xr[2];
#pragma unroll
    for (int r = 0; r < 2; ++r) xr[r] = ld128(rx, lane * 16, (t0 + 2 * w + r) * (DM * 2));
    u32x4 jm[4], jo[2];
    unsigned cv = 0;
    const int mcg = b % 24, mkb = b / 24;
    if (b < 192) {
      const rsrc_t rm = mk_rsrc(p.w_mod, DM * 3072 * 2);
#pragma unroll
      for (int q = 0; q < 4; ++q) jm[q] = ld128(rm, g * (3072 * 2) + i16 * 16, ((64 * mkb + 16 * w + 4 * q) * 3072 + 128 * mcg) * 2);
      cv = ((const GAS unsigned *)p.cvec)[32 * mkb + (lane & 31)];
    }
    if (b >= 144) {
      const rsrc_t ro = mk_rsrc(p.w_out, FH * DM * 2);
      const int bb = b - 144;
#pragma unroll
      for (int q = 0; q < 2; ++q) jo[q] = ld128(ro, (tid >> 4) * (DM * 2) + (tid & 15) * 16, ((32 * (bb >> 2) + 16 * q) * DM + 128 * (bb & 3)) * 2);
    }
    {
      const int po = d0 * NS + (w & 1) * 64 + lane;
      const u16 *pa[6] = {p.a_re, p.a_im, p.b_re, p.b_im, p.c_re, p.c_im};
#pragma unroll
      for (int a = 0; a < 6; ++a)
#pragma unroll
        for (int c = 0; c < 2; ++c) prm[c][a] = ((const GAS u16 *)pa[a])[po + c * NS];
#pragma unroll
      for (int c = 0; c < 2; ++c) ldt[c] = ((const GAS u16 *)p.log_dt)[d0 + c];
    }
    SB();
    const int s0o = (64 * rt + tile_row(w, i16)) * (SEQ * 2) + (512 * w + 8 * g) * 2;
#pragma unroll
    for (int q = 0; q < 6; ++q) stash[q][0] = ld128(tr.rw, s0o, q * 64);
    SB();
#pragma unroll
    for (int e = 0; e < 12; ++e) {
      const int l = 2047 - (8 * tid + e);
      const int lr = (int)rbf((float)(e < 8 ? l : max(l, 0)));
      kidx[e] = min(lr, 384 + (lr >> 2)) * 2;
      asm volatile("" : "+v"(kidx[e]));
    }
    SB();
    f32x2 xv[2][4], xn[2][4];
    {
      float st[4];
#pragma unroll
      for (int r = 0; r < 2; ++r) {
#pragma unroll
        for (int e = 0; e < 4; ++e) xv[r][e] = bf_2(xr[r][e]);
        const f32x2 s2 = (xv[r][0] + xv[r][1]) + (xv[r][2] + xv[r][3]);
        const f32x2 q2 = pk_fma(xv[r][0], xv[r][0], xv[r][1] * xv[r][1]) + pk_fma(xv[r][2], xv[r][2], xv[r][3] * xv[r][3]);
        st[2 * r] = s2[0] + s2[1];
        st[2 * r + 1] = q2[0] + q2[1];
      }
      wave_sum4(st);
#pragma unroll
      for (int r = 0; r < 2; ++r) {
        const float mean = st[2 * r] * (1.f / DM), var = __builtin_fmaf(st[2 * r + 1], 1.f / DM, -mean * mean);
        const float rsd = ln_rstd(fmaxf(var, 0.f)), mb = -rbf(mean) * rsd;
#pragma unroll
        for (int e = 0; e < 4; ++e) xn[r][e] = pk_fma(xv[r][e], f32x2{rsd, rsd}, f32x2{mb, mb});
      }
    }
    {
      u32x4 a0, a1, n0, n1;
#pragma unroll
      for (int e = 0; e < 4; ++e) {
        const unsigned lo = __builtin_amdgcn_perm(xr[1][e], xr[0][e], 0x05040100u), hi = __builtin_amdgcn_perm(xr[1][e], xr[0][e], 0x07060302u);
        const unsigned nl = pack_bf(xn[0][e][0], xn[1][e][0]), nh = pack_bf(xn[0][e][1], xn[1][e][1]);
        if (e < 2) { a0[2 * e] = lo; a0[2 * e + 1] = hi; n0[2 * e] = nl; n0[2 * e + 1] = nh; }
        else { a1[2 * e - 4] = lo; a1[2 * e - 3] = hi; n1[2 * e - 4] = nl; n1[2 * e - 3] = nh; }
      }
      LAS u32x4 *B0 = (LAS u32x4 *)(L + B_LN + w * 2048) + 2 * lane;
      B0[0] = a0; B0[1] = a1; B0[512] = n0; B0[513] = n1;
    }
    if (b < 192) {
      LAS unsigned char *MW = L + B_MW + (16 * w + g) * 272 + i16 * 16;
#pragma unroll
      for (int q = 0; q < 4; ++q) *(LAS u32x4 *)(MW + q * 4 * 272) = jm[q];
      if (w == 0 && lane < 32) {
        const f32x2 c2 = bf_2(cv);
        const f32x2 e2 = f32x2{__builtin_amdgcn_exp2f(c2[0] * -1.4426950408889634f), __builtin_amdgcn_exp2f(c2[1] * -1.4426950408889634f)} + f32x2{1.f, 1.f};
        ((LAS unsigned *)(L + B_MS))[lane] = pack2(c2 * f32x2{__builtin_amdgcn_rcpf(e2[0]), __builtin_amdgcn_rcpf(e2[1])});
      }
    }
    if (b >= 144) {
#pragma unroll
      for (int q = 0; q < 2; ++q) *(LAS u32x4 *)(L + B_WO + q * 4096 + tid * 16) = jo[q];
    }
    BAR();
    {
      LAS unsigned *S = (LAS unsigned *)(L + B_LN) + ((tid & 2) ? 0 : 2048) + 2 * (tid >> 2) + (tid & 1);
      u32x4 v[4];
#pragma unroll
      for (int a = 0; a < 4; ++a) { LAS unsigned *Sa = S + a * 128; v[a] = u32x4{Sa[0], Sa[512], Sa[1024], Sa[1536]}; }
#pragma unroll
      for (int a = 0; a < 4; ++a) st_sc1(rs, tid * 16 + a * 4096, (int)O_XT + b * 16384, v[a]);
    }
    if (b < 192) {
      u32x4 sa4[2], wb[2][2];
      LAS unsigned char *Bq = L + B_MW + (8 * g + (i16 >> 2)) * 272 + (lane & 3) * 8 + 64 * w;
#pragma unroll
      for (int ks = 0; ks < 2; ++ks) {
        sa4[ks] = *(LAS u32x4 *)(L + B_MS + ks * 64 + g * 16);
#pragma unroll
        for (int ct = 0; ct < 2; ++ct) wb[ks][ct] = cat2(tr_read(Bq + ks * 32 * 272 + ct * 32), tr_read(Bq + (ks * 32 + 4) * 272 + ct * 32));
      }
      f32x4 m0, m1;
      mma16zn(m0, sa4[0], wb[0][0]);
      mma16zn(m1, sa4[0], wb[0][1]);
      mma16n(m0, sa4[1], wb[1][0]);
      mma16n(m1, sa4[1], wb[1][1]);
      MFMA_FENCE();
      asm volatile("" :: "v"(m0), "v"(m1));
      if (lane < 32) st_sc1_32(rs, lane * 4, (int)O_MOD + (mkb * 3072 + 128 * mcg + 32 * w) * 4, f2u(g == 0 ? m0[0] : m1[0]));
    }
    if (b >= 144) {
      const int bb = b - 144;
#pragma unroll
      for (int k = 0; k < 2; ++k) {
        const int dl = 2 * w + k;
        LAS unsigned char *A = L + B_WO + (4 * g + (i16 >> 2)) * 256 + (32 * (dl >> 1) + 8 * (lane & 3) + 4 * (dl & 1)) * 2;
        st_sc1(rs, lane * 16, (int)O_WOF + ((8 * (bb & 3) + dl) * 28 + (bb >> 2)) * 1024, cat2(tr_read(A), tr_read(A + 16 * 256)));
      }
    }
    if (b == 255 && tid < 64) st_sc1_32(rs, tid * 128, C_E2 * 4, 0u);
    SB();
    {
    const int part = w >> 1;
#pragma unroll
    for (int c = 0; c < 2; ++c) {
      const float dt = rbf(expf(bf_lo(ldt[c])));
      const float lr2 = dt * bf_lo(prm[c][0]) * 1.4426950408889634f, rev = dt * bf_lo(prm[c][1]) * 0.15915494309189535f;
      const f32x2 cc = {bf_lo(prm[c][4]), bf_lo(prm[c][5])};
      const f32x2 beta = cmul(f32x2{dt * bf_lo(prm[c][2]), dt * bf_lo(prm[c][3])}, cc, crot(cc));
      const f32x2 e0 = cexp_m(lr2, rev, part ? 512.f : 0.f), e1 = cexp_m(lr2, rev, part ? 64.f : 16.f), w1 = cexp_m(lr2, rev, part ? 4.f : 1.f);
      const f32x2 e1r = crot(e1);
      z[c][0] = cmul(beta, e0, crot(e0));
      z[c][1] = cmul(z[c][0], e1, e1r);
      zm[c] = cmul(e1, e1, e1r); zmr[c] = crot(zm[c]);
      y[c][0] = f32x2{1.f, 0.f};
      y[c][1] = w1;
      ym[c] = cmul(w1, w1, crot(w1)); ymr[c] = crot(ym[c]);
    }
#pragma unroll
    for (int c = 0; c < 2; ++c)
      asm volatile("" : "+v"(z[c][0]), "+v"(z[c][1]), "+v"(zm[c]), "+v"(zmr[c]), "+v"(y[c][1]), "+v"(ym[c]), "+v"(ymr[c]));
    }
    SB();
    epoch = __builtin_amdgcn_readfirstlane(epoch_raw) + 1u;
    WAIT_VM(0);
    BAR();
    if (tid < 8) __hip_atomic_store(ctl + C_E1 + tid * 256 + b, epoch, RLX_AGENT);
    SB();
  }

  {
    FRESH_IDS();
    {
      const int part = w >> 1, n = (w & 1) * 64 + lane;
      const int s0o = (64 * rt + tile_row(w, i16)) * (SEQ * 2) + (512 * w + 8 * g) * 2;
      LAS unsigned char *T0 = L + B_KT + n * KT_N + part * 64, *T1 = L + B_TR + n * KT_N + part * 64;
      asm volatile("" : "+v"(T0), "+v"(T1));
#pragma unroll
      for (int grp = 0; grp < 6; ++grp) {
        u32x4 re[2], im[2];
#pragma unroll
        for (int q = 0; q < 4; ++q) {
#pragma unroll
          for (int c = 0; c < 2; ++c) {
            if (grp < 4) {
              re[c][q] = pack_bf(z[c][0][0], z[c][1][0]); im[c][q] = pack_bf(z[c][0][1], z[c][1][1]);
              z[c][0] = cmul(z[c][0], zm[c], zmr[c]); z[c][1] = cmul(z[c][1], zm[c], zmr[c]);
            } else {
              re[c][q] = pack_bf(y[c][0][0], y[c][1][0]); im[c][q] = pack_bf(-y[c][0][1], -y[c][1][1]);
              y[c][0] = cmul(y[c][0], ym[c], ymr[c]); y[c][1] = cmul(y[c][1], ym[c], ymr[c]);
            }
          }
        }
#pragma unroll
        for (int c = 0; c < 2; ++c) {
          LAS unsigned char *T = (c ? T1 : T0) + (grp < 4 ? 16 * grp : 128 - part * 32 + 16 * (grp - 4));
          *(LAS u32x4 *)T = re[c];
          *(LAS u32x4 *)(T + KT_IM) = im[c];
        }
        stash[6 + grp][0] = ld128(tr.rw, s0o, (6 + grp) * 64);
        SB();
      }
      BAR();
      u32x4 e1f = ld_sc1(rs, lane * 16, (C_E1 + (b & 7) * 256) * 4);
      f32x4 acc[2];
      u32x4 a[2][8], bq[2][8];
#pragma unroll
      for (int c = 0; c < 2; ++c) {
        LAS unsigned char *TA = L + (c ? B_TR : B_KT) + (4 * g + (i16 >> 3)) * KT_N + ((i16 >> 2) & 1) * KT_IM + 8 * (lane & 3) + 32 * w;
        LAS unsigned char *TB = L + (c ? B_TR : B_KT) + (4 * g + (i16 >> 3)) * KT_N + ((i16 >> 2) & 1) * KT_IM + 8 * (lane & 3) + 128 + 32 * (w >> 1);
#pragma unroll
        for (int ks = 0; ks < 8; ++ks) {
          a[c][ks] = cat2(tr_read(TA + ks * 16 * KT_N), tr_read(TA + (ks * 16 + 2) * KT_N));
          bq[c][ks] = cat2(tr_read(TB + ks * 16 * KT_N), tr_read(TB + (ks * 16 + 2) * KT_N));
        }
      }
      SB();
#pragma unroll
      for (int q = 12; q < 14; ++q) stash[q][0] = ld128(tr.rw, s0o, q * 64);
      mma16zn(acc[0], a[0][0], bq[0][0]);
      mma16zn(acc[1], a[1][0], bq[1][0]);
#pragma unroll
      for (int ks = 1; ks < 8; ++ks) { mma16(acc[0], a[0][ks], bq[0][ks]); mma16(acc[1], a[1][ks], bq[1][ks]); }
#pragma unroll
      for (int q = 14; q < 16; ++q) stash[q][0] = ld128(tr.rw, s0o, q * 64);
      MFMA_FENCE();
#pragma unroll
      for (int c = 0; c < 2; ++c) {
        LAS u16 *KD = (LAS u16 *)(L + B_KD + c * 2048) + (16 * w + 4 * g) * 16 + i16;
        const unsigned k01 = pack_bf(acc[c][0], acc[c][1]), k23 = pack_bf(acc[c][2], acc[c][3]);
        KD[0] = (u16)k01; KD[16] = (u16)(k01 >> 16); KD[32] = (u16)k23; KD[48] = (u16)(k23 >> 16);
      }
      if (w == 0) {
        for (unsigned spins = 0; spins < SPIN_CAP; ++spins) {
          if (__all(((e1f[0] ^ epoch) | (e1f[1] ^ epoch) | (e1f[2] ^ epoch) | (e1f[3] ^ epoch)) == 0u)) break;
          __builtin_amdgcn_s_sleep(POLL_SLEEP);
          e1f = ld_sc1(rs, lane * 16, (C_E1 + (b & 7) * 256) * 4);
        }
        __builtin_amdgcn_fence(__ATOMIC_ACQUIRE, "wavefront");
      }
      BAR();
    }
    const int ch = w >> 1, pp = w & 1;
    const int i32 = lane & 31, h = lane >> 5;
    const int wF = b < 224 ? b >> 1 : 0, wks = 8 * (b & 1) + 2 * w;
    u32x4 xn[4];
#pragma unroll
    for (int i = 0; i < 4; ++i) xn[i] = ld_sc1(rs, (lane >> 2) * 16384 + (lane & 3) * 16, (int)O_XT + (64 * w + 16 * i) * 16384 + d0 * 32);
    float mraw = u2f(ld_sc1_32(rs, (lane & 7) * (3072 * 4) + ((lane >> 4) % 3) * 2048 + ((lane >> 3) & 1) * 4, (int)O_MOD + d0 * 4));
    float cbs[2][8];
#pragma unroll
    for (int q = 0; q < 8; ++q) {
      cbs[0][q] = u2f(ld_sc1_32(rs, lane * 4, (int)O_MOD + (q * 3072 + 4 * 512 + 32 * wks) * 4));
      cbs[1][q] = u2f(ld_sc1_32(rs, lane * 4, (int)O_MOD + (q * 3072 + 3 * 512 + 32 * wks) * 4));
    }
    const rsrc_t rin = mk_rsrc(p.w_in, DM * FF * 2);
    u32x4 wr[2];
#pragma unroll
    for (int k = 0; k < 2; ++k) wr[k] = ld128(rin, (lane >> 1) * (FF * 2) + (lane & 1) * 16, (32 * (wks + k) * FF + 16 * wF) * 2);
    SB();
    {
      unsigned v[2][12];
#pragma unroll
      for (int c = 0; c < 2; ++c)
#pragma unroll
        for (int e = 0; e < 12; ++e) v[c][e] = *(LAS u16 *)(L + B_KD + c * 2048 + kidx[e]);
      SB();
#pragma unroll
      for (int c = 0; c < 2; ++c) {
#pragma unroll
        for (int e = 8; e < 12; ++e) v[c][e] = tid == 255 ? 0u : v[c][e];
        unsigned D[6];
#pragma unroll
        for (int e = 0; e < 6; ++e) D[e] = v[c][2 * e] | (v[c][2 * e + 1] << 16);
        unsigned A[5];
#pragma unroll
        for (int e = 0; e < 5; ++e) A[e] = __builtin_amdgcn_alignbit(D[e + 1], D[e], 16);
        LAS unsigned char *R = L + c * 4 * B_RC + 16 * tid;
        *(LAS u32x4 *)(R + 0 * B_RC) = u32x4{D[0], D[1], D[2], D[3]};
        *(LAS u32x4 *)(R + 1 * B_RC) = u32x4{A[0], A[1], A[2], A[3]};
        *(LAS u32x4 *)(R + 2 * B_RC) = u32x4{D[1], D[2], D[3], D[4]};
        *(LAS u32x4 *)(R + 3 * B_RC) = u32x4{A[1], A[2], A[3], A[4]};
      }
      if (tid < 32) *(LAS u32x4 *)(L + (tid >> 2) * B_RC + 4096 + 16 * (tid & 3)) = u32x4{0, 0, 0, 0};
    }
    SB();
    const float dsk = bf2f(((const CAS u16 *)p.dskip)[d0 + ch]);
    if (tid < 155) {
      *(LAS u32x4 *)(L + B_U + 16 * tid) = u32x4{0, 0, 0, 0};
      *(LAS u32x4 *)(L + B_U + U_CH + 16 * tid) = u32x4{0, 0, 0, 0};
    }
    mraw += dpp_f<0xB1>(mraw); mraw += dpp_f<0x4E>(mraw); mraw += dpp_f<0x141>(mraw);
    const unsigned mval = f2u(rbf(mraw));
    const float gaw = u2f(__builtin_amdgcn_readlane(mval, 32 + 8 * ch));
    {
      const int c = lane & 1;
      const bool isx = lane & 2;
      const float sa0 = u2f(__builtin_amdgcn_readlane(mval, 0)), sa1 = u2f(__builtin_amdgcn_readlane(mval, 8));
      const float ca0 = u2f(__builtin_amdgcn_readlane(mval, 16)), ca1 = u2f(__builtin_amdgcn_readlane(mval, 24));
      const float sa = c ? sa1 : sa0, c1 = rbf(1.f + (c ? ca1 : ca0));
      LAS unsigned char *PU = L + (isx ? B_XU + c * 5120 : B_U + c * U_CH + 31 * 80) + (16 * w + g) * 80 + 16 * ((lane >> 2) & 3);
#pragma unroll
      for (int i = 0; i < 4; ++i) {
        u32x4 hv;
#pragma unroll
        for (int e = 0; e < 4; ++e) { const unsigned m = pack2(pk_fma(bf_2(xn[i][e]), f32x2{c1, c1}, f32x2{sa, sa})); hv[e] = isx ? xn[i][e] : m; }
        *(LAS u32x4 *)(PU + i * 320) = hv;
      }
    }
    LAS unsigned char *WT = L + B_WT + w * 1024;
    *(LAS u32x4 *)(WT + lane * 16) = wr[0];
    u32x4 sbv[2];
    f32x2 c2[2][4];
    {
      float t[2];
#pragma unroll
      for (int a = 0; a < 2; ++a) t[a] = ((cbs[a][0] + cbs[a][1]) + (cbs[a][2] + cbs[a][3])) + ((cbs[a][4] + cbs[a][5]) + (cbs[a][6] + cbs[a][7]));
      LAS u16 *CS = (LAS u16 *)(L + B_CS + w * 256);
      const unsigned pk = pack_bf(1.f + rbf(t[0]), t[1]);
      CS[lane] = (u16)pk; CS[64 + lane] = (u16)(pk >> 16);
#pragma unroll
      for (int k = 0; k < 2; ++k) {
        const u32x4 cq = *(LAS u32x4 *)(L + B_CS + w * 256 + 64 * k + 16 * g);
        sbv[k] = *(LAS u32x4 *)(L + B_CS + w * 256 + 128 + 64 * k + 16 * g);
#pragma unroll
        for (int e = 0; e < 4; ++e) c2[k][e] = bf_2(cq[e]);
      }
    }
    u32x4 ones = opaque_ones();
    asm volatile("" : "+v"(sbv[0]), "+v"(sbv[1]));
    tr.begin(lane, w, rt, (w + 1) & 3);
    BAR();

    f32x16 acc0, acc1;
    u32x4 raw[2], scl[2];
    f32x4 e1, e2;
    {
      LAS unsigned char *Al = L + ch * 4 * B_RC + (3 - (i32 & 3)) * B_RC + (2044 - 4 * (i32 >> 2) + 8 * h) * 2 - 64 * pp - 128 * 15 - 2048;
      LAS unsigned char *Ah = Al + 8;
      asm volatile("" : "+v"(Ah));
      LAS unsigned char *Bb = L + B_U + ch * U_CH + 80 * i32 + 16 * h - 80 * pp;
      LAS unsigned char *WA = WT + (8 * g + (i16 >> 2)) * 32 + 8 * (lane & 3);
      u32x2 a0l[3], a0h[3], a1l[3], a1h[3];
      u32x4 b0[3], b1[3];
#define CONV_LOAD_A(n, sl)                                                                          \
  do {                                                                                              \
    const int o_ = 128 * (15 - ((n) >> 1)) + 32 * ((n) & 1);                                        \
    a0l[sl] = *(LAS u32x2 *)(Al + o_ + 2048); a0h[sl] = *(LAS u32x2 *)(Ah + o_ + 2048);             \
  } while (0)
#define CONV_LOAD_A1(n, sl)                                                                         \
  do {                                                                                              \
    const int o_ = 128 * (15 - ((n) >> 1)) + 32 * ((n) & 1);                                        \
    a1l[sl] = *(LAS u32x2 *)(Al + o_); a1h[sl] = *(LAS u32x2 *)(Ah + o_);                           \
  } while (0)
#define CONV_LOAD_B(n, sl)                                                                          \
  do {                                                                                              \
    b0[sl] = *(LAS u32x4 *)(Bb + (31 - 2 * ((n) >> 1)) * 80 + 32 * ((n) & 1));                      \
    b1[sl] = *(LAS u32x4 *)(Bb + (63 - 2 * ((n) >> 1)) * 80 + 32 * ((n) & 1));                      \
  } while (0)
#pragma unroll
      for (int n = 0; n < 2; ++n) { CONV_LOAD_A(n, n); CONV_LOAD_B(n, n); CONV_LOAD_A1(n, n); }
      SB();
#pragma unroll
      for (int n = 0; n < 32; ++n) {
        const int c = n % 3, nx = (n + 2) % 3;
        const u32x4 A0 = cat2(a0l[c], a0h[c]), A1 = cat2(a1l[c], a1h[c]);
        if (n == 0) mma32z(acc0, A0, b0[c]); else if (n == 1) mma32n(acc0, A0, b0[c]); else mma32(acc0, A0, b0[c]);
        if (n + 2 < 32) CONV_LOAD_A(n + 2, nx);
        SB();
        if (n == 0) mma32z(acc1, A0, b1[c]); else mma32(acc1, A0, b1[c]);
        if (n + 2 < 32) CONV_LOAD_B(n + 2, nx);
        SB();
        if (n < 2) mma32n(acc1, A1, b0[c]); else mma32(acc1, A1, b0[c]);
        if (n + 2 < 32) CONV_LOAD_A1(n + 2, nx);
        if (n < 16) tr.step(n);
        if (n == 2 || n == 8) raw[n == 8] = cat2(tr_read(WA), tr_read(WA + 128));
        if (n == 5) *(LAS u32x4 *)(WT + lane * 16) = wr[1];
        if (n == 4 || n == 5 || n == 10 || n == 11) {
          const int k = n >= 10, e0 = 2 * (n & 1);
          scl[k][e0] = pack2(bf_2(raw[k][e0]) * c2[k][e0]);
          scl[k][e0 + 1] = pack2(bf_2(raw[k][e0 + 1]) * c2[k][e0 + 1]);
        }
        if (n == 6) { mma16zn(e1, ones, scl[0]); mma16z(e2, sbv[0], raw[0]); }
        if (n == 12) { mma16n(e1, ones, scl[1]); mma16(e2, sbv[1], raw[1]); }
        SB();
      }
      MFMA_FENCE();
    }
    if (pp) { const f32x16 t = acc0; acc0 = acc1; acc1 = t; }
    {
      LAS f32x4 *R = (LAS f32x4 *)(L + B_RED + (ch * 2 + (1 - pp)) * 4096) + lane;
#pragma unroll
      for (int rg = 0; rg < 4; ++rg) R[rg * 64] = f32x4{acc1[4 * rg], acc1[4 * rg + 1], acc1[4 * rg + 2], acc1[4 * rg + 3]};
    }
    asm volatile("" :: "v"(e1), "v"(e2));
    if (b < 224) {
#pragma unroll
      for (int k = 0; k < 2; ++k) st_sc1(rs, lane * 16, (int)O_WP + (wF * 16 + wks + k) * 1024, scl[k]);
    }
    if (g == 0) {
      LAS float *EP = (LAS float *)(L + B_WT + 1024 * w + 768) + i16;
      EP[0] = e1[0]; EP[16] = e2[0];
    }
    BAR();
    if (b < 224 && tid < 32) {
      LAS float *EP = (LAS float *)(L + B_WT + 768) + tid;
      st_sc1_32(rs, (tid & 15) * 4 + (tid >> 4) * (FF * 4), (int)O_E12 + ((b & 1) * 2 * FF + 16 * wF) * 4, f2u((EP[0] + EP[256]) + (EP[512] + EP[768])));
    }
    {
      const int d = d0 + ch;
      LAS f32x4 *R = (LAS f32x4 *)(L + B_RED + (ch * 2 + pp) * 4096) + lane;
      LAS unsigned char *U = L + B_U + ch * U_CH + (31 + 32 * pp + i32) * 80 + 8 * h;
      LAS unsigned char *XU = L + B_XU + ch * 5120 + (32 * pp + i32) * 80 + 8 * h;
      f32x4 o[4];
      u32x2 hv[4], xe[4];
#pragma unroll
      for (int rg = 0; rg < 4; ++rg) { o[rg] = R[rg * 64]; hv[rg] = *(LAS u32x2 *)(U + 16 * rg); xe[rg] = *(LAS u32x2 *)(XU + 16 * rg); }
      SB();
      unsigned o0[4], o1[4];
      const f32x2 dk2 = {dsk, dsk}, ga2 = {gaw, gaw};
#pragma unroll
      for (int rg = 0; rg < 4; ++rg) {
        const f32x2 ya = lo2(o[rg]) + f32x2{acc0[4 * rg], acc0[4 * rg + 1]}, yb = hi2(o[rg]) + f32x2{acc0[4 * rg + 2], acc0[4 * rg + 3]};
        o0[rg] = pack2(pk_fma(ga2, pk_fma(bf_2(hv[rg].x), dk2, ya), bf_2(xe[rg].x)));
        o1[rg] = pack2(pk_fma(ga2, pk_fma(bf_2(hv[rg].y), dk2, yb), bf_2(xe[rg].y)));
      }
      const bool sw = (d >> 3) & 1;
      const int vo = (i32 >> 1) * 65536 + (i32 & 1) * 64 + 16 * h;
#pragma unroll
      for (int k = 0; k < 2; ++k) {
        auto s0 = __builtin_amdgcn_permlane32_swap(o0[2 * k], o0[2 * k + 1], false, false);
        auto s1 = __builtin_amdgcn_permlane32_swap(o1[2 * k], o1[2 * k + 1], false, false);
        const unsigned l0 = s0[0], h0 = s0[1], l1 = s1[0], h1 = s1[1];
        const u32x4 v = sw ? u32x4{h0, h1, l0, l1} : u32x4{l0, l1, h0, h1};
        st_sc1(rs, vo + 32 * k, (int)O_X1T + 16 * pp * 65536 + d * 128, v);
      }
    }
    SB();
    WAIT_VM(0);
    BAR();
    if (tid < 8) __hip_atomic_fetch_add(ctl + C_E2 + (tid * 8 + (b & 7)) * 32, 1u, RLX_AGENT);
    SB();
    transit_read(L, lane, w, stash, 1);
    tr.begin(lane, w, rt, (w + 2) & 3);
#pragma unroll
    for (int i = 0; i < 16; ++i) { tr.step(i); if (R2_SLEEP) __builtin_amdgcn_s_sleep(R2_SLEEP); }
    if (w == 0) wave_poll<true>(ctl + C_E2 + ((b & 7) * 8 + (lane & 7)) * 32, lane < 8, 32u);
    WAIT_VM(0);
    BAR();
  }

  u32x4 xres[2];
  {
    FRESH_IDS();
    const int ph2[2] = {2 * w, 2 * w + 1};
    const int ph1[2] = {w < 3 ? 2 * w : 5, w < 3 ? 2 * w + 1 : 6};
    const int Fs[4] = {56 + 7 * j + ph2[0], 56 + 7 * j + (ph2[1] < 7 ? ph2[1] : 0), 7 * j + ph1[0], 7 * j + (w == 2 ? 0 : ph1[1])};
    int wo[4];
#pragma unroll
    for (int s = 0; s < 4; ++s) wo[s] = (int)O_WP + Fs[s] * 16 * 1024 + lane * 16;
    u32x4 wf[G1_WIN][4];
    unsigned ep[4], gbv[8];
    {
      u32x4 xs[16];
      const int xo = (int)O_X1T + rt * 65536 + tid * 16;
#pragma unroll
      for (int i = 0; i < 16; ++i) xs[i] = ld_sc1(rs, xo, i * 4096);
      SB();
      transit_read(L, lane, w, stash, 2);
      SB();
      const int r0 = tid >> 3, cc = tid & 7;
      LAS unsigned char *X = L + B_X + r0 * 128 + ((cc ^ (4 * ((r0 >> 1) & 1))) * 16);
#pragma unroll
      for (int i = 0; i < 16; ++i) {
        *(LAS u32x4 *)(X + i * 4096) = xs[i];
        wf[i >> 2][i & 3] = ld_sc1(rs, wo[i & 3], (i >> 2) * 1024);
        SB();
      }
      const int tf = tid < 224 ? tid : 0, f = tf < 112 ? 112 * j + tf : FH + 112 * j + (tf - 112);
#pragma unroll
      for (int q = 0; q < 4; ++q) ep[q] = ld_sc1_32(rs, f * 4, (int)O_E12 + q * FF * 4);
#pragma unroll
      for (int q = 0; q < 8; ++q) gbv[q] = ld_sc1_32(rs, (tid & 63) * 4, (int)O_MOD + (q * 3072 + 5 * 512 + 64 * j) * 4);
      SB();
    }
    BAR();
    f32x4 acc[4][4], ssum[2], ssq[2];
    if (w < 2) gemm1_loop<0>(rs, L, lane, wo, wf, acc, ssum, ssq);
    else if (w == 2) gemm1_loop<2>(rs, L, lane, wo, wf, acc, ssum, ssq);
    else gemm1_loop<3>(rs, L, lane, wo, wf, acc, ssum, ssq);

    if (tid < 224) {
      LAS float *E = (LAS float *)(L + B_E) + tid;
      E[0] = u2f(ep[0]) + u2f(ep[2]);
      E[224] = u2f(ep[1]) + u2f(ep[3]);
    }
    if (tid < 64)
      ((LAS float *)(L + B_GB))[tid] = rbf(((u2f(gbv[0]) + u2f(gbv[1])) + (u2f(gbv[2]) + u2f(gbv[3]))) + ((u2f(gbv[4]) + u2f(gbv[5])) + (u2f(gbv[6]) + u2f(gbv[7]))));
    LAS float *SM = (LAS float *)(L + B_ST), *SR = SM + 64;
    asm volatile("" : "+v"(SM));
    SR = SM + 64;
    if (w >= 2 && g == (i16 >> 2)) {
#pragma unroll
      for (int k = 0; k < 2; ++k) {
        const int tau = (w == 2 ? 0 : 2) + k;
        const float mean = pick4(ssum[k], i16 & 3) * (1.f / DM);
        const float var = pick4(ssq[k], i16 & 3) * (1.f / DM) - mean * mean;
        const int s = tile_row(tau, i16);
        const float r = ln_rstd(fmaxf(var, 0.f));
        SM[s] = -rbf(mean) * r;
        SR[s] = r;
      }
    }
    {
      const int q = i16 >> 2, pq = lane & 3;
      LAS unsigned char *xb = L + B_X + (8 * g + q) * 128 + (((4 * (w >> 1) + pq) ^ (4 * (q >> 1))) * 16) + (((w & 1) ^ (g & 1)) * 8) + 2 * j * 4096;
#pragma unroll
      for (int dl = 0; dl < 2; ++dl) xres[dl] = cat2(tr_read(xb + dl * 4096), tr_read(xb + dl * 4096 + 512));
    }
    tr.begin(lane, w, rt, (w + 3) & 3);
    BAR();

    const int nz2 = w == 3 ? 1 : 2, nz1 = w == 2 ? 1 : 2;
    u32x4 zf[2][2];
    f32x4 e1z[2], e2z[2];
    float m1[4], r1[4];
    {
      LAS float *E1 = (LAS float *)(L + B_E);
      asm volatile("" : "+v"(E1));
      f32x4 m4[4], r4[4];
      float e1[2], e2[2];
#pragma unroll
      for (int t = 0; t < 4; ++t) { m4[t] = *(LAS f32x4 *)(SM + 32 * (t >> 1) + 8 * g + 4 * (t & 1)); r4[t] = *(LAS f32x4 *)(SR + 32 * (t >> 1) + 8 * g + 4 * (t & 1)); }
#pragma unroll
      for (int s = 0; s < 2; ++s) {
        const int p2 = s < nz2 ? ph2[s] : 0, p1 = s < nz1 ? ph1[s] : 0;
        e1[s] = E1[112 + 16 * p2 + i16]; e2[s] = E1[224 + 112 + 16 * p2 + i16];
        e1z[s] = *(LAS f32x4 *)(E1 + 16 * p1 + 4 * g); e2z[s] = *(LAS f32x4 *)(E1 + 224 + 16 * p1 + 4 * g);
      }
#pragma unroll
      for (int t = 0; t < 4; ++t) { const int sr = tile_row(t, i16); m1[t] = SM[sr]; r1[t] = SR[sr]; }
      SB();
      LAS unsigned char *ZW = L + B_ZT + i16 * 128 + 16 * g;
#pragma unroll
      for (int s = 0; s < 2; ++s) {
        if (s < nz2) {
          const f32x2 e12 = {e1[s], e1[s]}, e22 = {e2[s], e2[s]};
#pragma unroll
          for (int t = 0; t < 4; ++t) {
            const f32x4 a = acc[s][t];
            zf[s][t >> 1][2 * (t & 1)] = pack2(gelu2(pk_fma(lo2(a), lo2(r4[t]), pk_fma(lo2(m4[t]), e12, e22))));
            zf[s][t >> 1][2 * (t & 1) + 1] = pack2(gelu2(pk_fma(hi2(a), hi2(r4[t]), pk_fma(hi2(m4[t]), e12, e22))));
            if (s == 0) { tr.step(2 * t); tr.step(2 * t + 1); SB(); }
          }
#pragma unroll
          for (int kp = 0; kp < 2; ++kp) *(LAS u32x4 *)(ZW + ph2[s] * 2048 + 64 * kp) = zf[s][kp];
        }
      }
    }
    if (tid < 128) *(LAS u32x4 *)(L + B_ZT + 112 * 128 + 16 * tid) = u32x4{0, 0, 0, 0};
    BAR();
    {
      const int q = i16 >> 2, pq = lane & 3;
      const u32x4 ones = opaque_ones();
      f32x4 su, sq;
      u32x4 za[4];
      LAS unsigned char *A = L + B_ZT + (8 * g + q) * 128 + (16 * w + 4 * pq) * 2;
#pragma unroll
      for (int st = 0; st < 4; ++st) { za[st] = cat2(tr_read(A + st * 4096), tr_read(A + st * 4096 + 512)); tr.step(8 + st); }
      mma16zn(su, za[0], ones);
      mma16z(sq, za[0], za[0]);
#pragma unroll
      for (int st = 1; st < 4; ++st) { mma16(su, za[st], ones); mma16(sq, za[st], za[st]); }
#pragma unroll
      for (int st = 12; st < 16; ++st) tr.step(st);
      MFMA_FENCE();
      if (g == q) {
        const u32x4 gr = {f2u(pick4(su, i16 & 3)), epoch, f2u(pick4(sq, i16 & 3)), epoch};
        st_sc1(rs, (16 * w + i16) * 16, (int)O_GRAN + (rt * 8 + j) * 1024, gr);
      }
    }
    {
      LAS unsigned char *Z1 = L + B_Z1 + lane * 8;
      asm volatile("" : "+v"(Z1));
#pragma unroll
      for (int s = 0; s < 2; ++s) {
        if (s < nz1) {
          u32x2 z[4];
#pragma unroll
          for (int t = 0; t < 4; ++t) {
            const f32x2 m2 = {m1[t], m1[t]}, r2 = {r1[t], r1[t]};
            const f32x4 a = acc[2 + s][t];
            z[t] = u32x2{pack2(gelu2(pk_fma(lo2(a), r2, pk_fma(m2, lo2(e1z[s]), lo2(e2z[s]))))),
                         pack2(gelu2(pk_fma(hi2(a), r2, pk_fma(m2, hi2(e1z[s]), hi2(e2z[s])))))};
          }
#pragma unroll
          for (int t = 0; t < 4; ++t) *(LAS u32x2 *)(Z1 + ph1[s] * 2048 + t * 512) = z[t];
        }
      }
    }
    {
      float su, sq;
      for (unsigned spins = 0; spins < SPIN_CAP; ++spins) {
        u32x4 gr[8];
#pragma unroll
        for (int q = 0; q < 8; ++q) gr[q] = ld_sc1(rs, lane * 16, (int)O_GRAN + (rt * 8 + q) * 1024);
        unsigned bad = 0u;
        su = 0.f; sq = 0.f;
#pragma unroll
        for (int q = 0; q < 8; ++q) { bad |= (gr[q][1] ^ epoch) | (gr[q][3] ^ epoch); su += u2f(gr[q][0]); sq += u2f(gr[q][2]); }
        if (__all(bad == 0u)) break;
        __builtin_amdgcn_s_sleep(POLL_SLEEP);
      }
      const float mean = su * (1.f / FH), var = sq * (1.f / FH) - mean * mean;
      const float r = ln_rstd(fmaxf(var, 0.f));
      LAS float *ZR = (LAS float *)(L + B_ZS + w * 512);
      asm volatile("" : "+v"(ZR));
      ZR[lane] = r;
      ZR[64 + lane] = -rbf(mean) * r;
      f32x4 r4[4], m4[4];
#pragma unroll
      for (int t = 0; t < 4; ++t) { r4[t] = *(LAS f32x4 *)(ZR + 32 * (t >> 1) + 8 * g + 4 * (t & 1)); m4[t] = *(LAS f32x4 *)(ZR + 64 + 32 * (t >> 1) + 8 * g + 4 * (t & 1)); }
      SB();
#pragma unroll
      for (int s = 0; s < 2; ++s) {
        if (s < nz2) {
#pragma unroll
          for (int kp = 0; kp < 2; ++kp) {
            u32x4 o;
#pragma unroll
            for (int hf = 0; hf < 2; ++hf) {
              o[2 * hf] = pack2(pk_fma(bf_2(zf[s][kp][2 * hf]), lo2(r4[2 * kp + hf]), lo2(m4[2 * kp + hf])));
              o[2 * hf + 1] = pack2(pk_fma(bf_2(zf[s][kp][2 * hf + 1]), hi2(r4[2 * kp + hf]), hi2(m4[2 * kp + hf])));
            }
            st_sc1(rs, lane * 16, (int)O_Z2F + ((7 * j + ph2[s]) * 64 + 2 * rt + kp) * 1024, o);
          }
        }
      }
    }
    SB();
    WAIT_VM(0);
    BAR();
    if (tid == 0) __hip_atomic_store(ctl + C_F3 + (rt * 8 + j) * 32, epoch, RLX_AGENT);
    SB();
    transit_read(L, lane, w, stash, 3);
    BAR();
  }

  u32x4 wof[4][7];
  {
    FRESH_IDS();
    const float bsp = bf2f(p.b_sp[64 * rt + tile_row(w, i16)]);
    wave_poll<false>(ctl + C_F3 + ((8 * w + (lane & 7)) * 8 + j) * 32, lane < 8, epoch);
    const int ao = (int)O_Z2F + (7 * j * 64 + 16 * w) * 1024, vo = lane * 16;
    u32x4 af[G2_WIN];
#pragma unroll
    for (int n = 0; n < G2_WIN; ++n) af[n] = ld_sc1(rs, vo, ao + ((n >> 4) * 64 + (n & 15)) * 1024);
    SB();
    f32x4 acc[2][4], keep[7];
    LAS f32x4 *R = (LAS f32x4 *)L + lane;
    LAS f32x4 *Rs[3];
#pragma unroll
    for (int s = 1; s < 4; ++s) Rs[s - 1] = R + (((s + w) & 3) * 3 + (s - 1)) * 7 * 64;
#pragma unroll
    for (int ph = 0; ph < 7; ++ph) {
#pragma unroll
      for (int k = 0; k < 16; ++k) {
        const int n = ph * 16 + k;
        const u32x4 cur = af[n % G2_WIN];
#pragma unroll
        for (int s = 0; s < 4; ++s) { if (k == 0) mma16az(acc[ph & 1][s], cur, stash[k][s]); else mma16a(acc[ph & 1][s], cur, stash[k][s]); }
        if (n + G2_WIN < 112) af[n % G2_WIN] = ld_sc1(rs, vo, ao + (((n + G2_WIN) >> 4) * 64 + ((n + G2_WIN) & 15)) * 1024);
        if (ph > 0 && k >= 3 && k < 6) Rs[k - 3][(ph - 1) * 64] = acc[(ph - 1) & 1][k - 2];
        if (ph > 0 && k == 6) keep[ph - 1] = acc[(ph - 1) & 1][0];
        if (n >= 84) {
          const int m = n - 84;
          wof[m / 7][m % 7] = ld_sc1(rs, vo, (int)O_WOF + ((4 * j + m / 7) * 28 + 7 * w + m % 7) * 1024);
        }
        SB();
      }
    }
    MFMA_FENCE();
#pragma unroll
    for (int s = 1; s < 4; ++s) Rs[s - 1][6 * 64] = acc[0][s];
    keep[6] = acc[0][0];
    SB();
    BAR();
    unsigned vv[7][2];
    {
      const f32x2 b2 = {bsp, bsp};
      LAS f32x4 *Rw = R + w * 3 * 7 * 64;
      LAS unsigned char *Z1 = L + B_Z1 + (w * 64 + lane) * 8;
      asm volatile("" : "+v"(Z1));
      f32x4 ra[7][3];
      u32x2 z1[7];
#pragma unroll
      for (int ph = 0; ph < 7; ++ph) {
#pragma unroll
        for (int q = 0; q < 3; ++q) ra[ph][q] = Rw[(q * 7 + ph) * 64];
        z1[ph] = *(LAS u32x2 *)(Z1 + ph * 2048);
      }
      SB();
#pragma unroll
      for (int ph = 0; ph < 7; ++ph) {
        const f32x4 s = (keep[ph] + ra[ph][0]) + (ra[ph][1] + ra[ph][2]);
        vv[ph][0] = pack2(bf_2(z1[ph].x) * (lo2(s) + b2));
        vv[ph][1] = pack2(bf_2(z1[ph].y) * (hi2(s) + b2));
      }
    }
    const int vb = (int)O_VF + (4 * rt + w) * 28 * 1024;
    if ((j & 1) == 0) {
      const int p0 = 7 * j / 2;
#pragma unroll
      for (int k = 0; k < 3; ++k) st_sc1(rs, vo, vb + (p0 + k) * 1024, u32x4{vv[2 * k][0], vv[2 * k][1], vv[2 * k + 1][0], vv[2 * k + 1][1]});
      st_sc1_64(rs, vo, vb + (p0 + 3) * 1024, u32x2{vv[6][0], vv[6][1]});
    } else {
      const int p0 = (7 * j - 1) / 2;
      st_sc1_64(rs, vo + 8, vb + p0 * 1024, u32x2{vv[0][0], vv[0][1]});
#pragma unroll
      for (int k = 0; k < 3; ++k)
        st_sc1(rs, vo, vb + (p0 + 1 + k) * 1024, u32x4{vv[2 * k + 1][0], vv[2 * k + 1][1], vv[2 * k + 2][0], vv[2 * k + 2][1]});
    }
    SB();
    WAIT_VM(0);
    BAR();
    if (tid == 0) __hip_atomic_store(ctl + C_F4 + (rt * 8 + j) * 32, epoch, RLX_AGENT);
  }

  {
    FRESH_IDS();
    wave_poll<false>(ctl + C_F4 + (rt * 8 + 2 * w + (lane & 1)) * 32, lane < 2, epoch);
    u32x4 vf[7][4];
#pragma unroll
    for (int k = 0; k < 7; ++k)
#pragma unroll
      for (int s = 0; s < 4; ++s) vf[k][s] = ld_sc1(rs, lane * 16, (int)O_VF + ((4 * rt + ((s + w) & 3)) * 28 + 7 * w + k) * 1024);
    SB();
    f32x4 acc[4][4];
#pragma unroll
    for (int k = 0; k < 7; ++k)
#pragma unroll
      for (int s = 0; s < 4; ++s) {
#pragma unroll
        for (int dd = 0; dd < 4; ++dd) { if (k == 0) mma16az(acc[dd][s], wof[dd][k], vf[k][s]); else mma16a(acc[dd][s], wof[dd][k], vf[k][s]); }
        SB();
      }
    MFMA_FENCE();
    LAS f32x4 *R = (LAS f32x4 *)L + lane;
#pragma unroll
    for (int s = 1; s < 4; ++s) {
      LAS f32x4 *Rs = R + (((s + w) & 3) * 3 + (s - 1)) * 4 * 64;
#pragma unroll
      for (int dd = 0; dd < 4; ++dd) Rs[dd * 64] = acc[dd][s];
    }
    BAR();
    const int t = 64 * rt + tile_row(w, i16);
    {
      LAS f32x4 *Rw = R + w * 3 * 4 * 64;
      LAS unsigned char *GB = L + B_GB + 32 * g;
      asm volatile("" : "+v"(GB));
      f32x4 ra[4][3], gb[4];
#pragma unroll
      for (int dd = 0; dd < 4; ++dd) {
#pragma unroll
        for (int q = 0; q < 3; ++q) ra[dd][q] = Rw[(q * 4 + dd) * 64];
        gb[dd] = *(LAS f32x4 *)(GB + 128 * (dd >> 1) + 16 * (dd & 1));
      }
      SB();
#pragma unroll
      for (int dl = 0; dl < 2; ++dl) {
        u32x4 o;
#pragma unroll
        for (int hf = 0; hf < 2; ++hf) {
          const int dd = 2 * dl + hf;
          const f32x4 s = (acc[dd][0] + ra[dd][0]) + (ra[dd][1] + ra[dd][2]);
          o[2 * hf] = pack2(pk_fma(lo2(gb[dd]), lo2(s), bf_2(xres[dl][2 * hf])));
          o[2 * hf + 1] = pack2(pk_fma(hi2(gb[dd]), hi2(s), bf_2(xres[dl][2 * hf + 1])));
        }
        *(GAS u32x4 *)(p.out + (size_t)t * DM + 64 * j + 32 * dl + 8 * g) = o;
      }
    }
    if (b == 0 && tid == 0) __hip_atomic_store(ctl + C_EPOCH, epoch, RLX_AGENT);
  }
}

extern "C" void kernel_launch(void *const *d_in, const int *in_sizes, int n_in, void *d_out, int out_size, void *d_ws,
                              size_t ws_size, hipStream_t stream) {
  Params p;
  p.x = (const u16 *)d_in[0];
  p.a_re = (const u16 *)d_in[1]; p.a_im = (const u16 *)d_in[2];
  p.b_re = (const u16 *)d_in[3]; p.b_im = (const u16 *)d_in[4];
  p.c_re = (const u16 *)d_in[5]; p.c_im = (const u16 *)d_in[6];
  p.log_dt = (const u16 *)d_in[7]; p.dskip = (const u16 *)d_in[8];
  p.w_in = (const u16 *)d_in[10]; p.w_sp = (const u16 *)d_in[11]; p.b_sp = (const u16 *)d_in[12];
  p.w_out = (const u16 *)d_in[13]; p.cvec = (const u16 *)d_in[14]; p.w_mod = (const u16 *)d_in[15];
  p.out = (u16 *)d_out;
  p.ws = (unsigned char *)d_ws;
  (void)hipFuncSetAttribute((const void *)kb_main, hipFuncAttributeMaxDynamicSharedMemorySize, B_LDS);
  kb_main<<<256, 256, B_LDS, stream>>>(p);
}
```

```cpp
#include <hip/hip_runtime.h>
#include <stdint.h>
#include <string.h>

#define GAS __attribute__((address_space(1)))
#define LAS __attribute__((address_space(3)))
#define CAS __attribute__((address_space(4)))
#define DEVI __device__ __forceinline__
typedef unsigned short u16;
typedef unsigned long long u64;
typedef float f32x2 __attribute__((ext_vector_type(2)));
typedef float f32x4 __attribute__((ext_vector_type(4)));
typedef float f32x16 __attribute__((ext_vector_type(16)));
typedef unsigned u32x2 __attribute__((ext_vector_type(2)));
typedef unsigned u32x4 __attribute__((ext_vector_type(4)));
typedef __bf16 bf16x2_t __attribute__((ext_vector_type(2)));
typedef GAS unsigned gu32;
typedef GAS u64 gu64;
typedef __amdgpu_buffer_rsrc_t rsrc_t;
#define RLX_AGENT __ATOMIC_RELAXED, __HIP_MEMORY_SCOPE_AGENT

constexpr int SEQ = 2048, DM = 512, NS = 128, FF = 1792, FH = 896;

constexpr size_t O_GRAN = 131072;
constexpr size_t O_E12 = O_GRAN + 32 * 8 * 64 * 16;
constexpr size_t O_MOD = 1 << 20;
constexpr size_t O_XT = 2 << 20;
constexpr size_t O_X1T = 9 << 20;
constexpr size_t O_WP = 11 << 20;
constexpr size_t O_WOF = 13 << 20;
constexpr size_t O_Z2F = 14 << 20;
constexpr size_t O_VF = 18 << 20;
constexpr size_t O_VH = 8 << 20;
constexpr int WS_USED = 22 << 20;
constexpr int C_EPOCH = 0;
constexpr int C_E1 = 1024;
constexpr int C_E2 = 4096;
constexpr int C_F3 = C_E2 + 64 * 32;
constexpr int C_F4 = C_F3 + 256 * 32;
static_assert((C_F4 + 256 * 32) * 4 <= (int)O_GRAN, "control block");

struct Params {
  const u16 *x, *a_re, *a_im, *b_re, *b_im, *c_re, *c_im, *log_dt, *dskip;
  const u16 *w_in, *w_sp, *b_sp, *w_out, *cvec, *w_mod;
  u16 *out;
  unsigned char *ws;
};

#define STAMP_DECL
#define STAMP(kid, slot) do {} while (0)
#define STAMP_FLUSH(kid, n) do {} while (0)
DEVI float u2f(unsigned u) { return __builtin_bit_cast(float, u); }
DEVI unsigned f2u(float f) { return __builtin_bit_cast(unsigned, f); }
DEVI float bf_lo(unsigned u) { return u2f(u << 16); }
DEVI float bf_hi(unsigned u) { return u2f(u & 0xffff0000u); }
DEVI f32x2 bf_2(unsigned u) { return f32x2{bf_lo(u), bf_hi(u)}; }
DEVI float bf2f(u16 h) { return u2f((unsigned)h << 16); }
DEVI unsigned pack2(f32x2 v) { return __builtin_bit_cast(unsigned, __builtin_convertvector(v, bf16x2_t)); }
DEVI unsigned pack_bf(float lo, float hi) { return pack2(f32x2{lo, hi}); }
DEVI float rbf(float f) { return bf_lo(pack_bf(f, f)); }
DEVI f32x2 pk_fma(f32x2 a, f32x2 b, f32x2 c) { return __builtin_elementwise_fma(a, b, c); }
DEVI f32x2 lo2(f32x4 v) { return f32x2{v[0], v[1]}; }
DEVI f32x2 hi2(f32x4 v) { return f32x2{v[2], v[3]}; }
template <int CTRL> DEVI float dpp_f(float x) {
  return __builtin_bit_cast(float, __builtin_amdgcn_update_dpp(0, __builtin_bit_cast(int, x), CTRL, 0xf, 0xf, true));
}
DEVI float wave_sum(float x) {
  x += dpp_f<0xB1>(x);
  x += dpp_f<0x4E>(x);
  x += dpp_f<0x141>(x);
  x += dpp_f<0x140>(x);
  auto s = __builtin_amdgcn_permlane16_swap(f2u(x), f2u(x), false, false);
  x = u2f(s[0]) + u2f(s[1]);
  auto t = __builtin_amdgcn_permlane32_swap(f2u(x), f2u(x), false, false);
  return u2f(t[0]) + u2f(t[1]);
}
DEVI void wave_sum4(float (&x)[4]) {
#pragma unroll
  for (int i = 0; i < 4; ++i) x[i] += dpp_f<0xB1>(x[i]);
#pragma unroll
  for (int i = 0; i < 4; ++i) x[i] += dpp_f<0x4E>(x[i]);
#pragma unroll
  for (int i = 0; i < 4; ++i) x[i] += dpp_f<0x141>(x[i]);
#pragma unroll
  for (int i = 0; i < 4; ++i) x[i] += dpp_f<0x140>(x[i]);
#pragma unroll
  for (int i = 0; i < 4; ++i) { auto s = __builtin_amdgcn_permlane16_swap(f2u(x[i]), f2u(x[i]), false, false); x[i] = u2f(s[0]) + u2f(s[1]); }
#pragma unroll
  for (int i = 0; i < 4; ++i) { auto t = __builtin_amdgcn_permlane32_swap(f2u(x[i]), f2u(x[i]), false, false); x[i] = u2f(t[0]) + u2f(t[1]); }
}
DEVI u32x2 tr_read(LAS unsigned char *p) {
  typedef short v4i16_t __attribute__((ext_vector_type(4)));
  return __builtin_bit_cast(u32x2, __builtin_amdgcn_ds_read_tr16_b64_v4i16((LAS v4i16_t *)p));
}
DEVI u32x4 cat2(u32x2 a, u32x2 b) { return u32x4{a.x, a.y, b.x, b.y}; }
DEVI float ln_rstd(float var) {
  return __builtin_amdgcn_rcpf(rbf(__builtin_amdgcn_sqrtf(rbf(rbf(var) + 1e-5f))));
}
DEVI f32x2 gelu2(f32x2 x) {
  constexpr float K0 = -0.7978845608028654f * 2.0f * 1.4426950408889634f, K1 = K0 * 0.044715f;
  const f32x2 u = x * pk_fma(x * x, f32x2{K1, K1}, f32x2{K0, K0});
  const f32x2 d = f32x2{__builtin_amdgcn_exp2f(u[0]), __builtin_amdgcn_exp2f(u[1])} + f32x2{1.f, 1.f};
  return x * f32x2{__builtin_amdgcn_rcpf(d[0]), __builtin_amdgcn_rcpf(d[1])};
}
DEVI void block_rt_j(int b, int &rt, int &j) {
  const int x = b & 7, q = b >> 3;
  rt = 8 * (x >> 1) + (q & 7);
  j = 4 * (x & 1) + (q >> 3);
}
DEVI int tile_row(int tau, int i) { return 32 * (tau >> 1) + 8 * (i >> 2) + 4 * (tau & 1) + (i & 3); }
DEVI f32x2 cmul(f32x2 z, f32x2 v, f32x2 vr) { return pk_fma(f32x2{z[1], z[1]}, vr, f32x2{z[0], z[0]} * v); }
DEVI f32x2 crot(f32x2 v) { return f32x2{-v[1], v[0]}; }
DEVI f32x2 cexp_m(float lr2, float rev, float m) {
  const float er = __builtin_amdgcn_exp2f(lr2 * m);
  const float fr = __builtin_amdgcn_fractf(rev * m);
  return f32x2{er * __builtin_amdgcn_cosf(fr), er * __builtin_amdgcn_sinf(fr)};
}

#define SB() __builtin_amdgcn_sched_barrier(0)
#define WAIT_VM(n) asm volatile("s_waitcnt vmcnt(" #n ")" ::: "memory")
#define WAIT_LGKM0() asm volatile("s_waitcnt lgkmcnt(0)" ::: "memory")
#define BAR() asm volatile("s_waitcnt lgkmcnt(0)\n\ts_barrier" ::: "memory")
constexpr int AUX_SC1 = 16;
constexpr unsigned SPIN_CAP = 1u << 18;
#define POLL_SLEEP 2
#define R2_SLEEP 2

constexpr int B_RC = 4160;
constexpr int B_U = 8 * B_RC;
constexpr int U_CH = 95 * 80 + 16;
constexpr int B_RED = B_U + 2 * U_CH;
constexpr int KT_N = 400;
constexpr int KT_IM = 192;
constexpr int B_KT = 4 * B_RC;
constexpr int B_KD = B_KT + 53248;
constexpr int B_WT = B_KD + 4096;
constexpr int B_TR = B_WT + 4096;
constexpr int TR_WAVE = 16 * 1040;
constexpr int B_Z1 = B_TR + 4 * TR_WAVE;
constexpr int B_ST = B_Z1 + 14336;
constexpr int B_E = B_ST + 512;
constexpr int B_ZS = B_E + 2 * 224 * 4;
constexpr int B_GB = B_ZS + 2048;
constexpr int B_LDS = B_GB + 256;
constexpr int B_CS = B_Z1 + 10240;
constexpr int B_XU = B_Z1;
constexpr int B_LN = 0;
constexpr int B_WO = 16384;
constexpr int B_MW = 24576;
constexpr int B_MS = B_MW + 64 * 272;
constexpr int B_X = 0;
constexpr int B_ZT = 0;
static_assert(B_RED + 16384 <= B_WT && B_MS + 128 <= B_WT && 65536 <= B_WT && (B_KD & 15) == 0 && (B_WT & 15) == 0 &&
              4 * 3 * 7 * 1024 <= B_Z1 && B_LDS <= 163840, "lds map");
constexpr int G1_WIN = 4;
constexpr int G2_WIN = 24;

DEVI rsrc_t mk_rsrc(const void *p, int bytes) { return __builtin_amdgcn_make_buffer_rsrc((void *)p, 0, bytes, 0x00020000); }
DEVI u32x4 ld128(rsrc_t rs, int voff, int soff) { return __builtin_amdgcn_raw_buffer_load_b128(rs, voff, soff, 0); }
DEVI unsigned ld16(rsrc_t rs, int voff, int soff) { return __builtin_amdgcn_raw_buffer_load_b16(rs, voff, soff, 0); }
DEVI u32x4 ld_sc1(rsrc_t rs, int voff, int soff) { return __builtin_amdgcn_raw_buffer_load_b128(rs, voff, soff, AUX_SC1); }
DEVI u32x2 ld_sc1_64(rsrc_t rs, int voff, int soff) { return __builtin_amdgcn_raw_buffer_load_b64(rs, voff, soff, AUX_SC1); }
DEVI unsigned ld_sc1_32(rsrc_t rs, int voff, int soff) { return __builtin_amdgcn_raw_buffer_load_b32(rs, voff, soff, AUX_SC1); }
DEVI void st_sc1(rsrc_t rs, int voff, int soff, u32x4 v) { __builtin_amdgcn_raw_buffer_store_b128(v, rs, voff, soff, AUX_SC1); }
DEVI void st_sc1_64(rsrc_t rs, int voff, int soff, u32x2 v) { __builtin_amdgcn_raw_buffer_store_b64(v, rs, voff, soff, AUX_SC1); }
DEVI void st_sc1_32(rsrc_t rs, int voff, int soff, unsigned v) { __builtin_amdgcn_raw_buffer_store_b32(v, rs, voff, soff, AUX_SC1); }
DEVI float pick4(f32x4 v, int r) { return r == 0 ? v[0] : r == 1 ? v[1] : r == 2 ? v[2] : v[3]; }
template <bool GE> DEVI void wave_poll(gu32 *word, bool on, unsigned want) {
  for (unsigned spins = 0; spins < SPIN_CAP; ++spins) {
    const unsigned v = on ? __hip_atomic_load(word, RLX_AGENT) : want;
    if (__all(GE ? v >= want : v == want)) break;
    __builtin_amdgcn_s_sleep(POLL_SLEEP);
  }
  __builtin_amdgcn_fence(__ATOMIC_ACQUIRE, "wavefront");
}

DEVI void mma16(f32x4 &c, u32x4 a, u32x4 b) { asm volatile("v_mfma_f32_16x16x32_bf16 %0, %1, %2, %0" : "+v"(c) : "v"(a), "v"(b)); }
DEVI void mma16a(f32x4 &c, u32x4 a, u32x4 b) { asm volatile("v_mfma_f32_16x16x32_bf16 %0, %1, %2, %0" : "+v"(c) : "v"(a), "a"(b)); }
DEVI void mma32(f32x16 &c, u32x4 a, u32x4 b) { asm volatile("v_mfma_f32_32x32x16_bf16 %0, %1, %2, %0" : "+v"(c) : "v"(a), "v"(b)); }
DEVI void mma16z(f32x4 &c, u32x4 a, u32x4 b) { asm volatile("v_mfma_f32_16x16x32_bf16 %0, %1, %2, 0" : "=&v"(c) : "v"(a), "v"(b)); }
DEVI void mma16n(f32x4 &c, u32x4 a, u32x4 b) { asm volatile("s_nop 1\n\tv_mfma_f32_16x16x32_bf16 %0, %1, %2, %0" : "+v"(c) : "v"(a), "v"(b)); }
DEVI void mma16zn(f32x4 &c, u32x4 a, u32x4 b) { asm volatile("s_nop 1\n\tv_mfma_f32_16x16x32_bf16 %0, %1, %2, 0" : "=&v"(c) : "v"(a), "v"(b)); }
DEVI void mma16az(f32x4 &c, u32x4 a, u32x4 b) { asm volatile("v_mfma_f32_16x16x32_bf16 %0, %1, %2, 0" : "=&v"(c) : "v"(a), "a"(b)); }
DEVI void mma16A(f32x4 &c, u32x4 a, u32x4 b) { asm volatile("v_mfma_f32_16x16x32_bf16 %0, %1, %2, %0" : "+v"(c) : "a"(a), "v"(b)); }
DEVI void mma16Az(f32x4 &c, u32x4 a, u32x4 b) { asm volatile("v_mfma_f32_16x16x32_bf16 %0, %1, %2, 0" : "=&v"(c) : "a"(a), "v"(b)); }
DEVI void mma32z(f32x16 &c, u32x4 a, u32x4 b) { asm volatile("s_nop 1\n\tv_mfma_f32_32x32x16_bf16 %0, %1, %2, 0" : "=&v"(c) : "v"(a), "v"(b)); }
DEVI void mma32n(f32x16 &c, u32x4 a, u32x4 b) { asm volatile("s_nop 1\n\tv_mfma_f32_32x32x16_bf16 %0, %1, %2, %0" : "+v"(c) : "v"(a), "v"(b)); }
#define MFMA_FENCE() do { SB(); asm volatile("s_nop 7\n\ts_nop 7" ::: "memory"); SB(); } while (0)
#define FRESH_IDS()                                   \
  int tid = threadIdx.x;                              \
  asm volatile("" : "+v"(tid));                       \
  const int lane = tid & 63, i16 = lane & 15, g = lane >> 4; \
  (void)i16; (void)g
DEVI u32x4 opaque_ones() {
  u32x4 o = u32x4{0x3f803f80u, 0x3f803f80u, 0x3f803f80u, 0x3f803f80u};
  asm volatile("" : "+v"(o));
  return o;
}
struct Transit {
  rsrc_t rw;
  int v[4], so, la;
  DEVI void begin(int lane, int w, int rt, int tau) {
#pragma unroll
    for (int c = 0; c < 4; ++c) v[c] = lane * 16 + c * 4096;
    so = ((64 * rt + 32 * (tau >> 1) + 4 * (tau & 1)) * SEQ + 512 * w) * 2;
    la = B_TR + w * TR_WAVE;
  }
  DEVI void step(int i) {
    asm volatile("s_mov_b32 m0, %0\n\ts_add_u32 %0, %0, 1040\n\tbuffer_load_dwordx4 %2, %3, %1 offen lds"
                 : "+s"(la) : "s"(so), "v"(v[i & 3]), "s"(rw) : "memory", "scc");
    if ((i & 3) == 3) so += 32768;
  }
  DEVI void all() {
#pragma unroll
    for (int i = 0; i < 16; ++i) step(i);
  }
};
DEVI void transit_read(LAS unsigned char *L, int lane, int w, u32x4 (&stash)[16][4], int slot) {
  LAS unsigned char *T = L + B_TR + w * TR_WAVE + (lane & 15) * 1040 + (lane >> 4) * 16;
  asm volatile("" : "+v"(T));
#pragma unroll
  for (int k = 0; k < 16; ++k) {
    const u32x4 v = *(LAS u32x4 *)(T + 64 * k);
    if (slot == 0) stash[k][0] = v; else if (slot == 1) stash[k][1] = v; else if (slot == 2) stash[k][2] = v; else stash[k][3] = v;
  }
  WAIT_LGKM0();
}

template <int VAR>
DEVI void gemm1_loop(rsrc_t rs, LAS unsigned char *L, int lane, const int (&wo)[4], u32x4 (&wf)[G1_WIN][4], f32x4 (&acc)[4][4],
                     f32x4 (&ssum)[2], f32x4 (&ssq)[2]) {
  const int g = lane >> 4, q = (lane & 15) >> 2, pq = lane & 3;
  LAS unsigned char *xb[4];
#pragma unroll
  for (int tau = 0; tau < 4; ++tau)
    xb[tau] = L + B_X + (8 * g + q) * 128 + (((4 * (tau >> 1) + pq) ^ (4 * (q >> 1))) * 16) + (((tau & 1) ^ (g & 1)) * 8);
  constexpr bool act[4] = {true, VAR != 3, true, VAR != 2};
  const u32x4 ones = opaque_ones();
  u32x2 xl[2][4], xh[2][4];
#pragma unroll
  for (int tau = 0; tau < 4; ++tau) { xl[0][tau] = tr_read(xb[tau]); xh[0][tau] = tr_read(xb[tau] + 512); }
  SB();
#pragma unroll
  for (int ks = 0; ks < 16; ++ks) {
    const int c = ks & 1, n = c ^ 1;
    u32x4 cur[4];
#pragma unroll
    for (int s = 0; s < 4; ++s) cur[s] = wf[ks % G1_WIN][s];
#pragma unroll
    for (int tau = 0; tau < 4; ++tau) {
      const u32x4 xf = cat2(xl[c][tau], xh[c][tau]);
      if (ks == 0) mma16z(acc[0][tau], xf, cur[0]); else mma16(acc[0][tau], xf, cur[0]);
      if (ks + 1 < 16) xl[n][tau] = tr_read(xb[tau] + (ks + 1) * 4096);
      SB();
      if (act[1]) { if (ks == 0) mma16z(acc[1][tau], xf, cur[1]); else mma16(acc[1][tau], xf, cur[1]); }
      else if (tau >= 2) { if (ks == 0) mma16z(ssum[tau - 2], xf, ones); else mma16(ssum[tau - 2], xf, ones); }
      if (ks + 1 < 16) xh[n][tau] = tr_read(xb[tau] + (ks + 1) * 4096 + 512);
      SB();
      if (ks == 0) mma16z(acc[2][tau], cur[2], xf); else mma16(acc[2][tau], cur[2], xf);
      SB();
      if (act[3]) { if (ks == 0) mma16z(acc[3][tau], cur[3], xf); else mma16(acc[3][tau], cur[3], xf); }
      else if (tau < 2) { if (ks == 0) mma16z(ssum[tau], xf, ones); else mma16(ssum[tau], xf, ones); }
      if (VAR == 2 && tau < 2) { if (ks == 0) mma16z(ssq[tau], xf, xf); else mma16(ssq[tau], xf, xf); }
      if (VAR == 3 && tau >= 2) { if (ks == 0) mma16z(ssq[tau - 2], xf, xf); else mma16(ssq[tau - 2], xf, xf); }
      if (ks + G1_WIN < 16 && act[tau]) wf[ks % G1_WIN][tau] = ld_sc1(rs, wo[tau], (ks + G1_WIN) * 1024);
      SB();
    }
  }
  MFMA_FENCE();
}

__global__ __launch_bounds__(256, 1) void kb_main(Params p) {
  extern __shared__ __attribute__((aligned(16))) unsigned char smem_raw[];
  LAS unsigned char *L = (LAS unsigned char *)smem_raw;
  const int b = blockIdx.x;
  const int w = __builtin_amdgcn_readfirstlane(threadIdx.x >> 6);
  const rsrc_t rs = mk_rsrc(p.ws, WS_USED);
  gu32 *ctl = (gu32 *)p.ws;
  int rt, j;
  block_rt_j(b, rt, j);
  const int d0 = 64 * (b & 7) + 2 * (b >> 3);
  const unsigned epoch_raw = __hip_atomic_load(ctl + C_EPOCH, RLX_AGENT);
  u32x4 e1_touch;
  unsigned p_touch[2];
  {
    const int l = threadIdx.x & 63;
    e1_touch = ld_sc1(rs, l * 16, (C_E1 + (b & 7) * 256) * 4);
    const int wq = threadIdx.x >> 6;
    const int dw = l < 8 ? C_F3 + ((8 * wq + l) * 8 + j) * 32 : l < 16 ? C_F4 + (rt * 8 + (l & 7)) * 32 : C_E2 + ((b & 7) * 8 + (l & 7)) * 32;
    p_touch[0] = ld_sc1_32(rs, dw * 4, 0);
    p_touch[1] = ld_sc1_32(rs, l * 128, (int)O_GRAN + rt * 8192);
  }
  unsigned epoch;

  u32x4 stash[16][4];
  Transit tr;
  tr.rw = mk_rsrc(p.w_sp, SEQ * SEQ * 2);

  unsigned prm[2][6], ldt[2];
  f32x2 z[2][2], zm[2], zmr[2], y[2][2], ym[2], ymr[2];
  int kidx[12];
  {
    FRESH_IDS();
    const int t0 = 8 * b;
    const rsrc_t rx = mk_rsrc(p.x, SEQ * DM * 2);
    u32x4 xr[2];
#pragma unroll
    for (int r = 0; r < 2; ++r) xr[r] = ld128(rx, lane * 16, (t0 + 2 * w + r) * (DM * 2));
    u32x4 jm[4], jo[2];
    unsigned cv = 0;
    const int mcg = b % 24, mkb = b / 24;
    if (b < 192) {
      const rsrc_t rm = mk_rsrc(p.w_mod, DM * 3072 * 2);
#pragma unroll
      for (int q = 0; q < 4; ++q) jm[q] = ld128(rm, g * (3072 * 2) + i16 * 16, ((64 * mkb + 16 * w + 4 * q) * 3072 + 128 * mcg) * 2);
      cv = ((const GAS unsigned *)p.cvec)[32 * mkb + (lane & 31)];
    }
    if (b >= 144) {
      const rsrc_t ro = mk_rsrc(p.w_out, FH * DM * 2);
      const int bb = b - 144;
#pragma unroll
      for (int q = 0; q < 2; ++q) jo[q] = ld128(ro, (tid >> 4) * (DM * 2) + (tid & 15) * 16, ((32 * (bb >> 2) + 16 * q) * DM + 128 * (bb & 3)) * 2);
    }
    {
      const int po = d0 * NS + (w & 1) * 64 + lane;
      const u16 *pa[6] = {p.a_re, p.a_im, p.b_re, p.b_im, p.c_re, p.c_im};
#pragma unroll
      for (int a = 0; a < 6; ++a)
#pragma unroll
        for (int c = 0; c < 2; ++c) prm[c][a] = ((const GAS u16 *)pa[a])[po + c * NS];
#pragma unroll
      for (int c = 0; c < 2; ++c) ldt[c] = ((const GAS u16 *)p.log_dt)[d0 + c];
    }
    SB();
    const int s0o = (64 * rt + tile_row(w, i16)) * (SEQ * 2) + (512 * w + 8 * g) * 2;
#pragma unroll
    for (int q = 0; q < 6; ++q) stash[q][0] = ld128(tr.rw, s0o, q * 64);
    SB();
#pragma unroll
    for (int e = 0; e < 12; ++e) {
      const int l = 2047 - (8 * tid + e);
      const int lr = (int)rbf((float)(e < 8 ? l : max(l, 0)));
      kidx[e] = min(lr, 384 + (lr >> 2)) * 2;
      asm volatile("" : "+v"(kidx[e]));
    }
    SB();
    f32x2 xv[2][4], xn[2][4];
    {
      float st[4];
#pragma unroll
      for (int r = 0; r < 2; ++r) {
#pragma unroll
        for (int e = 0; e < 4; ++e) xv[r][e] = bf_2(xr[r][e]);
        const f32x2 s2 = (xv[r][0] + xv[r][1]) + (xv[r][2] + xv[r][3]);
        const f32x2 q2 = pk_fma(xv[r][0], xv[r][0], xv[r][1] * xv[r][1]) + pk_fma(xv[r][2], xv[r][2], xv[r][3] * xv[r][3]);
        st[2 * r] = s2[0] + s2[1];
        st[2 * r + 1] = q2[0] + q2[1];
      }
      wave_sum4(st);
#pragma unroll
      for (int r = 0; r < 2; ++r) {
        const float mean = st[2 * r] * (1.f / DM), var = __builtin_fmaf(st[2 * r + 1], 1.f / DM, -mean * mean);
        const float rsd = ln_rstd(fmaxf(var, 0.f)), mb = -rbf(mean) * rsd;
#pragma unroll
        for (int e = 0; e < 4; ++e) xn[r][e] = pk_fma(xv[r][e], f32x2{rsd, rsd}, f32x2{mb, mb});
      }
    }
    {
      u32x4 a0, a1, n0, n1;
#pragma unroll
      for (int e = 0; e < 4; ++e) {
        const unsigned lo = __builtin_amdgcn_perm(xr[1][e], xr[0][e], 0x05040100u), hi = __builtin_amdgcn_perm(xr[1][e], xr[0][e], 0x07060302u);
        const unsigned nl = pack_bf(xn[0][e][0], xn[1][e][0]), nh = pack_bf(xn[0][e][1], xn[1][e][1]);
        if (e < 2) { a0[2 * e] = lo; a0[2 * e + 1] = hi; n0[2 * e] = nl; n0[2 * e + 1] = nh; }
        else { a1[2 * e - 4] = lo; a1[2 * e - 3] = hi; n1[2 * e - 4] = nl; n1[2 * e - 3] = nh; }
      }
      LAS u32x4 *B0 = (LAS u32x4 *)(L + B_LN + w * 2048) + 2 * lane;
      B0[0] = a0; B0[1] = a1; B0[512] = n0; B0[513] = n1;
    }
    if (b < 192) {
      LAS unsigned char *MW = L + B_MW + (16 * w + g) * 272 + i16 * 16;
#pragma unroll
      for (int q = 0; q < 4; ++q) *(LAS u32x4 *)(MW + q * 4 * 272) = jm[q];
      if (w == 0 && lane < 32) {
        const f32x2 c2 = bf_2(cv);
        const f32x2 e2 = f32x2{__builtin_amdgcn_exp2f(c2[0] * -1.4426950408889634f), __builtin_amdgcn_exp2f(c2[1] * -1.4426950408889634f)} + f32x2{1.f, 1.f};
        ((LAS unsigned *)(L + B_MS))[lane] = pack2(c2 * f32x2{__builtin_amdgcn_rcpf(e2[0]), __builtin_amdgcn_rcpf(e2[1])});
      }
    }
    if (b >= 144) {
#pragma unroll
      for (int q = 0; q < 2; ++q) *(LAS u32x4 *)(L + B_WO + q * 4096 + tid * 16) = jo[q];
    }
    BAR();
    {
      LAS unsigned *S = (LAS unsigned *)(L + B_LN) + ((tid & 2) ? 0 : 2048) + 2 * (tid >> 2) + (tid & 1);
      u32x4 v[4];
#pragma unroll
      for (int a = 0; a < 4; ++a) { LAS unsigned *Sa = S + a * 128; v[a] = u32x4{Sa[0], Sa[512], Sa[1024], Sa[1536]}; }
#pragma unroll
      for (int a = 0; a < 4; ++a) st_sc1(rs, tid * 16 + a * 4096, (int)O_XT + b * 16384, v[a]);
    }
    if (b < 192) {
      u32x4 sa4[2], wb[2][2];
      LAS unsigned char *Bq = L + B_MW + (8 * g + (i16 >> 2)) * 272 + (lane & 3) * 8 + 64 * w;
#pragma unroll
      for (int ks = 0; ks < 2; ++ks) {
        sa4[ks] = *(LAS u32x4 *)(L + B_MS + ks * 64 + g * 16);
#pragma unroll
        for (int ct = 0; ct < 2; ++ct) wb[ks][ct] = cat2(tr_read(Bq + ks * 32 * 272 + ct * 32), tr_read(Bq + (ks * 32 + 4) * 272 + ct * 32));
      }
      f32x4 m0, m1;
      mma16zn(m0, sa4[0], wb[0][0]);
      mma16zn(m1, sa4[0], wb[0][1]);
      mma16n(m0, sa4[1], wb[1][0]);
      mma16n(m1, sa4[1], wb[1][1]);
      MFMA_FENCE();
      asm volatile("" :: "v"(m0), "v"(m1));
      if (lane < 32) st_sc1_32(rs, lane * 4, (int)O_MOD + (mkb * 3072 + 128 * mcg + 32 * w) * 4, f2u(g == 0 ? m0[0] : m1[0]));
    }
    if (b >= 144) {
      const int bb = b - 144;
#pragma unroll
      for (int k = 0; k < 2; ++k) {
        const int dl = 2 * w + k;
        LAS unsigned char *A = L + B_WO + (4 * g + (i16 >> 2)) * 256 + (32 * (dl >> 1) + 8 * (lane & 3) + 4 * (dl & 1)) * 2;
        st_sc1(rs, lane * 16, (int)O_WOF + ((8 * (bb & 3) + dl) * 28 + (bb >> 2)) * 1024, cat2(tr_read(A), tr_read(A + 16 * 256)));
      }
    }
    if (b == 255) {
#pragma unroll
      for (int q = 0; q < 2; ++q) st_sc1(rs, tid * 16 + q * 4096, C_E2 * 4, u32x4{0, 0, 0, 0});
    }
    SB();
    {
    const int part = w >> 1;
#pragma unroll
    for (int c = 0; c < 2; ++c) {
      const float dt = rbf(expf(bf_lo(ldt[c])));
      const float lr2 = dt * bf_lo(prm[c][0]) * 1.4426950408889634f, rev = dt * bf_lo(prm[c][1]) * 0.15915494309189535f;
      const f32x2 cc = {bf_lo(prm[c][4]), bf_lo(prm[c][5])};
      const f32x2 beta = cmul(f32x2{dt * bf_lo(prm[c][2]), dt * bf_lo(prm[c][3])}, cc, crot(cc));
      const f32x2 e0 = cexp_m(lr2, rev, part ? 512.f : 0.f), e1 = cexp_m(lr2, rev, part ? 64.f : 16.f), w1 = cexp_m(lr2, rev, part ? 4.f : 1.f);
      const f32x2 e1r = crot(e1);
      z[c][0] = cmul(beta, e0, crot(e0));
      z[c][1] = cmul(z[c][0], e1, e1r);
      zm[c] = cmul(e1, e1, e1r); zmr[c] = crot(zm[c]);
      y[c][0] = f32x2{1.f, 0.f};
      y[c][1] = w1;
      ym[c] = cmul(w1, w1, crot(w1)); ymr[c] = crot(ym[c]);
    }
#pragma unroll
    for (int c = 0; c < 2; ++c)
      asm volatile("" : "+v"(z[c][0]), "+v"(z[c][1]), "+v"(zm[c]), "+v"(zmr[c]), "+v"(y[c][1]), "+v"(ym[c]), "+v"(ymr[c]));
    }
    SB();
    asm volatile("" :: "v"(e1_touch), "v"(p_touch[0]), "v"(p_touch[1]));
    epoch = __builtin_amdgcn_readfirstlane(epoch_raw) + 1u;
    WAIT_VM(0);
    BAR();
    if (tid < 8) __hip_atomic_store(ctl + C_E1 + tid * 256 + b, epoch, RLX_AGENT);
    SB();
  }

  {
    FRESH_IDS();
    {
      const int part = w >> 1, n = (w & 1) * 64 + lane;
      const int s0o = (64 * rt + tile_row(w, i16)) * (SEQ * 2) + (512 * w + 8 * g) * 2;
      LAS unsigned char *T0 = L + B_KT + n * KT_N + part * 64, *T1 = L + B_TR + n * KT_N + part * 64;
      asm volatile("" : "+v"(T0), "+v"(T1));
#pragma unroll
      for (int grp = 0; grp < 6; ++grp) {
        u32x4 re[2], im[2];
#pragma unroll
        for (int q = 0; q < 4; ++q) {
#pragma unroll
          for (int c = 0; c < 2; ++c) {
            if (grp < 4) {
              re[c][q] = pack_bf(z[c][0][0], z[c][1][0]); im[c][q] = pack_bf(z[c][0][1], z[c][1][1]);
              z[c][0] = cmul(z[c][0], zm[c], zmr[c]); z[c][1] = cmul(z[c][1], zm[c], zmr[c]);
            } else {
              re[c][q] = pack_bf(y[c][0][0], y[c][1][0]); im[c][q] = pack_bf(-y[c][0][1], -y[c][1][1]);
              y[c][0] = cmul(y[c][0], ym[c], ymr[c]); y[c][1] = cmul(y[c][1], ym[c], ymr[c]);
            }
          }
        }
#pragma unroll
        for (int c = 0; c < 2; ++c) {
          LAS unsigned char *T = (c ? T1 : T0) + (grp < 4 ? 16 * grp : 128 - part * 32 + 16 * (grp - 4));
          *(LAS u32x4 *)T = re[c];
          *(LAS u32x4 *)(T + KT_IM) = im[c];
        }
        stash[6 + grp][0] = ld128(tr.rw, s0o, (6 + grp) * 64);
        SB();
      }
      BAR();
      u32x4 e1f = ld_sc1(rs, lane * 16, (C_E1 + (b & 7) * 256) * 4);
      f32x4 acc[2];
      u32x4 a[2][8], bq[2][8];
#pragma unroll
      for (int c = 0; c < 2; ++c) {
        LAS unsigned char *TA = L + (c ? B_TR : B_KT) + (4 * g + (i16 >> 3)) * KT_N + ((i16 >> 2) & 1) * KT_IM + 8 * (lane & 3) + 32 * w;
        LAS unsigned char *TB = L + (c ? B_TR : B_KT) + (4 * g + (i16 >> 3)) * KT_N + ((i16 >> 2) & 1) * KT_IM + 8 * (lane & 3) + 128 + 32 * (w >> 1);
#pragma unroll
        for (int ks = 0; ks < 8; ++ks) {
          a[c][ks] = cat2(tr_read(TA + ks * 16 * KT_N), tr_read(TA + (ks * 16 + 2) * KT_N));
          bq[c][ks] = cat2(tr_read(TB + ks * 16 * KT_N), tr_read(TB + (ks * 16 + 2) * KT_N));
        }
      }
      SB();
#pragma unroll
      for (int q = 12; q < 14; ++q) stash[q][0] = ld128(tr.rw, s0o, q * 64);
      mma16zn(acc[0], a[0][0], bq[0][0]);
      mma16zn(acc[1], a[1][0], bq[1][0]);
#pragma unroll
      for (int ks = 1; ks < 8; ++ks) { mma16(acc[0], a[0][ks], bq[0][ks]); mma16(acc[1], a[1][ks], bq[1][ks]); }
#pragma unroll
      for (int q = 14; q < 16; ++q) stash[q][0] = ld128(tr.rw, s0o, q * 64);
      MFMA_FENCE();
#pragma unroll
      for (int c = 0; c < 2; ++c) {
        LAS u16 *KD = (LAS u16 *)(L + B_KD + c * 2048) + (16 * w + 4 * g) * 16 + i16;
        const unsigned k01 = pack_bf(acc[c][0], acc[c][1]), k23 = pack_bf(acc[c][2], acc[c][3]);
        KD[0] = (u16)k01; KD[16] = (u16)(k01 >> 16); KD[32] = (u16)k23; KD[48] = (u16)(k23 >> 16);
      }
      if (w == 0) {
        for (unsigned spins = 0; spins < SPIN_CAP; ++spins) {
          if (__all(((e1f[0] ^ epoch) | (e1f[1] ^ epoch) | (e1f[2] ^ epoch) | (e1f[3] ^ epoch)) == 0u)) break;
          __builtin_amdgcn_s_sleep(POLL_SLEEP);
          e1f = ld_sc1(rs, lane * 16, (C_E1 + (b & 7) * 256) * 4);
        }
        __builtin_amdgcn_fence(__ATOMIC_ACQUIRE, "wavefront");
      }
      BAR();
    }
    const int ch = w >> 1, pp = w & 1;
    const int i32 = lane & 31, h = lane >> 5;
    const int wF = b < 224 ? b >> 1 : 0, wks = 8 * (b & 1) + 2 * w;
    u32x4 xn[4];
#pragma unroll
    for (int i = 0; i < 4; ++i) xn[i] = ld_sc1(rs, (lane >> 2) * 16384 + (lane & 3) * 16, (int)O_XT + (64 * w + 16 * i) * 16384 + d0 * 32);
    float mraw = u2f(ld_sc1_32(rs, (lane & 7) * (3072 * 4) + ((lane >> 4) % 3) * 2048 + ((lane >> 3) & 1) * 4, (int)O_MOD + d0 * 4));
    float cbs[2][8];
#pragma unroll
    for (int q = 0; q < 8; ++q) {
      cbs[0][q] = u2f(ld_sc1_32(rs, lane * 4, (int)O_MOD + (q * 3072 + 4 * 512 + 32 * wks) * 4));
      cbs[1][q] = u2f(ld_sc1_32(rs, lane * 4, (int)O_MOD + (q * 3072 + 3 * 512 + 32 * wks) * 4));
    }
    float dsk = bf2f(((const GAS u16 *)p.dskip)[d0 + ch]);
    const rsrc_t rin = mk_rsrc(p.w_in, DM * FF * 2);
    u32x4 wr[2];
#pragma unroll
    for (int k = 0; k < 2; ++k) wr[k] = ld128(rin, (lane >> 1) * (FF * 2) + (lane & 1) * 16, (32 * (wks + k) * FF + 16 * wF) * 2);
    SB();
    {
      unsigned v[2][12];
#pragma unroll
      for (int c = 0; c < 2; ++c)
#pragma unroll
        for (int e = 0; e < 12; ++e) v[c][e] = *(LAS u16 *)(L + B_KD + c * 2048 + kidx[e]);
      SB();
#pragma unroll
      for (int c = 0; c < 2; ++c) {
#pragma unroll
        for (int e = 8; e < 12; ++e) v[c][e] = tid == 255 ? 0u : v[c][e];
        unsigned D[6];
#pragma unroll
        for (int e = 0; e < 6; ++e) D[e] = v[c][2 * e] | (v[c][2 * e + 1] << 16);
        unsigned A[5];
#pragma unroll
        for (int e = 0; e < 5; ++e) A[e] = __builtin_amdgcn_alignbit(D[e + 1], D[e], 16);
        LAS unsigned char *R = L + c * 4 * B_RC + 16 * tid;
        *(LAS u32x4 *)(R + 0 * B_RC) = u32x4{D[0], D[1], D[2], D[3]};
        *(LAS u32x4 *)(R + 1 * B_RC) = u32x4{A[0], A[1], A[2], A[3]};
        *(LAS u32x4 *)(R + 2 * B_RC) = u32x4{D[1], D[2], D[3], D[4]};
        *(LAS u32x4 *)(R + 3 * B_RC) = u32x4{A[1], A[2], A[3], A[4]};
      }
      if (tid < 32) *(LAS u32x4 *)(L + (tid >> 2) * B_RC + 4096 + 16 * (tid & 3)) = u32x4{0, 0, 0, 0};
    }
    SB();
    if (tid < 155) {
      *(LAS u32x4 *)(L + B_U + 16 * tid) = u32x4{0, 0, 0, 0};
      *(LAS u32x4 *)(L + B_U + U_CH + 16 * tid) = u32x4{0, 0, 0, 0};
    }
    mraw += dpp_f<0xB1>(mraw); mraw += dpp_f<0x4E>(mraw); mraw += dpp_f<0x141>(mraw);
    const unsigned mval = f2u(rbf(mraw));
    const float gaw = u2f(__builtin_amdgcn_readlane(mval, 32 + 8 * ch));
    {
      const int c = lane & 1;
      const bool isx = lane & 2;
      const float sa0 = u2f(__builtin_amdgcn_readlane(mval, 0)), sa1 = u2f(__builtin_amdgcn_readlane(mval, 8));
      const float ca0 = u2f(__builtin_amdgcn_readlane(mval, 16)), ca1 = u2f(__builtin_amdgcn_readlane(mval, 24));
      const float sa = c ? sa1 : sa0, c1 = rbf(1.f + (c ? ca1 : ca0));
      LAS unsigned char *PU = L + (isx ? B_XU + c * 5120 : B_U + c * U_CH + 31 * 80) + (16 * w + g) * 80 + 16 * ((lane >> 2) & 3);
#pragma unroll
      for (int i = 0; i < 4; ++i) {
        u32x4 hv;
#pragma unroll
        for (int e = 0; e < 4; ++e) { const unsigned m = pack2(pk_fma(bf_2(xn[i][e]), f32x2{c1, c1}, f32x2{sa, sa})); hv[e] = isx ? xn[i][e] : m; }
        *(LAS u32x4 *)(PU + i * 320) = hv;
      }
    }
    LAS unsigned char *WT = L + B_WT + w * 1024;
    *(LAS u32x4 *)(WT + lane * 16) = wr[0];
    u32x4 sbv[2];
    f32x2 c2[2][4];
    {
      float t[2];
#pragma unroll
      for (int a = 0; a < 2; ++a) t[a] = ((cbs[a][0] + cbs[a][1]) + (cbs[a][2] + cbs[a][3])) + ((cbs[a][4] + cbs[a][5]) + (cbs[a][6] + cbs[a][7]));
      LAS u16 *CS = (LAS u16 *)(L + B_CS + w * 256);
      const unsigned pk = pack_bf(1.f + rbf(t[0]), t[1]);
      CS[lane] = (u16)pk; CS[64 + lane] = (u16)(pk >> 16);
#pragma unroll
      for (int k = 0; k < 2; ++k) {
        const u32x4 cq = *(LAS u32x4 *)(L + B_CS + w * 256 + 64 * k + 16 * g);
        sbv[k] = *(LAS u32x4 *)(L + B_CS + w * 256 + 128 + 64 * k + 16 * g);
#pragma unroll
        for (int e = 0; e < 4; ++e) c2[k][e] = bf_2(cq[e]);
      }
    }
    u32x4 ones = opaque_ones();
    asm volatile("" : "+v"(sbv[0]), "+v"(sbv[1]), "+v"(dsk));
    tr.begin(lane, w, rt, (w + 1) & 3);
    BAR();

    f32x16 acc0, acc1;
    u32x4 raw[2], scl[2];
    f32x4 e1, e2;
    {
      LAS unsigned char *Al = L + ch * 4 * B_RC + (3 - (i32 & 3)) * B_RC + (2044 - 4 * (i32 >> 2) + 8 * h) * 2 - 64 * pp - 128 * 15 - 2048;
      LAS unsigned char *Ah = Al + 8;
      asm volatile("" : "+v"(Ah));
      LAS unsigned char *Bb = L + B_U + ch * U_CH + 80 * i32 + 16 * h - 80 * pp;
      LAS unsigned char *WA = WT + (8 * g + (i16 >> 2)) * 32 + 8 * (lane & 3);
      u32x2 a0l[3], a0h[3], a1l[3], a1h[3];
      u32x4 b0[3], b1[3];
#define CONV_LOAD_A(n, sl)                                                                          \
  do {                                                                                              \
    const int o_ = 128 * (15 - ((n) >> 1)) + 32 * ((n) & 1);                                        \
    a0l[sl] = *(LAS u32x2 *)(Al + o_ + 2048); a0h[sl] = *(LAS u32x2 *)(Ah + o_ + 2048);             \
  } while (0)
#define CONV_LOAD_A1(n, sl)                                                                         \
  do {                                                                                              \
    const int o_ = 128 * (15 - ((n) >> 1)) + 32 * ((n) & 1);                                        \
    a1l[sl] = *(LAS u32x2 *)(Al + o_); a1h[sl] = *(LAS u32x2 *)(Ah + o_);                           \
  } while (0)
#define CONV_LOAD_B(n, sl)                                                                          \
  do {                                                                                              \
    b0[sl] = *(LAS u32x4 *)(Bb + (31 - 2 * ((n) >> 1)) * 80 + 32 * ((n) & 1));                      \
    b1[sl] = *(LAS u32x4 *)(Bb + (63 - 2 * ((n) >> 1)) * 80 + 32 * ((n) & 1));                      \
  } while (0)
#pragma unroll
      for (int n = 0; n < 2; ++n) { CONV_LOAD_A(n, n); CONV_LOAD_B(n, n); CONV_LOAD_A1(n, n); }
      SB();
#pragma unroll
      for (int n = 0; n < 32; ++n) {
        const int c = n % 3, nx = (n + 2) % 3;
        const u32x4 A0 = cat2(a0l[c], a0h[c]), A1 = cat2(a1l[c], a1h[c]);
        if (n == 0) mma32z(acc0, A0, b0[c]); else if (n == 1) mma32n(acc0, A0, b0[c]); else mma32(acc0, A0, b0[c]);
        if (n + 2 < 32) CONV_LOAD_A(n + 2, nx);
        SB();
        if (n == 0) mma32z(acc1, A0, b1[c]); else mma32(acc1, A0, b1[c]);
        if (n + 2 < 32) CONV_LOAD_B(n + 2, nx);
        SB();
        if (n < 2) mma32n(acc1, A1, b0[c]); else mma32(acc1, A1, b0[c]);
        if (n + 2 < 32) CONV_LOAD_A1(n + 2, nx);
        if (n < 16) tr.step(n);
        if (n == 2 || n == 8) raw[n == 8] = cat2(tr_read(WA), tr_read(WA + 128));
        if (n == 5) *(LAS u32x4 *)(WT + lane * 16) = wr[1];
        if (n == 4 || n == 5 || n == 10 || n == 11) {
          const int k = n >= 10, e0 = 2 * (n & 1);
          scl[k][e0] = pack2(bf_2(raw[k][e0]) * c2[k][e0]);
          scl[k][e0 + 1] = pack2(bf_2(raw[k][e0 + 1]) * c2[k][e0 + 1]);
        }
        if (n == 6) { mma16zn(e1, ones, scl[0]); mma16z(e2, sbv[0], raw[0]); }
        if (n == 12) { mma16n(e1, ones, scl[1]); mma16(e2, sbv[1], raw[1]); }
        SB();
      }
      MFMA_FENCE();
    }
    if (pp) { const f32x16 t = acc0; acc0 = acc1; acc1 = t; }
    {
      LAS f32x4 *R = (LAS f32x4 *)(L + B_RED + (ch * 2 + (1 - pp)) * 4096) + lane;
#pragma unroll
      for (int rg = 0; rg < 4; ++rg) R[rg * 64] = f32x4{acc1[4 * rg], acc1[4 * rg + 1], acc1[4 * rg + 2], acc1[4 * rg + 3]};
    }
    asm volatile("" :: "v"(e1), "v"(e2));
    if (b < 224) {
#pragma unroll
      for (int k = 0; k < 2; ++k) st_sc1(rs, lane * 16, (int)O_WP + (wF * 16 + wks + k) * 1024, scl[k]);
    }
    if (g == 0) {
      LAS float *EP = (LAS float *)(L + B_WT + 1024 * w + 768) + i16;
      EP[0] = e1[0]; EP[16] = e2[0];
    }
    BAR();
    if (b < 224 && tid < 32) {
      LAS float *EP = (LAS float *)(L + B_WT + 768) + tid;
      st_sc1_32(rs, (tid & 15) * 4 + (tid >> 4) * (FF * 4), (int)O_E12 + ((b & 1) * 2 * FF + 16 * wF) * 4, f2u((EP[0] + EP[256]) + (EP[512] + EP[768])));
    }
    {
      const int d = d0 + ch;
      LAS f32x4 *R = (LAS f32x4 *)(L + B_RED + (ch * 2 + pp) * 4096) + lane;
      LAS unsigned char *U = L + B_U + ch * U_CH + (31 + 32 * pp + i32) * 80 + 8 * h;
      LAS unsigned char *XU = L + B_XU + ch * 5120 + (32 * pp + i32) * 80 + 8 * h;
      f32x4 o[4];
      u32x2 hv[4], xe[4];
#pragma unroll
      for (int rg = 0; rg < 4; ++rg) { o[rg] = R[rg * 64]; hv[rg] = *(LAS u32x2 *)(U + 16 * rg); xe[rg] = *(LAS u32x2 *)(XU + 16 * rg); }
      SB();
      unsigned o0[4], o1[4];
      const f32x2 dk2 = {dsk, dsk}, ga2 = {gaw, gaw};
#pragma unroll
      for (int rg = 0; rg < 4; ++rg) {
        const f32x2 ya = lo2(o[rg]) + f32x2{acc0[4 * rg], acc0[4 * rg + 1]}, yb = hi2(o[rg]) + f32x2{acc0[4 * rg + 2], acc0[4 * rg + 3]};
        o0[rg] = pack2(pk_fma(ga2, pk_fma(bf_2(hv[rg].x), dk2, ya), bf_2(xe[rg].x)));
        o1[rg] = pack2(pk_fma(ga2, pk_fma(bf_2(hv[rg].y), dk2, yb), bf_2(xe[rg].y)));
      }
      const bool sw = (d >> 3) & 1;
      u32x4 pc[2];
#pragma unroll
      for (int k = 0; k < 2; ++k) {
        auto s0 = __builtin_amdgcn_permlane32_swap(o0[2 * k], o0[2 * k + 1], false, false);
        auto s1 = __builtin_amdgcn_permlane32_swap(o1[2 * k], o1[2 * k + 1], false, false);
        const unsigned l0 = s0[0], h0 = s0[1], l1 = s1[0], h1 = s1[1];
        pc[k] = sw ? u32x4{h0, h1, l0, l1} : u32x4{l0, l1, h0, h1};
      }
      const bool odd = i32 & 1;
      u32x4 a, bq;
#pragma unroll
      for (int e = 0; e < 4; ++e) {
        const unsigned t = odd ? pc[0][e] : pc[1][e];
        const unsigned u = __builtin_amdgcn_update_dpp(0, (int)t, 0xB1, 0xf, 0xf, true);
        a[e] = odd ? u : pc[0][e];
        bq[e] = odd ? pc[1][e] : u;
      }
      const int vo = (i32 >> 1) * 65536 + (i32 & 1) * 32 + 16 * h;
      st_sc1(rs, vo, (int)O_X1T + 16 * pp * 65536 + d * 128, a);
      st_sc1(rs, vo + 64, (int)O_X1T + 16 * pp * 65536 + d * 128, bq);
    }
    SB();
    WAIT_VM(0);
    BAR();
    if (tid < 8) __hip_atomic_fetch_add(ctl + C_E2 + (tid * 8 + (b & 7)) * 32, 1u, RLX_AGENT);
    SB();
    transit_read(L, lane, w, stash, 1);
    tr.begin(lane, w, rt, (w + 2) & 3);
#pragma unroll
    for (int i = 0; i < 16; ++i) { tr.step(i); if (R2_SLEEP) __builtin_amdgcn_s_sleep(R2_SLEEP); }
    if (w == 0) wave_poll<true>(ctl + C_E2 + ((b & 7) * 8 + (lane & 7)) * 32, lane < 8, 32u);
    WAIT_VM(0);
    BAR();
  }

  u32x4 xres[2];
  {
    FRESH_IDS();
    const int ph2[2] = {2 * w, 2 * w + 1};
    const int ph1[2] = {w < 3 ? 2 * w : 5, w < 3 ? 2 * w + 1 : 6};
    const int Fs[4] = {56 + 7 * j + ph2[0], 56 + 7 * j + (ph2[1] < 7 ? ph2[1] : 0), 7 * j + ph1[0], 7 * j + (w == 2 ? 0 : ph1[1])};
    int wo[4];
#pragma unroll
    for (int s = 0; s < 4; ++s) wo[s] = (int)O_WP + Fs[s] * 16 * 1024 + lane * 16;
    u32x4 wf[G1_WIN][4];
    unsigned ep[4], gbv[8];
    {
      u32x4 xs[16];
      const int xo = (int)O_X1T + rt * 65536 + tid * 16;
#pragma unroll
      for (int i = 0; i < 16; ++i) xs[i] = ld_sc1(rs, xo, i * 4096);
      SB();
      transit_read(L, lane, w, stash, 2);
      SB();
      const int r0 = tid >> 3, cc = tid & 7;
      LAS unsigned char *X = L + B_X + r0 * 128 + ((cc ^ (4 * ((r0 >> 1) & 1))) * 16);
#pragma unroll
      for (int i = 0; i < 16; ++i) {
        *(LAS u32x4 *)(X + i * 4096) = xs[i];
        wf[i >> 2][i & 3] = ld_sc1(rs, wo[i & 3], (i >> 2) * 1024);
        SB();
      }
      const int tf = tid < 224 ? tid : 0, f = tf < 112 ? 112 * j + tf : FH + 112 * j + (tf - 112);
#pragma unroll
      for (int q = 0; q < 4; ++q) ep[q] = ld_sc1_32(rs, f * 4, (int)O_E12 + q * FF * 4);
#pragma unroll
      for (int q = 0; q < 8; ++q) gbv[q] = ld_sc1_32(rs, (tid & 63) * 4, (int)O_MOD + (q * 3072 + 5 * 512 + 64 * j) * 4);
      SB();
    }
    BAR();
    f32x4 acc[4][4], ssum[2], ssq[2];
    if (w < 2) gemm1_loop<0>(rs, L, lane, wo, wf, acc, ssum, ssq);
    else if (w == 2) gemm1_loop<2>(rs, L, lane, wo, wf, acc, ssum, ssq);
    else gemm1_loop<3>(rs, L, lane, wo, wf, acc, ssum, ssq);

    if (tid < 224) {
      LAS float *E = (LAS float *)(L + B_E) + tid;
      E[0] = u2f(ep[0]) + u2f(ep[2]);
      E[224] = u2f(ep[1]) + u2f(ep[3]);
    }
    if (tid < 64)
      ((LAS float *)(L + B_GB))[tid] = rbf(((u2f(gbv[0]) + u2f(gbv[1])) + (u2f(gbv[2]) + u2f(gbv[3]))) + ((u2f(gbv[4]) + u2f(gbv[5])) + (u2f(gbv[6]) + u2f(gbv[7]))));
    LAS float *SM = (LAS float *)(L + B_ST), *SR = SM + 64;
    asm volatile("" : "+v"(SM));
    SR = SM + 64;
    if (w >= 2 && g == (i16 >> 2)) {
#pragma unroll
      for (int k = 0; k < 2; ++k) {
        const int tau = (w == 2 ? 0 : 2) + k;
        const float mean = pick4(ssum[k], i16 & 3) * (1.f / DM);
        const float var = pick4(ssq[k], i16 & 3) * (1.f / DM) - mean * mean;
        const int s = tile_row(tau, i16);
        const float r = ln_rstd(fmaxf(var, 0.f));
        SM[s] = -rbf(mean) * r;
        SR[s] = r;
      }
    }
    {
      const int q = i16 >> 2, pq = lane & 3;
      LAS unsigned char *xb = L + B_X + (8 * g + q) * 128 + (((4 * (w >> 1) + pq) ^ (4 * (q >> 1))) * 16) + (((w & 1) ^ (g & 1)) * 8) + 2 * j * 4096;
#pragma unroll
      for (int dl = 0; dl < 2; ++dl) xres[dl] = cat2(tr_read(xb + dl * 4096), tr_read(xb + dl * 4096 + 512));
    }
    tr.begin(lane, w, rt, (w + 3) & 3);
    BAR();

    const int nz2 = w == 3 ? 1 : 2, nz1 = w == 2 ? 1 : 2;
    u32x4 zf[2][2];
    f32x4 e1z[2], e2z[2];
    float m1[4], r1[4];
    {
      LAS float *E1 = (LAS float *)(L + B_E);
      asm volatile("" : "+v"(E1));
      f32x4 m4[4], r4[4];
      float e1[2], e2[2];
#pragma unroll
      for (int t = 0; t < 4; ++t) { m4[t] = *(LAS f32x4 *)(SM + 32 * (t >> 1) + 8 * g + 4 * (t & 1)); r4[t] = *(LAS f32x4 *)(SR + 32 * (t >> 1) + 8 * g + 4 * (t & 1)); }
#pragma unroll
      for (int s = 0; s < 2; ++s) {
        const int p2 = s < nz2 ? ph2[s] : 0, p1 = s < nz1 ? ph1[s] : 0;
        e1[s] = E1[112 + 16 * p2 + i16]; e2[s] = E1[224 + 112 + 16 * p2 + i16];
        e1z[s] = *(LAS f32x4 *)(E1 + 16 * p1 + 4 * g); e2z[s] = *(LAS f32x4 *)(E1 + 224 + 16 * p1 + 4 * g);
      }
#pragma unroll
      for (int t = 0; t < 4; ++t) { const int sr = tile_row(t, i16); m1[t] = SM[sr]; r1[t] = SR[sr]; }
      SB();
      LAS unsigned char *ZW = L + B_ZT + i16 * 128 + 16 * g;
#pragma unroll
      for (int s = 0; s < 2; ++s) {
        if (s < nz2) {
          const f32x2 e12 = {e1[s], e1[s]}, e22 = {e2[s], e2[s]};
#pragma unroll
          for (int t = 0; t < 4; ++t) {
            const f32x4 a = acc[s][t];
            zf[s][t >> 1][2 * (t & 1)] = pack2(gelu2(pk_fma(lo2(a), lo2(r4[t]), pk_fma(lo2(m4[t]), e12, e22))));
            zf[s][t >> 1][2 * (t & 1) + 1] = pack2(gelu2(pk_fma(hi2(a), hi2(r4[t]), pk_fma(hi2(m4[t]), e12, e22))));
            if (s == 0) { tr.step(2 * t); tr.step(2 * t + 1); SB(); }
          }
#pragma unroll
          for (int kp = 0; kp < 2; ++kp) *(LAS u32x4 *)(ZW + ph2[s] * 2048 + 64 * kp) = zf[s][kp];
        }
      }
    }
    if (tid < 128) *(LAS u32x4 *)(L + B_ZT + 112 * 128 + 16 * tid) = u32x4{0, 0, 0, 0};
    BAR();
    {
      const int q = i16 >> 2, pq = lane & 3;
      const u32x4 ones = opaque_ones();
      f32x4 su, sq;
      u32x4 za[4];
      LAS unsigned char *A = L + B_ZT + (8 * g + q) * 128 + (16 * w + 4 * pq) * 2;
#pragma unroll
      for (int st = 0; st < 4; ++st) { za[st] = cat2(tr_read(A + st * 4096), tr_read(A + st * 4096 + 512)); tr.step(8 + st); }
      mma16zn(su, za[0], ones);
      mma16z(sq, za[0], za[0]);
#pragma unroll
      for (int st = 1; st < 4; ++st) { mma16(su, za[st], ones); mma16(sq, za[st], za[st]); }
#pragma unroll
      for (int st = 12; st < 16; ++st) tr.step(st);
      MFMA_FENCE();
      if (g == q) {
        const u32x4 gr = {f2u(pick4(su, i16 & 3)), epoch, f2u(pick4(sq, i16 & 3)), epoch};
        st_sc1(rs, (16 * w + i16) * 16, (int)O_GRAN + (rt * 8 + j) * 1024, gr);
      }
    }
    {
      LAS unsigned char *Z1 = L + B_Z1 + lane * 8;
      asm volatile("" : "+v"(Z1));
#pragma unroll
      for (int s = 0; s < 2; ++s) {
        if (s < nz1) {
          u32x2 z[4];
#pragma unroll
          for (int t = 0; t < 4; ++t) {
            const f32x2 m2 = {m1[t], m1[t]}, r2 = {r1[t], r1[t]};
            const f32x4 a = acc[2 + s][t];
            z[t] = u32x2{pack2(gelu2(pk_fma(lo2(a), r2, pk_fma(m2, lo2(e1z[s]), lo2(e2z[s]))))),
                         pack2(gelu2(pk_fma(hi2(a), r2, pk_fma(m2, hi2(e1z[s]), hi2(e2z[s])))))};
          }
#pragma unroll
          for (int t = 0; t < 4; ++t) *(LAS u32x2 *)(Z1 + ph1[s] * 2048 + t * 512) = z[t];
        }
      }
    }
    {
      float su, sq;
      for (unsigned spins = 0; spins < SPIN_CAP; ++spins) {
        u32x4 gr[8];
#pragma unroll
        for (int q = 0; q < 8; ++q) gr[q] = ld_sc1(rs, lane * 16, (int)O_GRAN + (rt * 8 + q) * 1024);
        unsigned bad = 0u;
        su = 0.f; sq = 0.f;
#pragma unroll
        for (int q = 0; q < 8; ++q) { bad |= (gr[q][1] ^ epoch) | (gr[q][3] ^ epoch); su += u2f(gr[q][0]); sq += u2f(gr[q][2]); }
        if (__all(bad == 0u)) break;
        __builtin_amdgcn_s_sleep(POLL_SLEEP);
      }
      const float mean = su * (1.f / FH), var = sq * (1.f / FH) - mean * mean;
      const float r = ln_rstd(fmaxf(var, 0.f));
      LAS float *ZR = (LAS float *)(L + B_ZS + w * 512);
      asm volatile("" : "+v"(ZR));
      ZR[lane] = r;
      ZR[64 + lane] = -rbf(mean) * r;
      f32x4 r4[4], m4[4];
#pragma unroll
      for (int t = 0; t < 4; ++t) { r4[t] = *(LAS f32x4 *)(ZR + 32 * (t >> 1) + 8 * g + 4 * (t & 1)); m4[t] = *(LAS f32x4 *)(ZR + 64 + 32 * (t >> 1) + 8 * g + 4 * (t & 1)); }
      SB();
#pragma unroll
      for (int s = 0; s < 2; ++s) {
        if (s < nz2) {
#pragma unroll
          for (int kp = 0; kp < 2; ++kp) {
            u32x4 o;
#pragma unroll
            for (int hf = 0; hf < 2; ++hf) {
              o[2 * hf] = pack2(pk_fma(bf_2(zf[s][kp][2 * hf]), lo2(r4[2 * kp + hf]), lo2(m4[2 * kp + hf])));
              o[2 * hf + 1] = pack2(pk_fma(bf_2(zf[s][kp][2 * hf + 1]), hi2(r4[2 * kp + hf]), hi2(m4[2 * kp + hf])));
            }
            st_sc1(rs, lane * 16, (int)O_Z2F + ((7 * j + ph2[s]) * 64 + 2 * rt + kp) * 1024, o);
          }
        }
      }
    }
    SB();
    WAIT_VM(0);
    BAR();
    if (tid < 8) st_sc1(rs, tid * 16, (C_F3 + (rt * 8 + j) * 32) * 4, u32x4{epoch, epoch, epoch, epoch});
    SB();
    transit_read(L, lane, w, stash, 3);
    BAR();
  }

  u32x4 wof[4][7];
  {
    FRESH_IDS();
    const float bsp = bf2f(p.b_sp[64 * rt + tile_row(w, i16)]);
    wave_poll<false>(ctl + C_F3 + ((8 * w + (lane & 7)) * 8 + j) * 32, lane < 8, epoch);
    const int ao = (int)O_Z2F + (7 * j * 64 + 16 * w) * 1024, vo = lane * 16;
    u32x4 af[G2_WIN];
#pragma unroll
    for (int n = 0; n < G2_WIN; ++n) af[n] = ld_sc1(rs, vo, ao + ((n >> 4) * 64 + (n & 15)) * 1024);
    SB();
    f32x4 acc[2][4], keep[7];
    LAS f32x4 *R = (LAS f32x4 *)L + lane;
    LAS f32x4 *Rs[3];
#pragma unroll
    for (int s = 1; s < 4; ++s) Rs[s - 1] = R + (((s + w) & 3) * 3 + (s - 1)) * 7 * 64;
#pragma unroll
    for (int ph = 0; ph < 7; ++ph) {
#pragma unroll
      for (int k = 0; k < 16; ++k) {
        const int n = ph * 16 + k;
        const u32x4 cur = af[n % G2_WIN];
#pragma unroll
        for (int s = 0; s < 4; ++s) { if (k == 0) mma16az(acc[ph & 1][s], cur, stash[k][s]); else mma16a(acc[ph & 1][s], cur, stash[k][s]); }
        if (n + G2_WIN < 112) af[n % G2_WIN] = ld_sc1(rs, vo, ao + (((n + G2_WIN) >> 4) * 64 + ((n + G2_WIN) & 15)) * 1024);
        if (ph > 0 && k >= 3 && k < 6) Rs[k - 3][(ph - 1) * 64] = acc[(ph - 1) & 1][k - 2];
        if (ph > 0 && k == 6) keep[ph - 1] = acc[(ph - 1) & 1][0];
        if (n >= 84) {
          const int m = n - 84;
          wof[m / 7][m % 7] = ld_sc1(rs, vo, (int)O_WOF + ((4 * j + m / 7) * 28 + 7 * w + m % 7) * 1024);
        }
        SB();
      }
    }
    MFMA_FENCE();
#pragma unroll
    for (int s = 1; s < 4; ++s) Rs[s - 1][6 * 64] = acc[0][s];
    keep[6] = acc[0][0];
    SB();
    BAR();
    unsigned vv[7][2];
    {
      const f32x2 b2 = {bsp, bsp};
      LAS f32x4 *Rw = R + w * 3 * 7 * 64;
      LAS unsigned char *Z1 = L + B_Z1 + (w * 64 + lane) * 8;
      asm volatile("" : "+v"(Z1));
      f32x4 ra[7][3];
      u32x2 z1[7];
#pragma unroll
      for (int ph = 0; ph < 7; ++ph) {
#pragma unroll
        for (int q = 0; q < 3; ++q) ra[ph][q] = Rw[(q * 7 + ph) * 64];
        z1[ph] = *(LAS u32x2 *)(Z1 + ph * 2048);
      }
      SB();
#pragma unroll
      for (int ph = 0; ph < 7; ++ph) {
        const f32x4 s = (keep[ph] + ra[ph][0]) + (ra[ph][1] + ra[ph][2]);
        vv[ph][0] = pack2(bf_2(z1[ph].x) * (lo2(s) + b2));
        vv[ph][1] = pack2(bf_2(z1[ph].y) * (hi2(s) + b2));
      }
    }
    const int vb = (int)O_VF + (4 * rt + w) * 28 * 1024;
    if ((j & 1) == 0) {
      const int p0 = 7 * j / 2;
#pragma unroll
      for (int k = 0; k < 3; ++k) st_sc1(rs, vo, vb + (p0 + k) * 1024, u32x4{vv[2 * k][0], vv[2 * k][1], vv[2 * k + 1][0], vv[2 * k + 1][1]});
      st_sc1_64(rs, lane * 8, (int)O_VH + ((4 * rt + w) * 8 + j) * 512, u32x2{vv[6][0], vv[6][1]});
    } else {
      const int p0 = (7 * j - 1) / 2;
      st_sc1_64(rs, lane * 8, (int)O_VH + ((4 * rt + w) * 8 + j) * 512, u32x2{vv[0][0], vv[0][1]});
#pragma unroll
      for (int k = 0; k < 3; ++k)
        st_sc1(rs, vo, vb + (p0 + 1 + k) * 1024, u32x4{vv[2 * k + 1][0], vv[2 * k + 1][1], vv[2 * k + 2][0], vv[2 * k + 2][1]});
    }
    SB();
    WAIT_VM(0);
    BAR();
    if (tid < 8) st_sc1(rs, tid * 16, (C_F4 + (rt * 8 + j) * 32) * 4, u32x4{epoch, epoch, epoch, epoch});
  }

  {
    FRESH_IDS();
    wave_poll<false>(ctl + C_F4 + (rt * 8 + 2 * w + (lane & 1)) * 32, lane < 2, epoch);
    u32x4 vf[7][4];
#pragma unroll
    for (int k = 0; k < 7; ++k)
#pragma unroll
      for (int s = 0; s < 4; ++s) {
        if (k == 3) {
          const int ho = (int)O_VH + ((4 * rt + ((s + w) & 3)) * 8 + 2 * w) * 512;
          vf[k][s] = cat2(ld_sc1_64(rs, lane * 8, ho), ld_sc1_64(rs, lane * 8, ho + 512));
        } else {
          vf[k][s] = ld_sc1(rs, lane * 16, (int)O_VF + ((4 * rt + ((s + w) & 3)) * 28 + 7 * w + k) * 1024);
        }
      }
    SB();
    f32x4 acc[4][4];
#pragma unroll
    for (int k = 0; k < 7; ++k)
#pragma unroll
      for (int s = 0; s < 4; ++s) {
#pragma unroll
        for (int dd = 0; dd < 4; ++dd) { if (k == 0) mma16az(acc[dd][s], wof[dd][k], vf[k][s]); else mma16a(acc[dd][s], wof[dd][k], vf[k][s]); }
        SB();
      }
    MFMA_FENCE();
    LAS f32x4 *R = (LAS f32x4 *)L + lane;
#pragma unroll
    for (int s = 1; s < 4; ++s) {
      LAS f32x4 *Rs = R + (((s + w) & 3) * 3 + (s - 1)) * 4 * 64;
#pragma unroll
      for (int dd = 0; dd < 4; ++dd) Rs[dd * 64] = acc[dd][s];
    }
    BAR();
    const int t = 64 * rt + tile_row(w, i16);
    {
      LAS f32x4 *Rw = R + w * 3 * 4 * 64;
      LAS unsigned char *GB = L + B_GB + 32 * g;
      asm volatile("" : "+v"(GB));
      f32x4 ra[4][3], gb[4];
#pragma unroll
      for (int dd = 0; dd < 4; ++dd) {
#pragma unroll
        for (int q = 0; q < 3; ++q) ra[dd][q] = Rw[(q * 4 + dd) * 64];
        gb[dd] = *(LAS f32x4 *)(GB + 128 * (dd >> 1) + 16 * (dd & 1));
      }
      SB();
#pragma unroll
      for (int dl = 0; dl < 2; ++dl) {
        u32x4 o;
#pragma unroll
        for (int hf = 0; hf < 2; ++hf) {
          const int dd = 2 * dl + hf;
          const f32x4 s = (acc[dd][0] + ra[dd][0]) + (ra[dd][1] + ra[dd][2]);
          o[2 * hf] = pack2(pk_fma(lo2(gb[dd]), lo2(s), bf_2(xres[dl][2 * hf])));
          o[2 * hf + 1] = pack2(pk_fma(hi2(gb[dd]), hi2(s), bf_2(xres[dl][2 * hf + 1])));
        }
        *(GAS u32x4 *)(p.out + (size_t)t * DM + 64 * j + 32 * dl + 8 * g) = o;
      }
    }
    if (b == 0 && tid == 0) __hip_atomic_store(ctl + C_EPOCH, epoch, RLX_AGENT);
  }
}

extern "C" void kernel_launch(void *const *d_in, const int *in_sizes, int n_in, void *d_out, int out_size, void *d_ws,
                              size_t ws_size, hipStream_t stream) {
  Params p;
  p.x = (const u16 *)d_in[0];
  p.a_re = (const u16 *)d_in[1]; p.a_im = (const u16 *)d_in[2];
  p.b_re = (const u16 *)d_in[3]; p.b_im = (const u16 *)d_in[4];
  p.c_re = (const u16 *)d_in[5]; p.c_im = (const u16 *)d_in[6];
  p.log_dt = (const u16 *)d_in[7]; p.dskip = (const u16 *)d_in[8];
  p.w_in = (const u16 *)d_in[10]; p.w_sp = (const u16 *)d_in[11]; p.b_sp = (const u16 *)d_in[12];
  p.w_out = (const u16 *)d_in[13]; p.cvec = (const u16 *)d_in[14]; p.w_mod = (const u16 *)d_in[15];
  p.out = (u16 *)d_out;
  p.ws = (unsigned char *)d_ws;
  (void)hipFuncSetAttribute((const void *)kb_main, hipFuncAttributeMaxDynamicSharedMemorySize, B_LDS);
  kb_main<<<256, 256, B_LDS, stream>>>(p);
}
```

```cpp
#include <hip/hip_runtime.h>
#include <stdint.h>

#define GAS __attribute__((address_space(1)))
#define LAS __attribute__((address_space(3)))
#define DEVI __device__ __forceinline__
typedef unsigned short u16;
typedef float f32x2 __attribute__((ext_vector_type(2)));
typedef float f32x4 __attribute__((ext_vector_type(4)));
typedef float f32x16 __attribute__((ext_vector_type(16)));
typedef unsigned u32x2 __attribute__((ext_vector_type(2)));
typedef unsigned u32x4 __attribute__((ext_vector_type(4)));
typedef __bf16 bf16x2_t __attribute__((ext_vector_type(2)));
typedef GAS unsigned gu32;
typedef __amdgpu_buffer_rsrc_t rsrc_t;
#define RLX_AGENT __ATOMIC_RELAXED, __HIP_MEMORY_SCOPE_AGENT

constexpr int SEQ = 2048, DM = 512, NS = 128, FF = 1792, FH = 896;

constexpr size_t O_GRAN = 131072;
constexpr size_t O_E12 = O_GRAN + 32 * 8 * 64 * 16;
constexpr size_t O_MOD = 1 << 20;
constexpr size_t O_XT = 2 << 20;
constexpr size_t O_X1T = 9 << 20;
constexpr size_t O_WP = 11 << 20;
constexpr size_t O_WOF = 13 << 20;
constexpr size_t O_Z2F = 14 << 20;
constexpr size_t O_VF = 18 << 20;
constexpr size_t O_VH = 8 << 20;
constexpr int WS_USED = 22 << 20;
constexpr int C_EPOCH = 0;
constexpr int C_E1 = 1024;
constexpr int C_E2 = 4096;
constexpr int C_F3 = C_E2 + 64 * 32;
constexpr int C_F4 = C_F3 + 256 * 32;
static_assert((C_F4 + 256 * 32) * 4 <= (int)O_GRAN, "control block");

struct Params {
  const u16 *x, *a_re, *a_im, *b_re, *b_im, *c_re, *c_im, *log_dt, *dskip;
  const u16 *w_in, *w_sp, *b_sp, *w_out, *cvec, *w_mod;
  u16 *out;
  unsigned char *ws;
};

DEVI float u2f(unsigned u) { return __builtin_bit_cast(float, u); }
DEVI unsigned f2u(float f) { return __builtin_bit_cast(unsigned, f); }
DEVI float bf_lo(unsigned u) { return u2f(u << 16); }
DEVI float bf_hi(unsigned u) { return u2f(u & 0xffff0000u); }
DEVI f32x2 bf_2(unsigned u) { return f32x2{bf_lo(u), bf_hi(u)}; }
DEVI float bf2f(u16 h) { return u2f((unsigned)h << 16); }
DEVI unsigned pack2(f32x2 v) { return __builtin_bit_cast(unsigned, __builtin_convertvector(v, bf16x2_t)); }
DEVI unsigned pack_bf(float lo, float hi) { return pack2(f32x2{lo, hi}); }
DEVI float rbf(float f) { return bf_lo(pack_bf(f, f)); }
DEVI f32x2 pk_fma(f32x2 a, f32x2 b, f32x2 c) { return __builtin_elementwise_fma(a, b, c); }
DEVI f32x2 lo2(f32x4 v) { return f32x2{v[0], v[1]}; }
DEVI f32x2 hi2(f32x4 v) { return f32x2{v[2], v[3]}; }
template <int CTRL> DEVI float dpp_f(float x) {
  return __builtin_bit_cast(float, __builtin_amdgcn_update_dpp(0, __builtin_bit_cast(int, x), CTRL, 0xf, 0xf, true));
}
DEVI void wave_sum4(float (&x)[4]) {
#pragma unroll
  for (int i = 0; i < 4; ++i) x[i] += dpp_f<0xB1>(x[i]);
#pragma unroll
  for (int i = 0; i < 4; ++i) x[i] += dpp_f<0x4E>(x[i]);
#pragma unroll
  for (int i = 0; i < 4; ++i) x[i] += dpp_f<0x141>(x[i]);
#pragma unroll
  for (int i = 0; i < 4; ++i) x[i] += dpp_f<0x140>(x[i]);
#pragma unroll
  for (int i = 0; i < 4; ++i) { auto s = __builtin_amdgcn_permlane16_swap(f2u(x[i]), f2u(x[i]), false, false); x[i] = u2f(s[0]) + u2f(s[1]); }
#pragma unroll
  for (int i = 0; i < 4; ++i) { auto t = __builtin_amdgcn_permlane32_swap(f2u(x[i]), f2u(x[i]), false, false); x[i] = u2f(t[0]) + u2f(t[1]); }
}
DEVI u32x2 tr_read(LAS unsigned char *p) {
  typedef short v4i16_t __attribute__((ext_vector_type(4)));
  return __builtin_bit_cast(u32x2, __builtin_amdgcn_ds_read_tr16_b64_v4i16((LAS v4i16_t *)p));
}
DEVI u32x4 cat2(u32x2 a, u32x2 b) { return u32x4{a.x, a.y, b.x, b.y}; }
DEVI float ln_rstd(float var) {
  return __builtin_amdgcn_rcpf(rbf(__builtin_amdgcn_sqrtf(rbf(rbf(var) + 1e-5f))));
}
DEVI f32x2 gelu2(f32x2 x) {
  constexpr float K0 = -0.7978845608028654f * 2.0f * 1.4426950408889634f, K1 = K0 * 0.044715f;
  const f32x2 u = x * pk_fma(x * x, f32x2{K1, K1}, f32x2{K0, K0});
  const f32x2 d = f32x2{__builtin_amdgcn_exp2f(u[0]), __builtin_amdgcn_exp2f(u[1])} + f32x2{1.f, 1.f};
  return x * f32x2{__builtin_amdgcn_rcpf(d[0]), __builtin_amdgcn_rcpf(d[1])};
}
DEVI void block_rt_j(int b, int &rt, int &j) {
  const int x = b & 7, q = b >> 3;
  rt = 8 * (x >> 1) + (q & 7);
  j = 4 * (x & 1) + (q >> 3);
}
DEVI int tile_row(int tau, int i) { return 32 * (tau >> 1) + 8 * (i >> 2) + 4 * (tau & 1) + (i & 3); }
DEVI f32x2 cmul(f32x2 z, f32x2 v, f32x2 vr) { return pk_fma(f32x2{z[1], z[1]}, vr, f32x2{z[0], z[0]} * v); }
DEVI f32x2 crot(f32x2 v) { return f32x2{-v[1], v[0]}; }
DEVI f32x2 cexp_m(float lr2, float rev, float m) {
  const float er = __builtin_amdgcn_exp2f(lr2 * m);
  const float fr = __builtin_amdgcn_fractf(rev * m);
  return f32x2{er * __builtin_amdgcn_cosf(fr), er * __builtin_amdgcn_sinf(fr)};
}

#define SB() __builtin_amdgcn_sched_barrier(0)
#define WAIT_VM(n) asm volatile("s_waitcnt vmcnt(" #n ")" ::: "memory")
#define WAIT_LGKM0() asm volatile("s_waitcnt lgkmcnt(0)" ::: "memory")
#define BAR() asm volatile("s_waitcnt lgkmcnt(0)\n\ts_barrier" ::: "memory")
constexpr int AUX_SC1 = 16;
constexpr unsigned SPIN_CAP = 1u << 18;
constexpr int POLL_SLEEP = 2;
constexpr int R2_SLEEP = 2;

constexpr int B_RC = 4160;
constexpr int B_U = 8 * B_RC;
constexpr int U_CH = 95 * 80 + 16;
constexpr int B_RED = B_U + 2 * U_CH;
constexpr int KT_N = 400;
constexpr int KT_IM = 192;
constexpr int B_KT = 4 * B_RC;
constexpr int B_KD = B_KT + 53248;
constexpr int B_WT = B_KD + 4096;
constexpr int B_TR = B_WT + 4096;
constexpr int TR_WAVE = 16 * 1040;
constexpr int B_Z1 = B_TR + 4 * TR_WAVE;
constexpr int B_ST = B_Z1 + 14336;
constexpr int B_E = B_ST + 512;
constexpr int B_ZS = B_E + 2 * 224 * 4;
constexpr int B_GB = B_ZS + 2048;
constexpr int B_LDS = B_GB + 256;
constexpr int B_CS = B_Z1 + 10240;
constexpr int B_XU = B_Z1;
constexpr int B_LN = 0;
constexpr int B_WO = 16384;
constexpr int B_MW = 24576;
constexpr int B_MS = B_MW + 64 * 272;
constexpr int B_X = 0;
constexpr int B_ZT = 0;
static_assert(B_RED + 16384 <= B_WT && B_MS + 128 <= B_WT && 65536 <= B_WT && (B_KD & 15) == 0 && (B_WT & 15) == 0 &&
              4 * 3 * 7 * 1024 <= B_Z1 && B_LDS <= 163840, "lds map");
constexpr int G1_WIN = 4;
constexpr int G2_WIN = 24;

DEVI rsrc_t mk_rsrc(const void *p, int bytes) { return __builtin_amdgcn_make_buffer_rsrc((void *)p, 0, bytes, 0x00020000); }
DEVI u32x4 ld128(rsrc_t rs, int voff, int soff) { return __builtin_amdgcn_raw_buffer_load_b128(rs, voff, soff, 0); }
DEVI u32x4 ld_sc1(rsrc_t rs, int voff, int soff) { return __builtin_amdgcn_raw_buffer_load_b128(rs, voff, soff, AUX_SC1); }
DEVI u32x2 ld_sc1_64(rsrc_t rs, int voff, int soff) { return __builtin_amdgcn_raw_buffer_load_b64(rs, voff, soff, AUX_SC1); }
DEVI unsigned ld_sc1_32(rsrc_t rs, int voff, int soff) { return __builtin_amdgcn_raw_buffer_load_b32(rs, voff, soff, AUX_SC1); }
DEVI void st_sc1(rsrc_t rs, int voff, int soff, u32x4 v) { __builtin_amdgcn_raw_buffer_store_b128(v, rs, voff, soff, AUX_SC1); }
DEVI void st_sc1_64(rsrc_t rs, int voff, int soff, u32x2 v) { __builtin_amdgcn_raw_buffer_store_b64(v, rs, voff, soff, AUX_SC1); }
DEVI void st_sc1_32(rsrc_t rs, int voff, int soff, unsigned v) { __builtin_amdgcn_raw_buffer_store_b32(v, rs, voff, soff, AUX_SC1); }
DEVI float pick4(f32x4 v, int r) { return r == 0 ? v[0] : r == 1 ? v[1] : r == 2 ? v[2] : v[3]; }
template <bool GE> DEVI void wave_poll(gu32 *word, bool on, unsigned want) {
  for (unsigned spins = 0; spins < SPIN_CAP; ++spins) {
    const unsigned v = on ? __hip_atomic_load(word, RLX_AGENT) : want;
    if (__all(GE ? v >= want : v == want)) break;
    __builtin_amdgcn_s_sleep(POLL_SLEEP);
  }
  __builtin_amdgcn_fence(__ATOMIC_ACQUIRE, "wavefront");
}

DEVI void mma16(f32x4 &c, u32x4 a, u32x4 b) { asm volatile("v_mfma_f32_16x16x32_bf16 %0, %1, %2, %0" : "+v"(c) : "v"(a), "v"(b)); }
DEVI void mma16a(f32x4 &c, u32x4 a, u32x4 b) { asm volatile("v_mfma_f32_16x16x32_bf16 %0, %1, %2, %0" : "+v"(c) : "v"(a), "a"(b)); }
DEVI void mma32(f32x16 &c, u32x4 a, u32x4 b) { asm volatile("v_mfma_f32_32x32x16_bf16 %0, %1, %2, %0" : "+v"(c) : "v"(a), "v"(b)); }
DEVI void mma16z(f32x4 &c, u32x4 a, u32x4 b) { asm volatile("v_mfma_f32_16x16x32_bf16 %0, %1, %2, 0" : "=&v"(c) : "v"(a), "v"(b)); }
DEVI void mma16n(f32x4 &c, u32x4 a, u32x4 b) { asm volatile("s_nop 1\n\tv_mfma_f32_16x16x32_bf16 %0, %1, %2, %0" : "+v"(c) : "v"(a), "v"(b)); }
DEVI void mma16zn(f32x4 &c, u32x4 a, u32x4 b) { asm volatile("s_nop 1\n\tv_mfma_f32_16x16x32_bf16 %0, %1, %2, 0" : "=&v"(c) : "v"(a), "v"(b)); }
DEVI void mma16az(f32x4 &c, u32x4 a, u32x4 b) { asm volatile("v_mfma_f32_16x16x32_bf16 %0, %1, %2, 0" : "=&v"(c) : "v"(a), "a"(b)); }
DEVI void mma32z(f32x16 &c, u32x4 a, u32x4 b) { asm volatile("s_nop 1\n\tv_mfma_f32_32x32x16_bf16 %0, %1, %2, 0" : "=&v"(c) : "v"(a), "v"(b)); }
DEVI void mma32n(f32x16 &c, u32x4 a, u32x4 b) { asm volatile("s_nop 1\n\tv_mfma_f32_32x32x16_bf16 %0, %1, %2, %0" : "+v"(c) : "v"(a), "v"(b)); }
#define MFMA_FENCE() do { SB(); asm volatile("s_nop 7\n\ts_nop 7" ::: "memory"); SB(); } while (0)
#define FRESH_IDS()                                   \
  int tid = threadIdx.x;                              \
  asm volatile("" : "+v"(tid));                       \
  const int lane = tid & 63, i16 = lane & 15, g = lane >> 4; \
  (void)i16; (void)g
DEVI u32x4 opaque_ones() {
  u32x4 o = u32x4{0x3f803f80u, 0x3f803f80u, 0x3f803f80u, 0x3f803f80u};
  asm volatile("" : "+v"(o));
  return o;
}
struct Transit {
  rsrc_t rw;
  int v[4], so, la;
  DEVI void begin(int lane, int w, int rt, int tau) {
#pragma unroll
    for (int c = 0; c < 4; ++c) v[c] = lane * 16 + c * 4096;
    so = ((64 * rt + 32 * (tau >> 1) + 4 * (tau & 1)) * SEQ + 512 * w) * 2;
    la = B_TR + w * TR_WAVE;
  }
  DEVI void step(int i) {
    asm volatile("s_mov_b32 m0, %0\n\ts_add_u32 %0, %0, 1040\n\tbuffer_load_dwordx4 %2, %3, %1 offen lds"
                 : "+s"(la) : "s"(so), "v"(v[i & 3]), "s"(rw) : "memory", "scc");
    if ((i & 3) == 3) so += 32768;
  }
};
DEVI void transit_read(LAS unsigned char *L, int lane, int w, u32x4 (&stash)[16][4], int slot) {
  LAS unsigned char *T = L + B_TR + w * TR_WAVE + (lane & 15) * 1040 + (lane >> 4) * 16;
  asm volatile("" : "+v"(T));
#pragma unroll
  for (int k = 0; k < 16; ++k) {
    const u32x4 v = *(LAS u32x4 *)(T + 64 * k);
    if (slot == 0) stash[k][0] = v; else if (slot == 1) stash[k][1] = v; else if (slot == 2) stash[k][2] = v; else stash[k][3] = v;
  }
  WAIT_LGKM0();
}

template <int VAR>
DEVI void gemm1_loop(rsrc_t rs, LAS unsigned char *L, int lane, const int (&wo)[4], u32x4 (&wf)[G1_WIN][4], f32x4 (&acc)[4][4],
                     f32x4 (&ssum)[2], f32x4 (&ssq)[2]) {
  const int g = lane >> 4, q = (lane & 15) >> 2, pq = lane & 3;
  LAS unsigned char *xb[4];
#pragma unroll
  for (int tau = 0; tau < 4; ++tau)
    xb[tau] = L + B_X + (8 * g + q) * 128 + (((4 * (tau >> 1) + pq) ^ (4 * (q >> 1))) * 16) + (((tau & 1) ^ (g & 1)) * 8);
  constexpr bool act[4] = {true, VAR != 3, true, VAR != 2};
  const u32x4 ones = opaque_ones();
  u32x2 xl[2][4], xh[2][4];
#pragma unroll
  for (int tau = 0; tau < 4; ++tau) { xl[0][tau] = tr_read(xb[tau]); xh[0][tau] = tr_read(xb[tau] + 512); }
  SB();
#pragma unroll
  for (int ks = 0; ks < 16; ++ks) {
    const int c = ks & 1, n = c ^ 1;
    u32x4 cur[4];
#pragma unroll
    for (int s = 0; s < 4; ++s) cur[s] = wf[ks % G1_WIN][s];
#pragma unroll
    for (int tau = 0; tau < 4; ++tau) {
      const u32x4 xf = cat2(xl[c][tau], xh[c][tau]);
      if (ks == 0) mma16z(acc[0][tau], xf, cur[0]); else mma16(acc[0][tau], xf, cur[0]);
      if (ks + 1 < 16) xl[n][tau] = tr_read(xb[tau] + (ks + 1) * 4096);
      SB();
      if (act[1]) { if (ks == 0) mma16z(acc[1][tau], xf, cur[1]); else mma16(acc[1][tau], xf, cur[1]); }
      else if (tau >= 2) { if (ks == 0) mma16z(ssum[tau - 2], xf, ones); else mma16(ssum[tau - 2], xf, ones); }
      if (ks + 1 < 16) xh[n][tau] = tr_read(xb[tau] + (ks + 1) * 4096 + 512);
      SB();
      if (ks == 0) mma16z(acc[2][tau], cur[2], xf); else mma16(acc[2][tau], cur[2], xf);
      SB();
      if (act[3]) { if (ks == 0) mma16z(acc[3][tau], cur[3], xf); else mma16(acc[3][tau], cur[3], xf); }
      else if (tau < 2) { if (ks == 0) mma16z(ssum[tau], xf, ones); else mma16(ssum[tau], xf, ones); }
      if (VAR == 2 && tau < 2) { if (ks == 0) mma16z(ssq[tau], xf, xf); else mma16(ssq[tau], xf, xf); }
      if (VAR == 3 && tau >= 2) { if (ks == 0) mma16z(ssq[tau - 2], xf, xf); else mma16(ssq[tau - 2], xf, xf); }
      if (ks + G1_WIN < 16 && act[tau]) wf[ks % G1_WIN][tau] = ld_sc1(rs, wo[tau], (ks + G1_WIN) * 1024);
      SB();
    }
  }
  MFMA_FENCE();
}

__global__ __launch_bounds__(256, 1) void kb_main(Params p) {
  extern __shared__ __attribute__((aligned(16))) unsigned char smem_raw[];
  LAS unsigned char *L = (LAS unsigned char *)smem_raw;
  const int b = blockIdx.x;
  const int w = __builtin_amdgcn_readfirstlane(threadIdx.x >> 6);
  const rsrc_t rs = mk_rsrc(p.ws, WS_USED);
  gu32 *ctl = (gu32 *)p.ws;
  int rt, j;
  block_rt_j(b, rt, j);
  const int d0 = 64 * (b & 7) + 2 * (b >> 3);
  const unsigned epoch_raw = __hip_atomic_load(ctl + C_EPOCH, RLX_AGENT);
  const u32x4 e1_touch = ld_sc1(rs, (threadIdx.x & 63) * 16, (C_E1 + (b & 7) * 256) * 4);
  unsigned epoch;

  u32x4 stash[16][4];
  Transit tr;
  tr.rw = mk_rsrc(p.w_sp, SEQ * SEQ * 2);

  unsigned prm[2][6], ldt[2];
  f32x2 z[2][2], zm[2], zmr[2], y[2][2], ym[2], ymr[2];
  int kidx[12];
  {
    FRESH_IDS();
    const int t0 = 8 * b;
    const rsrc_t rx = mk_rsrc(p.x, SEQ * DM * 2);
    u32x4 xr[2];
#pragma unroll
    for (int r = 0; r < 2; ++r) xr[r] = ld128(rx, lane * 16, (t0 + 2 * w + r) * (DM * 2));
    u32x4 jm[4], jo[2];
    unsigned cv = 0;
    const int mcg = b % 24, mkb = b / 24;
    if (b < 192) {
      const rsrc_t rm = mk_rsrc(p.w_mod, DM * 3072 * 2);
#pragma unroll
      for (int q = 0; q < 4; ++q) jm[q] = ld128(rm, g * (3072 * 2) + i16 * 16, ((64 * mkb + 16 * w + 4 * q) * 3072 + 128 * mcg) * 2);
      cv = ((const GAS unsigned *)p.cvec)[32 * mkb + (lane & 31)];
    }
    if (b >= 144) {
      const rsrc_t ro = mk_rsrc(p.w_out, FH * DM * 2);
      const int bb = b - 144;
#pragma unroll
      for (int q = 0; q < 2; ++q) jo[q] = ld128(ro, (tid >> 4) * (DM * 2) + (tid & 15) * 16, ((32 * (bb >> 2) + 16 * q) * DM + 128 * (bb & 3)) * 2);
    }
    {
      const int po = d0 * NS + (w & 1) * 64 + lane;
      const u16 *pa[6] = {p.a_re, p.a_im, p.b_re, p.b_im, p.c_re, p.c_im};
#pragma unroll
      for (int a = 0; a < 6; ++a)
#pragma unroll
        for (int c = 0; c < 2; ++c) prm[c][a] = ((const GAS u16 *)pa[a])[po + c * NS];
#pragma unroll
      for (int c = 0; c < 2; ++c) ldt[c] = ((const GAS u16 *)p.log_dt)[d0 + c];
    }
    SB();
    const int s0o = (64 * rt + tile_row(w, i16)) * (SEQ * 2) + (512 * w + 8 * g) * 2;
#pragma unroll
    for (int q = 0; q < 6; ++q) stash[q][0] = ld128(tr.rw, s0o, q * 64);
    SB();
#pragma unroll
    for (int e = 0; e < 12; ++e) {
      const int l = 2047 - (8 * tid + e);
      const int lr = (int)rbf((float)(e < 8 ? l : max(l, 0)));
      kidx[e] = min(lr, 384 + (lr >> 2)) * 2;
      asm volatile("" : "+v"(kidx[e]));
    }
    SB();
    f32x2 xv[2][4], xn[2][4];
    {
      float st[4];
#pragma unroll
      for (int r = 0; r < 2; ++r) {
#pragma unroll
        for (int e = 0; e < 4; ++e) xv[r][e] = bf_2(xr[r][e]);
        const f32x2 s2 = (xv[r][0] + xv[r][1]) + (xv[r][2] + xv[r][3]);
        const f32x2 q2 = pk_fma(xv[r][0], xv[r][0], xv[r][1] * xv[r][1]) + pk_fma(xv[r][2], xv[r][2], xv[r][3] * xv[r][3]);
        st[2 * r] = s2[0] + s2[1];
        st[2 * r + 1] = q2[0] + q2[1];
      }
      wave_sum4(st);
#pragma unroll
      for (int r = 0; r < 2; ++r) {
        const float mean = st[2 * r] * (1.f / DM), var = __builtin_fmaf(st[2 * r + 1], 1.f / DM, -mean * mean);
        const float rsd = ln_rstd(fmaxf(var, 0.f)), mb = -rbf(mean) * rsd;
#pragma unroll
        for (int e = 0; e < 4; ++e) xn[r][e] = pk_fma(xv[r][e], f32x2{rsd, rsd}, f32x2{mb, mb});
      }
    }
    {
      u32x4 a0, a1, n0, n1;
#pragma unroll
      for (int e = 0; e < 4; ++e) {
        const unsigned lo = __builtin_amdgcn_perm(xr[1][e], xr[0][e], 0x05040100u), hi = __builtin_amdgcn_perm(xr[1][e], xr[0][e], 0x07060302u);
        const unsigned nl = pack_bf(xn[0][e][0], xn[1][e][0]), nh = pack_bf(xn[0][e][1], xn[1][e][1]);
        if (e < 2) { a0[2 * e] = lo; a0[2 * e + 1] = hi; n0[2 * e] = nl; n0[2 * e + 1] = nh; }
        else { a1[2 * e - 4] = lo; a1[2 * e - 3] = hi; n1[2 * e - 4] = nl; n1[2 * e - 3] = nh; }
      }
      LAS u32x4 *B0 = (LAS u32x4 *)(L + B_LN + w * 2048) + 2 * lane;
      B0[0] = a0; B0[1] = a1; B0[512] = n0; B0[513] = n1;
    }
    if (b < 192) {
      LAS unsigned char *MW = L + B_MW + (16 * w + g) * 272 + i16 * 16;
#pragma unroll
      for (int q = 0; q < 4; ++q) *(LAS u32x4 *)(MW + q * 4 * 272) = jm[q];
      if (w == 0 && lane < 32) {
        const f32x2 c2 = bf_2(cv);
        const f32x2 e2 = f32x2{__builtin_amdgcn_exp2f(c2[0] * -1.4426950408889634f), __builtin_amdgcn_exp2f(c2[1] * -1.4426950408889634f)} + f32x2{1.f, 1.f};
        ((LAS unsigned *)(L + B_MS))[lane] = pack2(c2 * f32x2{__builtin_amdgcn_rcpf(e2[0]), __builtin_amdgcn_rcpf(e2[1])});
      }
    }
    if (b >= 144) {
#pragma unroll
      for (int q = 0; q < 2; ++q) *(LAS u32x4 *)(L + B_WO + q * 4096 + tid * 16) = jo[q];
    }
    BAR();
    {
      LAS unsigned *S = (LAS unsigned *)(L + B_LN) + ((tid & 2) ? 0 : 2048) + 2 * (tid >> 2) + (tid & 1);
      u32x4 v[4];
#pragma unroll
      for (int a = 0; a < 4; ++a) { LAS unsigned *Sa = S + a * 128; v[a] = u32x4{Sa[0], Sa[512], Sa[1024], Sa[1536]}; }
#pragma unroll
      for (int a = 0; a < 4; ++a) st_sc1(rs, tid * 16 + a * 4096, (int)O_XT + b * 16384, v[a]);
    }
    if (b < 192) {
      u32x4 sa4[2], wb[2][2];
      LAS unsigned char *Bq = L + B_MW + (8 * g + (i16 >> 2)) * 272 + (lane & 3) * 8 + 64 * w;
#pragma unroll
      for (int ks = 0; ks < 2; ++ks) {
        sa4[ks] = *(LAS u32x4 *)(L + B_MS + ks * 64 + g * 16);
#pragma unroll
        for (int ct = 0; ct < 2; ++ct) wb[ks][ct] = cat2(tr_read(Bq + ks * 32 * 272 + ct * 32), tr_read(Bq + (ks * 32 + 4) * 272 + ct * 32));
      }
      f32x4 m0, m1;
      mma16zn(m0, sa4[0], wb[0][0]);
      mma16zn(m1, sa4[0], wb[0][1]);
      mma16n(m0, sa4[1], wb[1][0]);
      mma16n(m1, sa4[1], wb[1][1]);
      MFMA_FENCE();
      asm volatile("" :: "v"(m0), "v"(m1));
      if (lane < 32) st_sc1_32(rs, lane * 4, (int)O_MOD + (mkb * 3072 + 128 * mcg + 32 * w) * 4, f2u(g == 0 ? m0[0] : m1[0]));
    }
    if (b >= 144) {
      const int bb = b - 144;
#pragma unroll
      for (int k = 0; k < 2; ++k) {
        const int dl = 2 * w + k;
        LAS unsigned char *A = L + B_WO + (4 * g + (i16 >> 2)) * 256 + (32 * (dl >> 1) + 8 * (lane & 3) + 4 * (dl & 1)) * 2;
        st_sc1(rs, lane * 16, (int)O_WOF + ((8 * (bb & 3) + dl) * 28 + (bb >> 2)) * 1024, cat2(tr_read(A), tr_read(A + 16 * 256)));
      }
    }
    if (b == 255) {
#pragma unroll
      for (int q = 0; q < 2; ++q) st_sc1(rs, tid * 16 + q * 4096, C_E2 * 4, u32x4{0, 0, 0, 0});
    }
    SB();
    {
    const int part = w >> 1;
#pragma unroll
    for (int c = 0; c < 2; ++c) {
      const float dt = rbf(expf(bf_lo(ldt[c])));
      const float lr2 = dt * bf_lo(prm[c][0]) * 1.4426950408889634f, rev = dt * bf_lo(prm[c][1]) * 0.15915494309189535f;
      const f32x2 cc = {bf_lo(prm[c][4]), bf_lo(prm[c][5])};
      const f32x2 beta = cmul(f32x2{dt * bf_lo(prm[c][2]), dt * bf_lo(prm[c][3])}, cc, crot(cc));
      const f32x2 e0 = cexp_m(lr2, rev, part ? 512.f : 0.f), e1 = cexp_m(lr2, rev, part ? 64.f : 16.f), w1 = cexp_m(lr2, rev, part ? 4.f : 1.f);
      const f32x2 e1r = crot(e1);
      z[c][0] = cmul(beta, e0, crot(e0));
      z[c][1] = cmul(z[c][0], e1, e1r);
      zm[c] = cmul(e1, e1, e1r); zmr[c] = crot(zm[c]);
      y[c][0] = f32x2{1.f, 0.f};
      y[c][1] = w1;
      ym[c] = cmul(w1, w1, crot(w1)); ymr[c] = crot(ym[c]);
    }
#pragma unroll
    for (int c = 0; c < 2; ++c)
      asm volatile("" : "+v"(z[c][0]), "+v"(z[c][1]), "+v"(zm[c]), "+v"(zmr[c]), "+v"(y[c][1]), "+v"(ym[c]), "+v"(ymr[c]));
    }
    SB();
    asm volatile("" :: "v"(e1_touch));
    epoch = __builtin_amdgcn_readfirstlane(epoch_raw) + 1u;
    WAIT_VM(0);
    BAR();
    if (tid < 8) __hip_atomic_store(ctl + C_E1 + tid * 256 + b, epoch, RLX_AGENT);
    SB();
  }

  {
    FRESH_IDS();
    {
      const int part = w >> 1, n = (w & 1) * 64 + lane;
      const int s0o = (64 * rt + tile_row(w, i16)) * (SEQ * 2) + (512 * w + 8 * g) * 2;
      LAS unsigned char *T0 = L + B_KT + n * KT_N + part * 64, *T1 = L + B_TR + n * KT_N + part * 64;
      asm volatile("" : "+v"(T0), "+v"(T1));
      unsigned p_touch[2];
      {
        const int dw = lane < 8 ? C_F3 + ((8 * w + lane) * 8 + j) * 32 : lane < 16 ? C_F4 + (rt * 8 + (lane & 7)) * 32 : C_E2 + ((b & 7) * 8 + (lane & 7)) * 32;
        p_touch[0] = ld_sc1_32(rs, dw * 4, 0);
        p_touch[1] = ld_sc1_32(rs, lane * 128, (int)O_GRAN + rt * 8192);
      }
#pragma unroll
      for (int grp = 0; grp < 6; ++grp) {
        u32x4 re[2], im[2];
#pragma unroll
        for (int q = 0; q < 4; ++q) {
#pragma unroll
          for (int c = 0; c < 2; ++c) {
            if (grp < 4) {
              re[c][q] = pack_bf(z[c][0][0], z[c][1][0]); im[c][q] = pack_bf(z[c][0][1], z[c][1][1]);
              z[c][0] = cmul(z[c][0], zm[c], zmr[c]); z[c][1] = cmul(z[c][1], zm[c], zmr[c]);
            } else {
              re[c][q] = pack_bf(y[c][0][0], y[c][1][0]); im[c][q] = pack_bf(-y[c][0][1], -y[c][1][1]);
              y[c][0] = cmul(y[c][0], ym[c], ymr[c]); y[c][1] = cmul(y[c][1], ym[c], ymr[c]);
            }
          }
        }
#pragma unroll
        for (int c = 0; c < 2; ++c) {
          LAS unsigned char *T = (c ? T1 : T0) + (grp < 4 ? 16 * grp : 128 - part * 32 + 16 * (grp - 4));
          *(LAS u32x4 *)T = re[c];
          *(LAS u32x4 *)(T + KT_IM) = im[c];
        }
        stash[6 + grp][0] = ld128(tr.rw, s0o, (6 + grp) * 64);
        SB();
      }
      BAR();
      u32x4 e1f = ld_sc1(rs, lane * 16, (C_E1 + (b & 7) * 256) * 4);
      f32x4 acc[2];
      u32x4 a[2][8], bq[2][8];
#pragma unroll
      for (int c = 0; c < 2; ++c) {
        LAS unsigned char *TA = L + (c ? B_TR : B_KT) + (4 * g + (i16 >> 3)) * KT_N + ((i16 >> 2) & 1) * KT_IM + 8 * (lane & 3) + 32 * w;
        LAS unsigned char *TB = L + (c ? B_TR : B_KT) + (4 * g + (i16 >> 3)) * KT_N + ((i16 >> 2) & 1) * KT_IM + 8 * (lane & 3) + 128 + 32 * (w >> 1);
#pragma unroll
        for (int ks = 0; ks < 8; ++ks) {
          a[c][ks] = cat2(tr_read(TA + ks * 16 * KT_N), tr_read(TA + (ks * 16 + 2) * KT_N));
          bq[c][ks] = cat2(tr_read(TB + ks * 16 * KT_N), tr_read(TB + (ks * 16 + 2) * KT_N));
        }
      }
      SB();
#pragma unroll
      for (int q = 12; q < 14; ++q) stash[q][0] = ld128(tr.rw, s0o, q * 64);
      mma16zn(acc[0], a[0][0], bq[0][0]);
      mma16zn(acc[1], a[1][0], bq[1][0]);
#pragma unroll
      for (int ks = 1; ks < 8; ++ks) { mma16(acc[0], a[0][ks], bq[0][ks]); mma16(acc[1], a[1][ks], bq[1][ks]); }
#pragma unroll
      for (int q = 14; q < 16; ++q) stash[q][0] = ld128(tr.rw, s0o, q * 64);
      MFMA_FENCE();
#pragma unroll
      for (int c = 0; c < 2; ++c) {
        LAS u16 *KD = (LAS u16 *)(L + B_KD + c * 2048) + (16 * w + 4 * g) * 16 + i16;
        const unsigned k01 = pack_bf(acc[c][0], acc[c][1]), k23 = pack_bf(acc[c][2], acc[c][3]);
        KD[0] = (u16)k01; KD[16] = (u16)(k01 >> 16); KD[32] = (u16)k23; KD[48] = (u16)(k23 >> 16);
      }
      asm volatile("" :: "v"(p_touch[0]), "v"(p_touch[1]));
      if (w == 0) {
        for (unsigned spins = 0; spins < SPIN_CAP; ++spins) {
          if (__all(((e1f[0] ^ epoch) | (e1f[1] ^ epoch) | (e1f[2] ^ epoch) | (e1f[3] ^ epoch)) == 0u)) break;
          __builtin_amdgcn_s_sleep(POLL_SLEEP);
          e1f = ld_sc1(rs, lane * 16, (C_E1 + (b & 7) * 256) * 4);
        }
        __builtin_amdgcn_fence(__ATOMIC_ACQUIRE, "wavefront");
      }
      BAR();
    }
    const int ch = w >> 1, pp = w & 1;
    const int i32 = lane & 31, h = lane >> 5;
    const int wF = b < 224 ? b >> 1 : 0, wks = 8 * (b & 1) + 2 * w;
    u32x4 xn[4];
#pragma unroll
    for (int i = 0; i < 4; ++i) xn[i] = ld_sc1(rs, (lane >> 2) * 16384 + (lane & 3) * 16, (int)O_XT + (64 * w + 16 * i) * 16384 + d0 * 32);
    float mraw = u2f(ld_sc1_32(rs, (lane & 7) * (3072 * 4) + ((lane >> 4) % 3) * 2048 + ((lane >> 3) & 1) * 4, (int)O_MOD + d0 * 4));
    float cbs[2][8];
#pragma unroll
    for (int q = 0; q < 8; ++q) {
      cbs[0][q] = u2f(ld_sc1_32(rs, lane * 4, (int)O_MOD + (q * 3072 + 4 * 512 + 32 * wks) * 4));
      cbs[1][q] = u2f(ld_sc1_32(rs, lane * 4, (int)O_MOD + (q * 3072 + 3 * 512 + 32 * wks) * 4));
    }
    float dsk = bf2f(((const GAS u16 *)p.dskip)[d0 + ch]);
    const rsrc_t rin = mk_rsrc(p.w_in, DM * FF * 2);
    u32x4 wr[2];
#pragma unroll
    for (int k = 0; k < 2; ++k) wr[k] = ld128(rin, (lane >> 1) * (FF * 2) + (lane & 1) * 16, (32 * (wks + k) * FF + 16 * wF) * 2);
    SB();
    {
      unsigned v[2][12];
#pragma unroll
      for (int c = 0; c < 2; ++c)
#pragma unroll
        for (int e = 0; e < 12; ++e) v[c][e] = *(LAS u16 *)(L + B_KD + c * 2048 + kidx[e]);
      SB();
#pragma unroll
      for (int c = 0; c < 2; ++c) {
#pragma unroll
        for (int e = 8; e < 12; ++e) v[c][e] = tid == 255 ? 0u : v[c][e];
        unsigned D[6];
#pragma unroll
        for (int e = 0; e < 6; ++e) D[e] = v[c][2 * e] | (v[c][2 * e + 1] << 16);
        unsigned A[5];
#pragma unroll
        for (int e = 0; e < 5; ++e) A[e] = __builtin_amdgcn_alignbit(D[e + 1], D[e], 16);
        LAS unsigned char *R = L + c * 4 * B_RC + 16 * tid;
        *(LAS u32x4 *)(R + 0 * B_RC) = u32x4{D[0], D[1], D[2], D[3]};
        *(LAS u32x4 *)(R + 1 * B_RC) = u32x4{A[0], A[1], A[2], A[3]};
        *(LAS u32x4 *)(R + 2 * B_RC) = u32x4{D[1], D[2], D[3], D[4]};
        *(LAS u32x4 *)(R + 3 * B_RC) = u32x4{A[1], A[2], A[3], A[4]};
      }
      if (tid < 32) *(LAS u32x4 *)(L + (tid >> 2) * B_RC + 4096 + 16 * (tid & 3)) = u32x4{0, 0, 0, 0};
    }
    SB();
    if (tid < 155) {
      *(LAS u32x4 *)(L + B_U + 16 * tid) = u32x4{0, 0, 0, 0};
      *(LAS u32x4 *)(L + B_U + U_CH + 16 * tid) = u32x4{0, 0, 0, 0};
    }
    mraw += dpp_f<0xB1>(mraw); mraw += dpp_f<0x4E>(mraw); mraw += dpp_f<0x141>(mraw);
    const unsigned mval = f2u(rbf(mraw));
    const float gaw = u2f(__builtin_amdgcn_readlane(mval, 32 + 8 * ch));
    {
      const int c = lane & 1;
      const bool isx = lane & 2;
      const float sa0 = u2f(__builtin_amdgcn_readlane(mval, 0)), sa1 = u2f(__builtin_amdgcn_readlane(mval, 8));
      const float ca0 = u2f(__builtin_amdgcn_readlane(mval, 16)), ca1 = u2f(__builtin_amdgcn_readlane(mval, 24));
      const float sa = c ? sa1 : sa0, c1 = rbf(1.f + (c ? ca1 : ca0));
      LAS unsigned char *PU = L + (isx ? B_XU + c * 5120 : B_U + c * U_CH + 31 * 80) + (16 * w + g) * 80 + 16 * ((lane >> 2) & 3);
#pragma unroll
      for (int i = 0; i < 4; ++i) {
        u32x4 hv;
#pragma unroll
        for (int e = 0; e < 4; ++e) { const unsigned m = pack2(pk_fma(bf_2(xn[i][e]), f32x2{c1, c1}, f32x2{sa, sa})); hv[e] = isx ? xn[i][e] : m; }
        *(LAS u32x4 *)(PU + i * 320) = hv;
      }
    }
    LAS unsigned char *WT = L + B_WT + w * 1024;
    *(LAS u32x4 *)(WT + lane * 16) = wr[0];
    u32x4 sbv[2];
    f32x2 c2[2][4];
    {
      float t[2];
#pragma unroll
      for (int a = 0; a < 2; ++a) t[a] = ((cbs[a][0] + cbs[a][1]) + (cbs[a][2] + cbs[a][3])) + ((cbs[a][4] + cbs[a][5]) + (cbs[a][6] + cbs[a][7]));
      LAS u16 *CS = (LAS u16 *)(L + B_CS + w * 256);
      const unsigned pk = pack_bf(1.f + rbf(t[0]), t[1]);
      CS[lane] = (u16)pk; CS[64 + lane] = (u16)(pk >> 16);
#pragma unroll
      for (int k = 0; k < 2; ++k) {
        const u32x4 cq = *(LAS u32x4 *)(L + B_CS + w * 256 + 64 * k + 16 * g);
        sbv[k] = *(LAS u32x4 *)(L + B_CS + w * 256 + 128 + 64 * k + 16 * g);
#pragma unroll
        for (int e = 0; e < 4; ++e) c2[k][e] = bf_2(cq[e]);
      }
    }
    u32x4 ones = opaque_ones();
    asm volatile("" : "+v"(sbv[0]), "+v"(sbv[1]), "+v"(dsk));
    tr.begin(lane, w, rt, (w + 1) & 3);
    BAR();

    f32x16 acc0, acc1;
    u32x4 raw[2], scl[2];
    f32x4 e1, e2;
    {
      LAS unsigned char *Al = L + ch * 4 * B_RC + (3 - (i32 & 3)) * B_RC + (2044 - 4 * (i32 >> 2) + 8 * h) * 2 - 64 * pp - 128 * 15 - 2048;
      LAS unsigned char *Ah = Al + 8;
      asm volatile("" : "+v"(Ah));
      LAS unsigned char *Bb = L + B_U + ch * U_CH + 80 * i32 + 16 * h - 80 * pp;
      LAS unsigned char *WA = WT + (8 * g + (i16 >> 2)) * 32 + 8 * (lane & 3);
      u32x2 a0l[3], a0h[3], a1l[3], a1h[3];
      u32x4 b0[3], b1[3];
#define CONV_LOAD_A(n, sl)                                                                          \
  do {                                                                                              \
    const int o_ = 128 * (15 - ((n) >> 1)) + 32 * ((n) & 1);                                        \
    a0l[sl] = *(LAS u32x2 *)(Al + o_ + 2048); a0h[sl] = *(LAS u32x2 *)(Ah + o_ + 2048);             \
  } while (0)
#define CONV_LOAD_A1(n, sl)                                                                         \
  do {                                                                                              \
    const int o_ = 128 * (15 - ((n) >> 1)) + 32 * ((n) & 1);                                        \
    a1l[sl] = *(LAS u32x2 *)(Al + o_); a1h[sl] = *(LAS u32x2 *)(Ah + o_);                           \
  } while (0)
#define CONV_LOAD_B(n, sl)                                                                          \
  do {                                                                                              \
    b0[sl] = *(LAS u32x4 *)(Bb + (31 - 2 * ((n) >> 1)) * 80 + 32 * ((n) & 1));                      \
    b1[sl] = *(LAS u32x4 *)(Bb + (63 - 2 * ((n) >> 1)) * 80 + 32 * ((n) & 1));                      \
  } while (0)
#pragma unroll
      for (int n = 0; n < 2; ++n) { CONV_LOAD_A(n, n); CONV_LOAD_B(n, n); CONV_LOAD_A1(n, n); }
      SB();
#pragma unroll
      for (int n = 0; n < 32; ++n) {
        const int c = n % 3, nx = (n + 2) % 3;
        const u32x4 A0 = cat2(a0l[c], a0h[c]), A1 = cat2(a1l[c], a1h[c]);
        if (n == 0) mma32z(acc0, A0, b0[c]); else if (n == 1) mma32n(acc0, A0, b0[c]); else mma32(acc0, A0, b0[c]);
        if (n + 2 < 32) CONV_LOAD_A(n + 2, nx);
        SB();
        if (n == 0) mma32z(acc1, A0, b1[c]); else mma32(acc1, A0, b1[c]);
        if (n + 2 < 32) CONV_LOAD_B(n + 2, nx);
        SB();
        if (n < 2) mma32n(acc1, A1, b0[c]); else mma32(acc1, A1, b0[c]);
        if (n + 2 < 32) CONV_LOAD_A1(n + 2, nx);
        if (n < 16) tr.step(n);
        if (n == 2 || n == 8) raw[n == 8] = cat2(tr_read(WA), tr_read(WA + 128));
        if (n == 5) *(LAS u32x4 *)(WT + lane * 16) = wr[1];
        if (n == 4 || n == 5 || n == 10 || n == 11) {
          const int k = n >= 10, e0 = 2 * (n & 1);
          scl[k][e0] = pack2(bf_2(raw[k][e0]) * c2[k][e0]);
          scl[k][e0 + 1] = pack2(bf_2(raw[k][e0 + 1]) * c2[k][e0 + 1]);
        }
        if (n == 6) { mma16zn(e1, ones, scl[0]); mma16z(e2, sbv[0], raw[0]); }
        if (n == 12) { mma16n(e1, ones, scl[1]); mma16(e2, sbv[1], raw[1]); }
        SB();
      }
      MFMA_FENCE();
    }
    if (pp) { const f32x16 t = acc0; acc0 = acc1; acc1 = t; }
    {
      LAS f32x4 *R = (LAS f32x4 *)(L + B_RED + (ch * 2 + (1 - pp)) * 4096) + lane;
#pragma unroll
      for (int rg = 0; rg < 4; ++rg) R[rg * 64] = f32x4{acc1[4 * rg], acc1[4 * rg + 1], acc1[4 * rg + 2], acc1[4 * rg + 3]};
    }
    asm volatile("" :: "v"(e1), "v"(e2));
    if (b < 224) {
#pragma unroll
      for (int k = 0; k < 2; ++k) st_sc1(rs, lane * 16, (int)O_WP + (wF * 16 + wks + k) * 1024, scl[k]);
    }
    if (g == 0) {
      LAS float *EP = (LAS float *)(L + B_WT + 1024 * w + 768) + i16;
      EP[0] = e1[0]; EP[16] = e2[0];
    }
    BAR();
    if (b < 224 && tid < 32) {
      LAS float *EP = (LAS float *)(L + B_WT + 768) + tid;
      st_sc1_32(rs, (tid & 15) * 4 + (tid >> 4) * (FF * 4), (int)O_E12 + ((b & 1) * 2 * FF + 16 * wF) * 4, f2u((EP[0] + EP[256]) + (EP[512] + EP[768])));
    }
    {
      const int d = d0 + ch;
      LAS f32x4 *R = (LAS f32x4 *)(L + B_RED + (ch * 2 + pp) * 4096) + lane;
      LAS unsigned char *U = L + B_U + ch * U_CH + (31 + 32 * pp + i32) * 80 + 8 * h;
      LAS unsigned char *XU = L + B_XU + ch * 5120 + (32 * pp + i32) * 80 + 8 * h;
      f32x4 o[4];
      u32x2 hv[4], xe[4];
#pragma unroll
      for (int rg = 0; rg < 4; ++rg) { o[rg] = R[rg * 64]; hv[rg] = *(LAS u32x2 *)(U + 16 * rg); xe[rg] = *(LAS u32x2 *)(XU + 16 * rg); }
      SB();
      unsigned o0[4], o1[4];
      const f32x2 dk2 = {dsk, dsk}, ga2 = {gaw, gaw};
#pragma unroll
      for (int rg = 0; rg < 4; ++rg) {
        const f32x2 ya = lo2(o[rg]) + f32x2{acc0[4 * rg], acc0[4 * rg + 1]}, yb = hi2(o[rg]) + f32x2{acc0[4 * rg + 2], acc0[4 * rg + 3]};
        o0[rg] = pack2(pk_fma(ga2, pk_fma(bf_2(hv[rg].x), dk2, ya), bf_2(xe[rg].x)));
        o1[rg] = pack2(pk_fma(ga2, pk_fma(bf_2(hv[rg].y), dk2, yb), bf_2(xe[rg].y)));
      }
      const bool sw = (d >> 3) & 1;
      u32x4 pc[2];
#pragma unroll
      for (int k = 0; k < 2; ++k) {
        auto s0 = __builtin_amdgcn_permlane32_swap(o0[2 * k], o0[2 * k + 1], false, false);
        auto s1 = __builtin_amdgcn_permlane32_swap(o1[2 * k], o1[2 * k + 1], false, false);
        const unsigned l0 = s0[0], h0 = s0[1], l1 = s1[0], h1 = s1[1];
        pc[k] = sw ? u32x4{h0, h1, l0, l1} : u32x4{l0, l1, h0, h1};
      }
      const bool odd = i32 & 1;
      u32x4 a, bq;
#pragma unroll
      for (int e = 0; e < 4; ++e) {
        const unsigned t = odd ? pc[0][e] : pc[1][e];
        const unsigned u = __builtin_amdgcn_update_dpp(0, (int)t, 0xB1, 0xf, 0xf, true);
        a[e] = odd ? u : pc[0][e];
        bq[e] = odd ? pc[1][e] : u;
      }
      const int vo = (i32 >> 1) * 65536 + (i32 & 1) * 32 + 16 * h;
      st_sc1(rs, vo, (int)O_X1T + 16 * pp * 65536 + d * 128, a);
      st_sc1(rs, vo + 64, (int)O_X1T + 16 * pp * 65536 + d * 128, bq);
    }
    SB();
    WAIT_VM(0);
    BAR();
    if (tid < 8) __hip_atomic_fetch_add(ctl + C_E2 + (tid * 8 + (b & 7)) * 32, 1u, RLX_AGENT);
    SB();
    transit_read(L, lane, w, stash, 1);
    tr.begin(lane, w, rt, (w + 2) & 3);
#pragma unroll
    for (int i = 0; i < 16; ++i) { tr.step(i); __builtin_amdgcn_s_sleep(R2_SLEEP); }
    if (w == 0) wave_poll<true>(ctl + C_E2 + ((b & 7) * 8 + (lane & 7)) * 32, lane < 8, 32u);
    WAIT_VM(0);
    BAR();
  }

  u32x4 xres[2];
  {
    FRESH_IDS();
    const int ph2[2] = {2 * w, 2 * w + 1};
    const int ph1[2] = {w < 3 ? 2 * w : 5, w < 3 ? 2 * w + 1 : 6};
    const int Fs[4] = {56 + 7 * j + ph2[0], 56 + 7 * j + (ph2[1] < 7 ? ph2[1] : 0), 7 * j + ph1[0], 7 * j + (w == 2 ? 0 : ph1[1])};
    int wo[4];
#pragma unroll
    for (int s = 0; s < 4; ++s) wo[s] = (int)O_WP + Fs[s] * 16 * 1024 + lane * 16;
    u32x4 wf[G1_WIN][4];
    unsigned ep[4], gbv[8];
    {
      u32x4 xs[16];
      const int xo = (int)O_X1T + rt * 65536 + tid * 16;
#pragma unroll
      for (int i = 0; i < 16; ++i) xs[i] = ld_sc1(rs, xo, i * 4096);
      SB();
      transit_read(L, lane, w, stash, 2);
      SB();
      const int r0 = tid >> 3, cc = tid & 7;
      LAS unsigned char *X = L + B_X + r0 * 128 + ((cc ^ (4 * ((r0 >> 1) & 1))) * 16);
#pragma unroll
      for (int i = 0; i < 16; ++i) {
        *(LAS u32x4 *)(X + i * 4096) = xs[i];
        wf[i >> 2][i & 3] = ld_sc1(rs, wo[i & 3], (i >> 2) * 1024);
        SB();
      }
      const int tf = tid < 224 ? tid : 0, f = tf < 112 ? 112 * j + tf : FH + 112 * j + (tf - 112);
#pragma unroll
      for (int q = 0; q < 4; ++q) ep[q] = ld_sc1_32(rs, f * 4, (int)O_E12 + q * FF * 4);
#pragma unroll
      for (int q = 0; q < 8; ++q) gbv[q] = ld_sc1_32(rs, (tid & 63) * 4, (int)O_MOD + (q * 3072 + 5 * 512 + 64 * j) * 4);
      SB();
    }
    BAR();
    f32x4 acc[4][4], ssum[2], ssq[2];
    if (w < 2) gemm1_loop<0>(rs, L, lane, wo, wf, acc, ssum, ssq);
    else if (w == 2) gemm1_loop<2>(rs, L, lane, wo, wf, acc, ssum, ssq);
    else gemm1_loop<3>(rs, L, lane, wo, wf, acc, ssum, ssq);

    if (tid < 224) {
      LAS float *E = (LAS float *)(L + B_E) + tid;
      E[0] = u2f(ep[0]) + u2f(ep[2]);
      E[224] = u2f(ep[1]) + u2f(ep[3]);
    }
    if (tid < 64)
      ((LAS float *)(L + B_GB))[tid] = rbf(((u2f(gbv[0]) + u2f(gbv[1])) + (u2f(gbv[2]) + u2f(gbv[3]))) + ((u2f(gbv[4]) + u2f(gbv[5])) + (u2f(gbv[6]) + u2f(gbv[7]))));
    LAS float *SM = (LAS float *)(L + B_ST), *SR = SM + 64;
    asm volatile("" : "+v"(SM));
    SR = SM + 64;
    if (w >= 2 && g == (i16 >> 2)) {
#pragma unroll
      for (int k = 0; k < 2; ++k) {
        const int tau = (w == 2 ? 0 : 2) + k;
        const float mean = pick4(ssum[k], i16 & 3) * (1.f / DM);
        const float var = pick4(ssq[k], i16 & 3) * (1.f / DM) - mean * mean;
        const int s = tile_row(tau, i16);
        const float r = ln_rstd(fmaxf(var, 0.f));
        SM[s] = -rbf(mean) * r;
        SR[s] = r;
      }
    }
    {
      const int q = i16 >> 2, pq = lane & 3;
      LAS unsigned char *xb = L + B_X + (8 * g + q) * 128 + (((4 * (w >> 1) + pq) ^ (4 * (q >> 1))) * 16) + (((w & 1) ^ (g & 1)) * 8) + 2 * j * 4096;
#pragma unroll
      for (int dl = 0; dl < 2; ++dl) xres[dl] = cat2(tr_read(xb + dl * 4096), tr_read(xb + dl * 4096 + 512));
    }
    tr.begin(lane, w, rt, (w + 3) & 3);
    BAR();

    const int nz2 = w == 3 ? 1 : 2, nz1 = w == 2 ? 1 : 2;
    u32x4 zf[2][2];
    f32x4 e1z[2], e2z[2];
    float m1[4], r1[4];
    {
      LAS float *E1 = (LAS float *)(L + B_E);
      asm volatile("" : "+v"(E1));
      f32x4 m4[4], r4[4];
      float e1[2], e2[2];
#pragma unroll
      for (int t = 0; t < 4; ++t) { m4[t] = *(LAS f32x4 *)(SM + 32 * (t >> 1) + 8 * g + 4 * (t & 1)); r4[t] = *(LAS f32x4 *)(SR + 32 * (t >> 1) + 8 * g + 4 * (t & 1)); }
#pragma unroll
      for (int s = 0; s < 2; ++s) {
        const int p2 = s < nz2 ? ph2[s] : 0, p1 = s < nz1 ? ph1[s] : 0;
        e1[s] = E1[112 + 16 * p2 + i16]; e2[s] = E1[224 + 112 + 16 * p2 + i16];
        e1z[s] = *(LAS f32x4 *)(E1 + 16 * p1 + 4 * g); e2z[s] = *(LAS f32x4 *)(E1 + 224 + 16 * p1 + 4 * g);
      }
#pragma unroll
      for (int t = 0; t < 4; ++t) { const int sr = tile_row(t, i16); m1[t] = SM[sr]; r1[t] = SR[sr]; }
      SB();
      LAS unsigned char *ZW = L + B_ZT + i16 * 128 + 16 * g;
#pragma unroll
      for (int s = 0; s < 2; ++s) {
        if (s < nz2) {
          const f32x2 e12 = {e1[s], e1[s]}, e22 = {e2[s], e2[s]};
#pragma unroll
          for (int t = 0; t < 4; ++t) {
            const f32x4 a = acc[s][t];
            zf[s][t >> 1][2 * (t & 1)] = pack2(gelu2(pk_fma(lo2(a), lo2(r4[t]), pk_fma(lo2(m4[t]), e12, e22))));
            zf[s][t >> 1][2 * (t & 1) + 1] = pack2(gelu2(pk_fma(hi2(a), hi2(r4[t]), pk_fma(hi2(m4[t]), e12, e22))));
            if (s == 0) { tr.step(2 * t); tr.step(2 * t + 1); SB(); }
          }
#pragma unroll
          for (int kp = 0; kp < 2; ++kp) *(LAS u32x4 *)(ZW + ph2[s] * 2048 + 64 * kp) = zf[s][kp];
        }
      }
    }
    if (tid < 128) *(LAS u32x4 *)(L + B_ZT + 112 * 128 + 16 * tid) = u32x4{0, 0, 0, 0};
    BAR();
    {
      const int q = i16 >> 2, pq = lane & 3;
      const u32x4 ones = opaque_ones();
      f32x4 su, sq;
      u32x4 za[4];
      LAS unsigned char *A = L + B_ZT + (8 * g + q) * 128 + (16 * w + 4 * pq) * 2;
#pragma unroll
      for (int st = 0; st < 4; ++st) { za[st] = cat2(tr_read(A + st * 4096), tr_read(A + st * 4096 + 512)); tr.step(8 + st); }
      mma16zn(su, za[0], ones);
      mma16z(sq, za[0], za[0]);
#pragma unroll
      for (int st = 1; st < 4; ++st) { mma16(su, za[st], ones); mma16(sq, za[st], za[st]); }
#pragma unroll
      for (int st = 12; st < 16; ++st) tr.step(st);
      MFMA_FENCE();
      if (g == q) {
        const u32x4 gr = {f2u(pick4(su, i16 & 3)), epoch, f2u(pick4(sq, i16 & 3)), epoch};
        st_sc1(rs, (16 * w + i16) * 16, (int)O_GRAN + (rt * 8 + j) * 1024, gr);
      }
    }
    {
      LAS unsigned char *Z1 = L + B_Z1 + lane * 8;
      asm volatile("" : "+v"(Z1));
#pragma unroll
      for (int s = 0; s < 2; ++s) {
        if (s < nz1) {
          u32x2 z[4];
#pragma unroll
          for (int t = 0; t < 4; ++t) {
            const f32x2 m2 = {m1[t], m1[t]}, r2 = {r1[t], r1[t]};
            const f32x4 a = acc[2 + s][t];
            z[t] = u32x2{pack2(gelu2(pk_fma(lo2(a), r2, pk_fma(m2, lo2(e1z[s]), lo2(e2z[s]))))),
                         pack2(gelu2(pk_fma(hi2(a), r2, pk_fma(m2, hi2(e1z[s]), hi2(e2z[s])))))};
          }
#pragma unroll
          for (int t = 0; t < 4; ++t) *(LAS u32x2 *)(Z1 + ph1[s] * 2048 + t * 512) = z[t];
        }
      }
    }
    {
      float su, sq;
      for (unsigned spins = 0; spins < SPIN_CAP; ++spins) {
        u32x4 gr[8];
#pragma unroll
        for (int q = 0; q < 8; ++q) gr[q] = ld_sc1(rs, lane * 16, (int)O_GRAN + (rt * 8 + q) * 1024);
        unsigned bad = 0u;
        su = 0.f; sq = 0.f;
#pragma unroll
        for (int q = 0; q < 8; ++q) { bad |= (gr[q][1] ^ epoch) | (gr[q][3] ^ epoch); su += u2f(gr[q][0]); sq += u2f(gr[q][2]); }
        if (__all(bad == 0u)) break;
        __builtin_amdgcn_s_sleep(POLL_SLEEP);
      }
      const float mean = su * (1.f / FH), var = sq * (1.f / FH) - mean * mean;
      const float r = ln_rstd(fmaxf(var, 0.f));
      LAS float *ZR = (LAS float *)(L + B_ZS + w * 512);
      asm volatile("" : "+v"(ZR));
      ZR[lane] = r;
      ZR[64 + lane] = -rbf(mean) * r;
      f32x4 r4[4], m4[4];
#pragma unroll
      for (int t = 0; t < 4; ++t) { r4[t] = *(LAS f32x4 *)(ZR + 32 * (t >> 1) + 8 * g + 4 * (t & 1)); m4[t] = *(LAS f32x4 *)(ZR + 64 + 32 * (t >> 1) + 8 * g + 4 * (t & 1)); }
      SB();
#pragma unroll
      for (int s = 0; s < 2; ++s) {
        if (s < nz2) {
#pragma unroll
          for (int kp = 0; kp < 2; ++kp) {
            u32x4 o;
#pragma unroll
            for (int hf = 0; hf < 2; ++hf) {
              o[2 * hf] = pack2(pk_fma(bf_2(zf[s][kp][2 * hf]), lo2(r4[2 * kp + hf]), lo2(m4[2 * kp + hf])));
              o[2 * hf + 1] = pack2(pk_fma(bf_2(zf[s][kp][2 * hf + 1]), hi2(r4[2 * kp + hf]), hi2(m4[2 * kp + hf])));
            }
            st_sc1(rs, lane * 16, (int)O_Z2F + ((7 * j + ph2[s]) * 64 + 2 * rt + kp) * 1024, o);
          }
        }
      }
    }
    SB();
    WAIT_VM(0);
    BAR();
    if (tid < 8) st_sc1(rs, tid * 16, (C_F3 + (rt * 8 + j) * 32) * 4, u32x4{epoch, epoch, epoch, epoch});
    SB();
    transit_read(L, lane, w, stash, 3);
    BAR();
  }

  u32x4 wof[4][7];
  {
    FRESH_IDS();
    const float bsp = bf2f(p.b_sp[64 * rt + tile_row(w, i16)]);
    wave_poll<false>(ctl + C_F3 + ((8 * w + (lane & 7)) * 8 + j) * 32, lane < 8, epoch);
    const int ao = (int)O_Z2F + (7 * j * 64 + 16 * w) * 1024, vo = lane * 16;
    u32x4 af[G2_WIN];
#pragma unroll
    for (int n = 0; n < G2_WIN; ++n) af[n] = ld_sc1(rs, vo, ao + ((n >> 4) * 64 + (n & 15)) * 1024);
    SB();
    f32x4 acc[2][4], keep[7];
    LAS f32x4 *R = (LAS f32x4 *)L + lane;
    LAS f32x4 *Rs[3];
#pragma unroll
    for (int s = 1; s < 4; ++s) Rs[s - 1] = R + (((s + w) & 3) * 3 + (s - 1)) * 7 * 64;
#pragma unroll
    for (int ph = 0; ph < 7; ++ph) {
#pragma unroll
      for (int k = 0; k < 16; ++k) {
        const int n = ph * 16 + k;
        const u32x4 cur = af[n % G2_WIN];
#pragma unroll
        for (int s = 0; s < 4; ++s) { if (k == 0) mma16az(acc[ph & 1][s], cur, stash[k][s]); else mma16a(acc[ph & 1][s], cur, stash[k][s]); }
        if (n + G2_WIN < 112) af[n % G2_WIN] = ld_sc1(rs, vo, ao + (((n + G2_WIN) >> 4) * 64 + ((n + G2_WIN) & 15)) * 1024);
        if (ph > 0 && k >= 3 && k < 6) Rs[k - 3][(ph - 1) * 64] = acc[(ph - 1) & 1][k - 2];
        if (ph > 0 && k == 6) keep[ph - 1] = acc[(ph - 1) & 1][0];
        if (n >= 84) {
          const int m = n - 84;
          wof[m / 7][m % 7] = ld_sc1(rs, vo, (int)O_WOF + ((4 * j + m / 7) * 28 + 7 * w + m % 7) * 1024);
        }
        SB();
      }
    }
    MFMA_FENCE();
#pragma unroll
    for (int s = 1; s < 4; ++s) Rs[s - 1][6 * 64] = acc[0][s];
    keep[6] = acc[0][0];
    SB();
    BAR();
    unsigned vv[7][2];
    {
      const f32x2 b2 = {bsp, bsp};
      LAS f32x4 *Rw = R + w * 3 * 7 * 64;
      LAS unsigned char *Z1 = L + B_Z1 + (w * 64 + lane) * 8;
      asm volatile("" : "+v"(Z1));
      f32x4 ra[7][3];
      u32x2 z1[7];
#pragma unroll
      for (int ph = 0; ph < 7; ++ph) {
#pragma unroll
        for (int q = 0; q < 3; ++q) ra[ph][q] = Rw[(q * 7 + ph) * 64];
        z1[ph] = *(LAS u32x2 *)(Z1 + ph * 2048);
      }
      SB();
#pragma unroll
      for (int ph = 0; ph < 7; ++ph) {
        const f32x4 s = (keep[ph] + ra[ph][0]) + (ra[ph][1] + ra[ph][2]);
        vv[ph][0] = pack2(bf_2(z1[ph].x) * (lo2(s) + b2));
        vv[ph][1] = pack2(bf_2(z1[ph].y) * (hi2(s) + b2));
      }
    }
    const int vb = (int)O_VF + (4 * rt + w) * 28 * 1024;
    if ((j & 1) == 0) {
      const int p0 = 7 * j / 2;
#pragma unroll
      for (int k = 0; k < 3; ++k) st_sc1(rs, vo, vb + (p0 + k) * 1024, u32x4{vv[2 * k][0], vv[2 * k][1], vv[2 * k + 1][0], vv[2 * k + 1][1]});
      st_sc1_64(rs, lane * 8, (int)O_VH + ((4 * rt + w) * 8 + j) * 512, u32x2{vv[6][0], vv[6][1]});
    } else {
      const int p0 = (7 * j - 1) / 2;
      st_sc1_64(rs, lane * 8, (int)O_VH + ((4 * rt + w) * 8 + j) * 512, u32x2{vv[0][0], vv[0][1]});
#pragma unroll
      for (int k = 0; k < 3; ++k)
        st_sc1(rs, vo, vb + (p0 + 1 + k) * 1024, u32x4{vv[2 * k + 1][0], vv[2 * k + 1][1], vv[2 * k + 2][0], vv[2 * k + 2][1]});
    }
    SB();
    WAIT_VM(0);
    BAR();
    if (tid < 8) st_sc1(rs, tid * 16, (C_F4 + (rt * 8 + j) * 32) * 4, u32x4{epoch, epoch, epoch, epoch});
  }

  {
    FRESH_IDS();
    wave_poll<false>(ctl + C_F4 + (rt * 8 + 2 * w + (lane & 1)) * 32, lane < 2, epoch);
    u32x4 vf[7][4];
#pragma unroll
    for (int k = 0; k < 7; ++k)
#pragma unroll
      for (int s = 0; s < 4; ++s) {
        if (k == 3) {
          const int ho = (int)O_VH + ((4 * rt + ((s + w) & 3)) * 8 + 2 * w) * 512;
          vf[k][s] = cat2(ld_sc1_64(rs, lane * 8, ho), ld_sc1_64(rs, lane * 8, ho + 512));
        } else {
          vf[k][s] = ld_sc1(rs, lane * 16, (int)O_VF + ((4 * rt + ((s + w) & 3)) * 28 + 7 * w + k) * 1024);
        }
      }
    SB();
    f32x4 acc[4][4];
#pragma unroll
    for (int k = 0; k < 7; ++k)
#pragma unroll
      for (int s = 0; s < 4; ++s) {
#pragma unroll
        for (int dd = 0; dd < 4; ++dd) { if (k == 0) mma16az(acc[dd][s], wof[dd][k], vf[k][s]); else mma16a(acc[dd][s], wof[dd][k], vf[k][s]); }
        SB();
      }
    MFMA_FENCE();
    LAS f32x4 *R = (LAS f32x4 *)L + lane;
#pragma unroll
    for (int s = 1; s < 4; ++s) {
      LAS f32x4 *Rs = R + (((s + w) & 3) * 3 + (s - 1)) * 4 * 64;
#pragma unroll
      for (int dd = 0; dd < 4; ++dd) Rs[dd * 64] = acc[dd][s];
    }
    BAR();
    const int t = 64 * rt + tile_row(w, i16);
    {
      LAS f32x4 *Rw = R + w * 3 * 4 * 64;
      LAS unsigned char *GB = L + B_GB + 32 * g;
      asm volatile("" : "+v"(GB));
      f32x4 ra[4][3], gb[4];
#pragma unroll
      for (int dd = 0; dd < 4; ++dd) {
#pragma unroll
        for (int q = 0; q < 3; ++q) ra[dd][q] = Rw[(q * 4 + dd) * 64];
        gb[dd] = *(LAS f32x4 *)(GB + 128 * (dd >> 1) + 16 * (dd & 1));
      }
      SB();
#pragma unroll
      for (int dl = 0; dl < 2; ++dl) {
        u32x4 o;
#pragma unroll
        for (int hf = 0; hf < 2; ++hf) {
          const int dd = 2 * dl + hf;
          const f32x4 s = (acc[dd][0] + ra[dd][0]) + (ra[dd][1] + ra[dd][2]);
          o[2 * hf] = pack2(pk_fma(lo2(gb[dd]), lo2(s), bf_2(xres[dl][2 * hf])));
          o[2 * hf + 1] = pack2(pk_fma(hi2(gb[dd]), hi2(s), bf_2(xres[dl][2 * hf + 1])));
        }
        *(GAS u32x4 *)(p.out + (size_t)t * DM + 64 * j + 32 * dl + 8 * g) = o;
      }
    }
    if (b == 0 && tid == 0) __hip_atomic_store(ctl + C_EPOCH, epoch, RLX_AGENT);
  }
}

extern "C" void kernel_launch(void *const *d_in, const int *in_sizes, int n_in, void *d_out, int out_size, void *d_ws,
                              size_t ws_size, hipStream_t stream) {
  Params p;
  p.x = (const u16 *)d_in[0];
  p.a_re = (const u16 *)d_in[1]; p.a_im = (const u16 *)d_in[2];
  p.b_re = (const u16 *)d_in[3]; p.b_im = (const u16 *)d_in[4];
  p.c_re = (const u16 *)d_in[5]; p.c_im = (const u16 *)d_in[6];
  p.log_dt = (const u16 *)d_in[7]; p.dskip = (const u16 *)d_in[8];
  p.w_in = (const u16 *)d_in[10]; p.w_sp = (const u16 *)d_in[11]; p.b_sp = (const u16 *)d_in[12];
  p.w_out = (const u16 *)d_in[13]; p.cvec = (const u16 *)d_in[14]; p.w_mod = (const u16 *)d_in[15];
  p.out = (u16 *)d_out;
  p.ws = (unsigned char *)d_ws;
  (void)hipFuncSetAttribute((const void *)kb_main, hipFuncAttributeMaxDynamicSharedMemorySize, B_LDS);
  kb_main<<<256, 256, B_LDS, stream>>>(p);
}
```
